```python
import torch

def _softmax(x, axis=-1):
    return torch.softmax(x, dim=axis)
def _gelu(x):
    return torch.nn.functional.gelu(x, approximate="tanh")
def _silu(x):
    return torch.nn.functional.silu(x)
def _sigmoid(x):
    return torch.sigmoid(x)
def _layernorm(x, axis=-1, eps=1e-5):
    m = x.mean(dim=axis, keepdim=True)
    v = x.var(dim=axis, keepdim=True, unbiased=False)
    return (x - m) / torch.sqrt(v + eps)
def _scatter_add(tgt, idx, src, axis=0):
    return tgt.clone().index_add_(axis, idx, src)
def _segment_sum(src, seg, n):
    out = torch.zeros((n,) + src.shape[1:], dtype=src.dtype)
    return out.index_add_(0, seg, src)
def _pad(x, pw):
    flat = tuple(v for pair in reversed(pw) for v in pair)
    return torch.nn.functional.pad(x, flat)

kernel_name = 'interleave_layers__sliding_block_cached__sliding_block_cached__decode'

def setup_inputs():

    _w = (lambda a: (lambda t: t.to(torch.bfloat16) if t.is_floating_point() else t)(torch.as_tensor(a)))
    def _A_setup():
        B, Sq, D = 4, 1, 512
        Hq, Hkv, Sk, Dh = 32, 8, 4096, 128
        assert Hq % Hkv == 0, (Hq, Hkv)
        return {
            "x": _w((lambda *s: torch.randn(*s, dtype=torch.bfloat16) * 0.05)(B, Sq, D)),
            "k_cache": _w((lambda *s: torch.randn(*s, dtype=torch.bfloat16) * 0.8)(B, Hkv, Sk, Dh)),
            "v_cache": _w((lambda *s: torch.randn(*s, dtype=torch.bfloat16) * 3.2)(B, Hkv, Sk, Dh)),
            "wq": _w((lambda *s: torch.randn(*s, dtype=torch.bfloat16) * 0.8)(D, Hq * Dh) * (D ** -0.5)),
            "wo": _w((lambda *s: torch.randn(*s, dtype=torch.bfloat16) * 3.2)(Hq * Dh, D) * ((Hq * Dh) ** -0.5)),
        }
    _A_in = _A_setup()

    def _B_setup():
        B, Sq, D = 4, 1, 512
        Hq, Hkv, Sk, Dh = 32, 8, 4096, 128
        assert Hq % Hkv == 0, (Hq, Hkv)
        return {
            "x": _w((lambda *s: torch.randn(*s, dtype=torch.bfloat16) * 0.05)(B, Sq, D)),
            "k_cache": _w((lambda *s: torch.randn(*s, dtype=torch.bfloat16) * 1.6)(B, Hkv, Sk, Dh)),
            "v_cache": _w((lambda *s: torch.randn(*s, dtype=torch.bfloat16) * 1.6)(B, Hkv, Sk, Dh)),
            "wq": _w((lambda *s: torch.randn(*s, dtype=torch.bfloat16) * 1.6)(D, Hq * Dh) * (D ** -0.5)),
            "wo": _w((lambda *s: torch.randn(*s, dtype=torch.bfloat16) * 1.6)(Hq * Dh, D) * ((Hq * Dh) ** -0.5)),
        }
    _B_in = _B_setup()

    return {
        "A_x": _A_in["x"],
        "A_k_cache": _A_in["k_cache"],
        "A_v_cache": _A_in["v_cache"],
        "A_wq": _A_in["wq"],
        "A_wo": _A_in["wo"],
        "B_x": _B_in["x"],
        "B_k_cache": _B_in["k_cache"],
        "B_v_cache": _B_in["v_cache"],
        "B_wq": _B_in["wq"],
        "B_wo": _B_in["wo"],
    }

def reference(A_x, A_k_cache, A_v_cache, A_wq, A_wo, B_x, B_k_cache, B_v_cache, B_wq, B_wo):
    def _A_ref(x, k_cache, v_cache, wq, wo):
        B, Sq, D = x.shape
        Hq, Dh = 32, 128
        Hkv, Sk = k_cache.shape[1], k_cache.shape[2]
        W = min(4096, max(256, Sk // 4))
        rep = Hq // Hkv
        q = (lambda x, *d: x.permute(*d))((x @ wq).reshape(B, Sq, Hq, Dh), 0, 2, 1, 3)
        k = (lambda x, n, axis: torch.repeat_interleave(x, n, dim=axis))(k_cache[:, :, Sk - W:, :], rep, axis=1)
        v = (lambda x, n, axis: torch.repeat_interleave(x, n, dim=axis))(v_cache[:, :, Sk - W:, :], rep, axis=1)
        scores = torch.einsum("bhqd,bhkd->bhqk", q, k) * (Dh ** -0.5)
        p = _softmax(scores, axis=-1)
        o = torch.einsum("bhqk,bhkd->bhqd", p, v)
        return (lambda x, *d: x.permute(*d))(o, 0, 2, 1, 3).reshape(B, Sq, Hq * Dh) @ wo

    def _B_ref(x, k_cache, v_cache, wq, wo):
        B, Sq, D = x.shape
        Hq, Dh = 32, 128
        Hkv, Sk = k_cache.shape[1], k_cache.shape[2]
        W = min(4096, max(256, Sk // 4))
        rep = Hq // Hkv
        q = (lambda x, *d: x.permute(*d))((x @ wq).reshape(B, Sq, Hq, Dh), 0, 2, 1, 3)
        k = (lambda x, n, axis: torch.repeat_interleave(x, n, dim=axis))(k_cache[:, :, Sk - W:, :], rep, axis=1)
        v = (lambda x, n, axis: torch.repeat_interleave(x, n, dim=axis))(v_cache[:, :, Sk - W:, :], rep, axis=1)
        scores = torch.einsum("bhqd,bhkd->bhqk", q, k) * (Dh ** -0.5)
        p = _softmax(scores, axis=-1)
        o = torch.einsum("bhqk,bhkd->bhqd", p, v)
        return (lambda x, *d: x.permute(*d))(o, 0, 2, 1, 3).reshape(B, Sq, Hq * Dh) @ wo

    x = A_x
    x = x + _A_ref(_layernorm(x, axis=-1), A_k_cache, A_v_cache, A_wq, A_wo)
    x = x + _B_ref(_layernorm(x, axis=-1), B_k_cache, B_v_cache, B_wq, B_wo)
    x = x + _A_ref(_layernorm(x, axis=-1), A_k_cache, A_v_cache, A_wq, A_wo)
    x = x + _B_ref(_layernorm(x, axis=-1), B_k_cache, B_v_cache, B_wq, B_wo)
    return x
```

```cpp
#include <hip/hip_runtime.h>
#include <hip/hip_bf16.h>
#include <stdint.h>

typedef unsigned short u16;
typedef unsigned v4u __attribute__((ext_vector_type(4)));
__device__ __forceinline__ float bf2f(u16 b) { return __uint_as_float(((unsigned)b) << 16); }
__device__ __forceinline__ float bflo(unsigned w) { return __uint_as_float(w << 16); }
__device__ __forceinline__ float bfhi(unsigned w) { return __uint_as_float(w & 0xffff0000u); }
__device__ __forceinline__ u16 f2bf(float f) { __hip_bfloat16 h = __float2bfloat16(f); return *(u16*)&h; }
__device__ __forceinline__ float rbf(float f) { return bf2f(f2bf(f)); }

constexpr int B = 4, D = 512, HQ = 32, HKV = 8, SK = 4096, DH = 128, W = 1024, NS = 8;

__global__ void __launch_bounds__(256) k_q(const u16* x, const u16* wq, float* q) {
  __shared__ float xn[B][D];
  __shared__ float red[32][B][64];
  int tid = threadIdx.x;
  int wave = tid >> 6, lane = tid & 63;
  {
    int b = wave;
    float v[8]; float s = 0;
    for (int i = 0; i < 8; i++) { v[i] = bf2f(x[b * D + lane * 8 + i]); s += v[i]; }
    for (int m = 32; m >= 1; m >>= 1) s += __shfl_xor(s, m);
    float mean = rbf(s / D);
    float mean_f = s / D;
    float ss = 0;
    for (int i = 0; i < 8; i++) { float d = v[i] - mean_f; ss += d * d; }
    for (int m = 32; m >= 1; m >>= 1) ss += __shfl_xor(ss, m);
    float var = rbf(ss / D);
    float den = rbf(sqrtf(rbf(var + 1e-5f)));
    for (int i = 0; i < 8; i++) xn[b][lane * 8 + i] = rbf(rbf(v[i] - mean) / den);
  }
  __syncthreads();
  int cg = tid & 7, kg = tid >> 3;
  int col0 = blockIdx.x * 64 + cg * 8;
  float acc[B][8];
  for (int b = 0; b < B; b++) for (int i = 0; i < 8; i++) acc[b][i] = 0;
  for (int r = 0; r < 16; r++) {
    int k = kg * 16 + r;
    v4u w = *(const v4u*)(wq + (size_t)k * (HQ * DH) + col0);
    float wf[8];
    for (int i = 0; i < 4; i++) { wf[2 * i] = bflo(w[i]); wf[2 * i + 1] = bfhi(w[i]); }
    for (int b = 0; b < B; b++) { float xv = xn[b][k]; for (int i = 0; i < 8; i++) acc[b][i] += xv * wf[i]; }
  }
  for (int b = 0; b < B; b++) for (int i = 0; i < 8; i++) red[kg][b][cg * 8 + i] = acc[b][i];
  __syncthreads();
  { int b = tid >> 6, c = tid & 63; float s = 0; for (int g = 0; g < 32; g++) s += red[g][b][c];
    q[b * (HQ * DH) + blockIdx.x * 64 + c] = rbf(s); }
}

__global__ void __launch_bounds__(256) k_attn(const float* q, const u16* kc, const u16* vc, float* po, float* pml) {
  __shared__ float qs[4][DH];
  __shared__ float sc[4][128];
  __shared__ float red[4][4][DH];
  int s = blockIdx.x, g = blockIdx.y, b = blockIdx.z;
  int tid = threadIdx.x, wave = tid >> 6, lane = tid & 63;
  for (int i = tid; i < 512; i += 256) qs[i >> 7][i & 127] = q[b * 4096 + g * 512 + i];
  __syncthreads();
  const u16* kb = kc + ((size_t)(b * HKV + g) * SK + (SK - W) + s * 128) * DH;
  const u16* vb = vc + ((size_t)(b * HKV + g) * SK + (SK - W) + s * 128) * DH;
  int r = tid >> 4, c = tid & 15;
  for (int it = 0; it < 8; it++) {
    int key = it * 16 + r;
    v4u kv = *(const v4u*)(kb + key * DH + c * 8);
    float kf[8];
    for (int i = 0; i < 4; i++) { kf[2 * i] = bflo(kv[i]); kf[2 * i + 1] = bfhi(kv[i]); }
    for (int h = 0; h < 4; h++) {
      float d = 0;
      for (int i = 0; i < 8; i++) d += kf[i] * qs[h][c * 8 + i];
      for (int m = 8; m >= 1; m >>= 1) d += __shfl_xor(d, m);
      if (c == 0) sc[h][key] = rbf(rbf(d) * 0.08838834764831845f);
    }
  }
  __syncthreads();
  {
    int h = wave;
    float a = sc[h][lane], c2 = sc[h][lane + 64];
    float m = fmaxf(a, c2);
    for (int o = 32; o >= 1; o >>= 1) m = fmaxf(m, __shfl_xor(m, o));
    float e0 = __expf(a - m), e1 = __expf(c2 - m);
    float l = e0 + e1;
    for (int o = 32; o >= 1; o >>= 1) l += __shfl_xor(l, o);
    sc[h][lane] = e0; sc[h][lane + 64] = e1;
    if (lane == 0) { int slot = ((b * HQ + g * 4 + h) * NS + s); pml[slot * 2] = m; pml[slot * 2 + 1] = l; }
  }
  __syncthreads();
  float acc[4][2] = {};
  for (int kk = 0; kk < 32; kk++) {
    int key = wave * 32 + kk;
    unsigned vv = *(const unsigned*)(vb + key * DH + lane * 2);
    float v0 = bflo(vv), v1 = bfhi(vv);
    for (int h = 0; h < 4; h++) { float p = sc[h][key]; acc[h][0] += p * v0; acc[h][1] += p * v1; }
  }
  for (int h = 0; h < 4; h++) { red[wave][h][lane * 2] = acc[h][0]; red[wave][h][lane * 2 + 1] = acc[h][1]; }
  __syncthreads();
  for (int i = tid; i < 512; i += 256) {
    int h = i >> 7, d = i & 127;
    float t = red[0][h][d] + red[1][h][d] + red[2][h][d] + red[3][h][d];
    po[(size_t)((b * HQ + g * 4 + h) * NS + s) * DH + d] = t;
  }
}

__global__ void __launch_bounds__(256) k_o(const float* po, const float* pml, const u16* wo, float* part) {
  __shared__ float os[B][DH];
  int h = blockIdx.x, tid = threadIdx.x;
  for (int i = tid; i < 512; i += 256) {
    int b = i >> 7, d = i & 127;
    int base = (b * HQ + h) * NS;
    float M = -INFINITY;
    for (int s = 0; s < NS; s++) M = fmaxf(M, pml[(base + s) * 2]);
    float L = 0, o = 0;
    for (int s = 0; s < NS; s++) { float w = __expf(pml[(base + s) * 2] - M); L += w * pml[(base + s) * 2 + 1]; o += w * po[(size_t)(base + s) * DH + d]; }
    os[b][d] = rbf(o / L);
  }
  __syncthreads();
  float acc[B][2] = {};
  for (int k = 0; k < DH; k++) {
    unsigned w = *(const unsigned*)(wo + (size_t)(h * DH + k) * D + tid * 2);
    float w0 = bflo(w), w1 = bfhi(w);
    for (int b = 0; b < B; b++) { acc[b][0] += os[b][k] * w0; acc[b][1] += os[b][k] * w1; }
  }
  for (int b = 0; b < B; b++) { part[(h * B + b) * D + tid * 2] = acc[b][0]; part[(h * B + b) * D + tid * 2 + 1] = acc[b][1]; }
}

__global__ void __launch_bounds__(256) k_res(const float* part, const u16* xin, u16* xout) {
  int i = blockIdx.x * 256 + threadIdx.x;
  float s = 0;
  for (int h = 0; h < HQ; h++) s += part[h * 2048 + i];
  xout[i] = f2bf(bf2f(xin[i]) + rbf(s));
}

extern "C" void kernel_launch(void* const* d_in, const int* in_sizes, int n_in,
                              void* d_out, int out_size, void* d_ws, size_t ws_size,
                              hipStream_t stream) {
  char* ws = (char*)d_ws;
  float* q = (float*)ws;
  float* po = (float*)(ws + (1 << 16));
  float* pml = (float*)(ws + (1 << 20));
  float* part = (float*)(ws + (2 << 20));
  u16* xb0 = (u16*)(ws + (3 << 20));
  u16* xb1 = (u16*)(ws + (3 << 20) + 65536);
  const u16* x = (const u16*)d_in[0];
  for (int l = 0; l < 4; l++) {
    int o = (l & 1) ? 5 : 0;
    const u16* kc = (const u16*)d_in[o + 1]; const u16* vc = (const u16*)d_in[o + 2];
    const u16* wq = (const u16*)d_in[o + 3]; const u16* wo = (const u16*)d_in[o + 4];
    u16* xo = (l == 3) ? (u16*)d_out : ((l & 1) ? xb1 : xb0);
    k_q<<<64, 256, 0, stream>>>(x, wq, q);
    k_attn<<<dim3(NS, HKV, B), 256, 0, stream>>>(q, kc, vc, po, pml);
    k_o<<<32, 256, 0, stream>>>(po, pml, wo, part);
    k_res<<<8, 256, 0, stream>>>(part, x, xo);
    x = xo;
  }
}
```

```cpp
#include <hip/hip_runtime.h>
#include <stdint.h>

typedef unsigned int u32;
typedef unsigned long long u64;
typedef u32 v4u __attribute__((ext_vector_type(4)));
typedef u32 v2u __attribute__((ext_vector_type(2)));
typedef float v4f __attribute__((ext_vector_type(4)));
typedef float v2f __attribute__((ext_vector_type(2)));
typedef __bf16 v8b __attribute__((ext_vector_type(8)));
typedef __bf16 v2b __attribute__((ext_vector_type(2)));
typedef short v4s __attribute__((ext_vector_type(4)));
#define LAS __attribute__((address_space(3)))
#define GAS __attribute__((address_space(1)))

#define DLY_C 6
#define DLY_A 8
#define DLY_B 10
#define ROT 1

namespace {
constexpr int SK = 4096, WIN = 1024, DH = 128;
constexpr u32 WS_EPOCH = 0;
constexpr u32 WS_RDY = 4096;
constexpr u32 WS_LSR = 8192;
constexpr u32 WS_BOX = WS_LSR + 131072;
constexpr u32 BOX = 5120, BOXL = 1280, BOX_Q = 1024, BOX_X = 1152;
constexpr u32 WS_END = WS_BOX + 256 * BOX;
constexpr u32 WS_DBG = 4u << 20;
constexpr u32 L_KV = 0;
constexpr u32 L_OM = 131072;
constexpr u32 L_ML = L_OM + 8192;
constexpr u32 L_Q = L_ML + 128;
constexpr u32 L_RED = L_Q + 1152;
constexpr u32 L_REDC = L_RED + 4096;
constexpr u32 L_CS = L_REDC + 4096;
constexpr u32 L_ST = L_CS + 512;
constexpr u32 L_RED3 = L_ST + 64;
constexpr u32 L_END = L_RED3 + 2048;
static_assert(L_END <= 160 * 1024, "lds");
static_assert(L_Q % 16 == 0 && L_RED % 16 == 0 && L_ST % 16 == 0 && L_RED3 % 16 == 0, "align");
constexpr u32 SENT = 0xFFFFFFFFu;
constexpr u32 SPIN_CAP = 1u << 15;
constexpr float C2 = 0.08838834764831845f * 1.4426950408889634f;

__device__ __forceinline__ float bflo(u32 w) { return __uint_as_float(w << 16); }
__device__ __forceinline__ float bfhi(u32 w) { return __uint_as_float(w & 0xffff0000u); }
__device__ __forceinline__ u32 pk(float lo, float hi) { v2f f = {lo, hi}; return __builtin_bit_cast(u32, __builtin_convertvector(f, v2b)); }
__device__ __forceinline__ u32 safe(u32 v) { return v < 0xFFFFFFFEu ? v : 0xFFFFFFFEu; }
template <int CTRL> __device__ __forceinline__ float dpp(float x) {
  return __builtin_bit_cast(float, __builtin_amdgcn_mov_dpp(__builtin_bit_cast(int, x), CTRL, 0xf, 0xf, true));
}
constexpr int XOR1 = 0xB1, XOR2 = 0x4E, HMIR = 0x141, ROR8 = 0x128, ROR4 = 0x124;
__device__ __forceinline__ float x16_add(float x) { auto s = __builtin_amdgcn_permlane16_swap(__float_as_uint(x), __float_as_uint(x), false, false); return __uint_as_float(s[0]) + __uint_as_float(s[1]); }
__device__ __forceinline__ float x32_add(float x) { auto s = __builtin_amdgcn_permlane32_swap(__float_as_uint(x), __float_as_uint(x), false, false); return __uint_as_float(s[0]) + __uint_as_float(s[1]); }
__device__ __forceinline__ float x16_max(float x) { auto s = __builtin_amdgcn_permlane16_swap(__float_as_uint(x), __float_as_uint(x), false, false); return fmaxf(__uint_as_float(s[0]), __uint_as_float(s[1])); }
__device__ __forceinline__ float x32_max(float x) { auto s = __builtin_amdgcn_permlane32_swap(__float_as_uint(x), __float_as_uint(x), false, false); return fmaxf(__uint_as_float(s[0]), __uint_as_float(s[1])); }
__device__ __forceinline__ v4f mfma(v4u a, v4u b, v4f c) {
  return __builtin_amdgcn_mfma_f32_16x16x32_bf16(__builtin_bit_cast(v8b, a), __builtin_bit_cast(v8b, b), c, 0, 0, 0);
}
#define BAR() asm volatile("s_waitcnt lgkmcnt(0)\n\ts_barrier" ::: "memory")
#define VMWAIT(n) asm volatile("s_waitcnt vmcnt(" #n ")" ::: "memory")

constexpr int R_WQA = 128, R_WOA = 160, R_WQB = 192, R_WOB = 224;
template <int BASE, u32 STRIDE>
__device__ __forceinline__ void bulk8(const char* base, u32 voff, int rb) {
  asm volatile("" : "+s"(base), "+s"(rb));
  const __amdgpu_buffer_rsrc_t r = __builtin_amdgcn_make_buffer_rsrc((void*)base, 0, 0x7fffffff, 0x00020000);
  const u32 o0 = ((0 + 2 * rb) & 7) * STRIDE, o1 = ((1 + 2 * rb) & 7) * STRIDE, o2 = ((2 + 2 * rb) & 7) * STRIDE, o3 = ((3 + 2 * rb) & 7) * STRIDE;
  const u32 o4 = ((4 + 2 * rb) & 7) * STRIDE, o5 = ((5 + 2 * rb) & 7) * STRIDE, o6 = ((6 + 2 * rb) & 7) * STRIDE, o7 = ((7 + 2 * rb) & 7) * STRIDE;
  asm volatile("s_nop 4\n\t"
               "buffer_load_dwordx4 v[%c10:%c11], %0, %1, %2 offen\n\tbuffer_load_dwordx4 v[%c12:%c13], %0, %1, %3 offen\n\t"
               "buffer_load_dwordx4 v[%c14:%c15], %0, %1, %4 offen\n\tbuffer_load_dwordx4 v[%c16:%c17], %0, %1, %5 offen\n\t"
               "buffer_load_dwordx4 v[%c18:%c19], %0, %1, %6 offen\n\tbuffer_load_dwordx4 v[%c20:%c21], %0, %1, %7 offen\n\t"
               "buffer_load_dwordx4 v[%c22:%c23], %0, %1, %8 offen\n\tbuffer_load_dwordx4 v[%c24:%c25], %0, %1, %9 offen"
               :: "v"(voff), "s"(r), "s"(o0), "s"(o1), "s"(o2), "s"(o3), "s"(o4), "s"(o5), "s"(o6), "s"(o7),
                  "i"(BASE), "i"(BASE + 3), "i"(BASE + 4), "i"(BASE + 7), "i"(BASE + 8), "i"(BASE + 11), "i"(BASE + 12), "i"(BASE + 15),
                  "i"(BASE + 16), "i"(BASE + 19), "i"(BASE + 20), "i"(BASE + 23), "i"(BASE + 24), "i"(BASE + 27), "i"(BASE + 28), "i"(BASE + 31)
               : "memory");
}
template <int B, int M, int R>
__device__ __forceinline__ void tp_pair(u32 slo, u32 shi) {
  u32 t0, t1;
  if constexpr (M == R) {
    asm volatile("v_perm_b32 %0, v%c4, v%c3, %1\n\tv_perm_b32 v%c4, v%c4, v%c3, %2\n\tv_mov_b32 v%c3, %0"
                 : "=&v"(t0) : "s"(slo), "s"(shi), "i"(B + 8 * M + R), "i"(B + 8 * M + 4 + R));
  } else {
    asm volatile("v_perm_b32 %0, v%c5, v%c4, %2\n\tv_perm_b32 %1, v%c5, v%c4, %3\n\t"
                 "v_perm_b32 v%c4, v%c7, v%c6, %2\n\tv_perm_b32 v%c5, v%c7, v%c6, %3\n\t"
                 "v_mov_b32 v%c6, %0\n\tv_mov_b32 v%c7, %1"
                 : "=&v"(t0), "=&v"(t1) : "s"(slo), "s"(shi), "i"(B + 8 * M + R), "i"(B + 8 * M + 4 + R), "i"(B + 8 * R + M), "i"(B + 8 * R + 4 + M));
  }
}
template <int B>
__device__ __forceinline__ void transpose8() {
  const u32 slo = 0x05040100u, shi = 0x07060302u;
  tp_pair<B, 0, 0>(slo, shi); tp_pair<B, 1, 1>(slo, shi); tp_pair<B, 2, 2>(slo, shi); tp_pair<B, 3, 3>(slo, shi);
  tp_pair<B, 0, 1>(slo, shi); tp_pair<B, 0, 2>(slo, shi); tp_pair<B, 0, 3>(slo, shi);
  tp_pair<B, 1, 2>(slo, shi); tp_pair<B, 1, 3>(slo, shi); tp_pair<B, 2, 3>(slo, shi);
  asm volatile("s_nop 1");
}
template <int B>
__device__ __forceinline__ void mfma8(v4f (&d)[8], v4u a) {
  asm volatile("s_nop 1\n\t"
               "v_mfma_f32_16x16x32_bf16 %0, %8, v[%c9:%c10], 0\n\tv_mfma_f32_16x16x32_bf16 %1, %8, v[%c11:%c12], 0\n\t"
               "v_mfma_f32_16x16x32_bf16 %2, %8, v[%c13:%c14], 0\n\tv_mfma_f32_16x16x32_bf16 %3, %8, v[%c15:%c16], 0\n\t"
               "v_mfma_f32_16x16x32_bf16 %4, %8, v[%c17:%c18], 0\n\tv_mfma_f32_16x16x32_bf16 %5, %8, v[%c19:%c20], 0\n\t"
               "v_mfma_f32_16x16x32_bf16 %6, %8, v[%c21:%c22], 0\n\tv_mfma_f32_16x16x32_bf16 %7, %8, v[%c23:%c24], 0\n\t"
               "s_nop 7"
               : "=&v"(d[0]), "=&v"(d[1]), "=&v"(d[2]), "=&v"(d[3]), "=&v"(d[4]), "=&v"(d[5]), "=&v"(d[6]), "=&v"(d[7])
               : "v"(a), "i"(B), "i"(B + 3), "i"(B + 4), "i"(B + 7), "i"(B + 8), "i"(B + 11), "i"(B + 12), "i"(B + 15),
                 "i"(B + 16), "i"(B + 19), "i"(B + 20), "i"(B + 23), "i"(B + 24), "i"(B + 27), "i"(B + 28), "i"(B + 31));
}

__device__ __forceinline__ void dma16(u32 voff, const char* sbase, u32 lds_dst) {
  u32 keep;
  asm volatile("s_mov_b32 %0, m0\n\ts_mov_b32 m0, %3\n\ts_nop 0\n\tglobal_load_lds_dwordx4 %1, %2 nt\n\ts_mov_b32 m0, %0"
               : "=&s"(keep) : "v"(voff), "s"(sbase), "s"(lds_dst) : "memory");
}
__device__ __forceinline__ v4u poll128(const char* base, u32 voff) {
  v4u x;
  for (u32 spins = 0;; ++spins) {
    asm volatile("global_load_dwordx4 %0, %1, %2 sc1\n\ts_waitcnt vmcnt(0)" : "=&v"(x) : "v"(voff), "s"(base) : "memory");
    const bool ok = (x[0] != SENT) & (x[1] != SENT) & (x[2] != SENT) & (x[3] != SENT);
    if (__all(ok) || spins > SPIN_CAP) break;
  }
  return x;
}
__device__ __forceinline__ void poll128x2(const char* base, u32 voff, u32 voff2, v4u& x, v4u& y) {
  for (u32 spins = 0;; ++spins) {
    asm volatile("global_load_dwordx4 %0, %2, %4 sc1\n\tglobal_load_dwordx4 %1, %3, %4 sc1\n\ts_waitcnt vmcnt(0)" : "=&v"(x), "=&v"(y) : "v"(voff), "v"(voff2), "s"(base) : "memory");
    const bool ok = (x[0] != SENT) & (x[1] != SENT) & (x[2] != SENT) & (x[3] != SENT) & (y[0] != SENT) & (y[1] != SENT) & (y[2] != SENT) & (y[3] != SENT);
    if (__all(ok) || spins > SPIN_CAP) break;
  }
}
#define STC(i) do {} while (0)
#define ST(i) do {} while (0)
}

struct Ctx {
  const char *kc, *vc, *wq, *wo;
};

template <int TYPE>
__device__ __forceinline__ void layer(const int rep, char* __restrict__ ws, char* lds, const __amdgpu_buffer_rsrc_t rs,
                                      const int b, const int g, const int s, const int w, const int lane0, const int tid,
                                      float (&xo)[8],
                                      const char* nkc, const char* nvc, const char* nwq, const char* cwo,
                                      const u32 tag, int& stampv) {
  constexpr int RQ = TYPE ? R_WQB : R_WQA, RO = TYPE ? R_WOB : R_WOA;
  const int L = rep * 2 + TYPE;
  const u32 cu = (u32)((b * 8 + g) * 8 + s);
  const int rb = ROT ? b : 0;
#define OPAQUE_LANE() int lane = lane0; asm volatile("" : "+v"(lane)); const int c8 = lane & 7, p = (lane >> 3) & 1, kq = lane >> 4; (void)c8; (void)p; (void)kq
  {
    OPAQUE_LANE();
    if (L > 0) {
      if (DLY_B) __builtin_amdgcn_s_sleep(DLY_B);
      const u32 voff = WS_BOX + (u32)((b * 8 + c8) * 8 + w) * BOX + (u32)(L - 1) * BOXL + BOX_X + kq * 32 + p * 16;
      v4u x = poll128(ws, voff);
      ST(L * 8 + 0);
      float v[8];
#pragma unroll
      for (int m = 0; m < 4; ++m) { v[2 * m] = bflo(x[m]); v[2 * m + 1] = bfhi(x[m]); }
#pragma unroll
      for (int j = 0; j < 8; ++j) v[j] += dpp<XOR1>(v[j]);
#pragma unroll
      for (int j = 0; j < 8; ++j) v[j] += dpp<XOR2>(v[j]);
#pragma unroll
      for (int j = 0; j < 8; ++j) v[j] += dpp<HMIR>(v[j]);
#pragma unroll
      for (int j = 0; j < 8; ++j) xo[j] += v[j];
    } else {
      ST(0);
    }
    v4u a;
#pragma unroll
    for (int m = 0; m < 4; ++m) a[m] = pk(xo[2 * m], xo[2 * m + 1]);
#pragma unroll
    for (int m = 0; m < 4; ++m) { xo[2 * m] = bflo(a[m]); xo[2 * m + 1] = bfhi(a[m]); }
    v4u af = a;
    if (c8 == 1) af = v4u{0x3F803F80u, 0x3F803F80u, 0x3F803F80u, 0x3F803F80u};
    if (rep == 0) {
      if (TYPE == 0) VMWAIT(8); else VMWAIT(0);
      ST(44 + TYPE);
      transpose8<RQ>();
    }
    v4f d[8];
    mfma8<RQ>(d, af);
    float s1 = 0.f, s2 = 0.f;
#pragma unroll
    for (int j = 0; j < 8; ++j) { s1 += xo[j]; s2 = fmaf(xo[j], xo[j], s2); }
    s1 += dpp<ROR8>(s1); s2 += dpp<ROR8>(s2);
    s1 = x16_add(s1); s2 = x16_add(s2);
    s1 = x32_add(s1); s2 = x32_add(s2);
    if (lane == 0) *(v2f*)(lds + L_ST + w * 8) = v2f{s1, s2};
    if (kq == 2 * p) {
      float* r = (float*)(lds + L_RED) + (w * 2 + p) * 64 + c8 * 8;
      *(v4f*)r = v4f{d[0][0], d[1][0], d[2][0], d[3][0]};
      *(v4f*)(r + 4) = v4f{d[4][0], d[5][0], d[6][0], d[7][0]};
      if (rep == 0) {
        float* rc = (float*)(lds + L_REDC) + (w * 2 + p) * 64 + c8 * 8;
        *(v4f*)rc = v4f{d[0][1], d[1][1], d[2][1], d[3][1]};
        *(v4f*)(rc + 4) = v4f{d[4][1], d[5][1], d[6][1], d[7][1]};
      }
    }
    BAR();
    if (w == 0) {
      const int n4 = lane & 15;
      v4f acc = {0.f, 0.f, 0.f, 0.f};
#pragma unroll
      for (int i = 0; i < 4; ++i) acc += *(const v4f*)((float*)(lds + L_RED) + (kq * 4 + i) * 64 + n4 * 4);
      v4f cs;
      if (rep == 0) {
        cs = v4f{0.f, 0.f, 0.f, 0.f};
#pragma unroll
        for (int i = 0; i < 4; ++i) cs += *(const v4f*)((float*)(lds + L_REDC) + (kq * 4 + i) * 64 + n4 * 4);
#pragma unroll
        for (int e = 0; e < 4; ++e) cs[e] = x32_add(x16_add(cs[e]));
        if (kq == 0) *(v4f*)((float*)(lds + L_CS) + TYPE * 64 + n4 * 4) = cs;
      } else {
        cs = *(const v4f*)((float*)(lds + L_CS) + TYPE * 64 + n4 * 4);
      }
      float t1 = 0.f, t2 = 0.f;
#pragma unroll
      for (int i = 0; i < 4; ++i) { v4f st = *(const v4f*)(lds + L_ST + i * 16); t1 += st[0] + st[2]; t2 += st[1] + st[3]; }
#pragma unroll
      for (int e = 0; e < 4; ++e) acc[e] = x32_add(x16_add(acc[e]));
      const float mean = t1 * (1.f / 512.f);
      const float var = fmaxf(t2 * (1.f / 512.f) - mean * mean, 0.f);
      const float rstd = __builtin_amdgcn_rsqf(var + 1e-5f) * C2;
      v2u qq;
      qq[0] = safe(pk((acc[0] - mean * cs[0]) * rstd, (acc[1] - mean * cs[1]) * rstd));
      qq[1] = safe(pk((acc[2] - mean * cs[2]) * rstd, (acc[3] - mean * cs[3]) * rstd));
      if (kq == 0) __builtin_amdgcn_raw_buffer_store_b64(qq, rs, WS_BOX + cu * BOX + L * BOXL + BOX_Q + n4 * 8, 0, 16);
      ST(L * 8 + 1);
      if (L == 0) {
        const u64 flag = (u64)tag | ((u64)(~tag) << 32);
        if (lane == 0) __hip_atomic_store((GAS u64*)(ws + WS_RDY + b * 512 + (s * 8 + g) * 8), flag, __ATOMIC_RELAXED, __HIP_MEMORY_SCOPE_AGENT);
        for (u32 spins = 0;; ++spins) {
          const u64 f = __hip_atomic_load((GAS u64*)(ws + WS_RDY + b * 512 + lane * 8), __ATOMIC_RELAXED, __HIP_MEMORY_SCOPE_AGENT);
          if (__all(f == flag) || spins > SPIN_CAP) break;
        }
        if (g == 0 && s == 0 && lane == 0) __hip_atomic_store((GAS u32*)(ws + WS_EPOCH + b * 256), tag, __ATOMIC_RELAXED, __HIP_MEMORY_SCOPE_AGENT);
      }
      if (DLY_C) __builtin_amdgcn_s_sleep(DLY_C);
      const u32 voff = WS_BOX + (u32)((b * 8 + g) * 8 + (lane >> 3)) * BOX + L * BOXL + BOX_Q + c8 * 16;
      v4u q = poll128(ws, voff);
      ST(L * 8 + 2);
      *(v4u*)(lds + L_Q + (lane >> 4) * 272 + (lane & 15) * 16) = q;
    }
    if (rep == 0) {
      const u32 vo = (u32)(lane >> 3) * 8192 + c8 * 16;
      asm volatile("" : "+s"(cwo));
      const char* bo = cwo + (size_t)(512 * g + 64 * w) * 1024 + 128 * s;
      bulk8<RO, 1024>(bo, vo, rb);
      if (TYPE == 0) {
        const u32 vq = (u32)(lane >> 3) * 65536 + c8 * 16;
        asm volatile("" : "+s"(nwq));
        const char* bq = nwq + (size_t)(64 * w) * 8192 + (512 * g + 64 * s) * 2;
        bulk8<R_WQB, 8192>(bq, vq, rb);
        VMWAIT(16);
      } else {
        VMWAIT(8);
      }
    }
  }
  ST(L * 8 + 6);
  BAR();
  ST(L * 8 + 7);
  if (w < 4) {
    OPAQUE_LANE();
    const char* kimg = lds + L_KV + TYPE * 65536;
    const u32 vimg = L_KV + TYPE * 65536 + 32768;
    const int r16 = lane & 15;
    v4f sc[2];
#pragma unroll
    for (int blk = 0; blk < 2; ++blk) {
      const int key = 32 * w + 8 * (r16 >> 2) + (r16 & 3) + 4 * blk;
      const u32 ka = 2048 * (key >> 3) + 64 * (key & 7) + 16 * (kq ^ ((key >> 2) & 3));
      v4f acc = {0.f, 0.f, 0.f, 0.f};
#pragma unroll
      for (int t = 0; t < 4; ++t) {
        const v4u kf = *(const v4u*)(kimg + ka + 512 * t);
        const v4u qf = *(const v4u*)(lds + L_Q + (lane & 3) * 272 + t * 64 + kq * 16);
        acc = mfma(kf, qf, acc);
      }
      sc[blk] = acc;
    }
    float m = fmaxf(fmaxf(fmaxf(sc[0][0], sc[0][1]), fmaxf(sc[0][2], sc[0][3])), fmaxf(fmaxf(sc[1][0], sc[1][1]), fmaxf(sc[1][2], sc[1][3])));
    m = x32_max(x16_max(m));
    v4u pa;
    pa[0] = pk(__builtin_amdgcn_exp2f(sc[0][0] - m), __builtin_amdgcn_exp2f(sc[0][1] - m));
    pa[1] = pk(__builtin_amdgcn_exp2f(sc[0][2] - m), __builtin_amdgcn_exp2f(sc[0][3] - m));
    pa[2] = pk(__builtin_amdgcn_exp2f(sc[1][0] - m), __builtin_amdgcn_exp2f(sc[1][1] - m));
    pa[3] = pk(__builtin_amdgcn_exp2f(sc[1][2] - m), __builtin_amdgcn_exp2f(sc[1][3] - m));
    const v4f dl = mfma(pa, v4u{0x3F803F80u, 0x3F803F80u, 0x3F803F80u, 0x3F803F80u}, v4f{0.f, 0.f, 0.f, 0.f});
    const int q4 = (lane & 15) >> 2, p2 = lane & 3;
    v4f o[8];
#pragma unroll
    for (int c = 0; c < 8; ++c) {
      v4u vf;
#pragma unroll
      for (int t = 0; t < 2; ++t) {
        const u32 addr = vimg + 2048 * (4 * w + kq) + 512 * (c >> 1) + 64 * (4 * t + q4) + 16 * ((2 * (c & 1) + (p2 >> 1)) ^ ((2 * kq + t) & 3)) + 8 * (p2 & 1);
        const v4s r = __builtin_amdgcn_ds_read_tr16_b64_v4i16((LAS v4s*)(size_t)addr);
        const v2u rr = __builtin_bit_cast(v2u, r);
        vf[2 * t] = rr[0]; vf[2 * t + 1] = rr[1];
      }
      o[c] = mfma(pa, vf, v4f{0.f, 0.f, 0.f, 0.f});
    }
    if (lane < 16) {
#pragma unroll
      for (int h = 0; h < 4; ++h) {
        float* dst = (float*)(lds + L_OM) + ((w * 4 + h) * 2) * 64 + lane * 4;
        *(v4f*)dst = v4f{o[0][h], o[1][h], o[2][h], o[3][h]};
        *(v4f*)(dst + 64) = v4f{o[4][h], o[5][h], o[6][h], o[7][h]};
      }
      if (lane < 4) ((float*)(lds + L_ML))[w * 4 + lane] = m;
      if (lane == 0) *(v4f*)((float*)(lds + L_ML) + 16 + w * 4) = dl;
    }
  }
  BAR();
  {
    OPAQUE_LANE();
    const int h = w >> 1, half = w & 1;
    const float* ml = (const float*)(lds + L_ML);
    const float m0 = ml[h], m1 = ml[4 + h], m2 = ml[8 + h], m3 = ml[12 + h];
    const float l0 = ml[16 + h], l1 = ml[20 + h], l2 = ml[24 + h], l3 = ml[28 + h];
    const float* om = (const float*)(lds + L_OM) + (h * 2 + half) * 64 + lane;
    const float o0 = om[0], o1 = om[512], o2 = om[1024], o3 = om[1536];
    const float M = fmaxf(fmaxf(m0, m1), fmaxf(m2, m3));
    const float w0 = __builtin_amdgcn_exp2f(m0 - M), w1 = __builtin_amdgcn_exp2f(m1 - M), w2 = __builtin_amdgcn_exp2f(m2 - M), w3 = __builtin_amdgcn_exp2f(m3 - M);
    const float Ls = w0 * l0 + w1 * l1 + w2 * l2 + w3 * l3;
    const float inv = __builtin_amdgcn_rcpf(Ls);
    const float ov = (w0 * o0 + w1 * o1 + w2 * o2 + w3 * o3) * inv;
    const float on = dpp<0x104>(ov);
    const u32 pr = safe(pk(ov, on));
    const int u = 8 * (lane & 3) + (lane >> 3);
    const int pos = (u & ~3) | ((u - rb) & 3);
    if (((lane >> 2) & 1) == 0) __builtin_amdgcn_raw_buffer_store_b32(pr, rs, WS_BOX + cu * BOX + L * BOXL + w * 128 + pos * 4, 0, 16);
    if (half == 0 && lane < 8) {
      const float lse = M + __builtin_amdgcn_logf(Ls);
      __builtin_amdgcn_raw_buffer_store_b32(safe(__float_as_uint(lse)), rs, WS_LSR + (u32)(((L * 32 + b * 8 + g) * 8 + lane) * 128 + h * 32 + s * 4), 0, 16);
    }
    ST(L * 8 + 3);
  }
  {
    OPAQUE_LANE();
    if (DLY_A) __builtin_amdgcn_s_sleep(DLY_A);
    const u32 voff = WS_BOX + (u32)((b * 8 + g) * 8 + c8) * BOX + L * BOXL + w * 128 + kq * 32 + p * 16;
    const u32 voff2 = WS_LSR + (u32)(((L * 32 + b * 8 + g) * 8 + s) * 128 + (w >> 1) * 32 + (kq & 1) * 16);
    v4u a, ls;
    poll128x2(ws, voff, voff2, a, ls);
    ST(L * 8 + 4);
    if (rep == 0 && TYPE == 0) {
      asm volatile("" : "+s"(nkc), "+s"(nvc));
      const int r8 = (lane & 31) >> 2;
#pragma unroll
      for (int i = 0; i < 4; ++i) {
        const u32 ch = 8 * (i & 1) + 4 * (lane >> 5) + ((lane & 3) ^ ((2 * (i >> 1) + (r8 >> 2)) & 3));
        const u32 vo = r8 * 256 + ch * 16;
        const size_t rowb = (size_t)(16 * w + 8 * (i >> 1)) * 256;
        dma16(vo, nkc + rowb, L_KV + 65536 + w * 4096 + i * 1024);
        dma16(vo, nvc + rowb, L_KV + 65536 + 32768 + w * 4096 + i * 1024);
      }
    }
    if (rep == 0) transpose8<RO>();
    v4f d[8];
    mfma8<RO>(d, a);
    const float e0 = __uint_as_float(ls[0]), e1 = __uint_as_float(ls[1]), e2 = __uint_as_float(ls[2]), e3 = __uint_as_float(ls[3]);
    float M = fmaxf(fmaxf(e0, e1), fmaxf(e2, e3));
    M = x16_max(M);
    float w0 = __builtin_amdgcn_exp2f(e0 - M), w1 = __builtin_amdgcn_exp2f(e1 - M), w2 = __builtin_amdgcn_exp2f(e2 - M), w3 = __builtin_amdgcn_exp2f(e3 - M);
    float Z = x16_add((w0 + w1) + (w2 + w3));
    float inv = __builtin_amdgcn_rcpf(Z);
    if (p != (kq >> 1)) inv = 0.f;
    w0 *= inv; w1 *= inv; w2 *= inv; w3 *= inv;
    float y[8];
#pragma unroll
    for (int t = 0; t < 8; ++t) y[t] = (w0 * d[t][0] + w1 * d[t][1]) + (w2 * d[t][2] + w3 * d[t][3]);
    float u[4];
#pragma unroll
    for (int t = 0; t < 4; ++t) { auto r = __builtin_amdgcn_permlane32_swap(__float_as_uint(y[t]), __float_as_uint(y[t + 4]), false, false); u[t] = __uint_as_float(r[0]) + __uint_as_float(r[1]); }
    float z[2];
#pragma unroll
    for (int t = 0; t < 2; ++t) { auto r = __builtin_amdgcn_permlane16_swap(__float_as_uint(u[t]), __float_as_uint(u[t + 2]), false, false); z[t] = __uint_as_float(r[0]) + __uint_as_float(r[1]); }
    const float z0 = z[0] + dpp<ROR8>(z[0]), z1 = z[1] + dpp<ROR8>(z[1]);
    const float yy = p ? z1 : z0;
    ((float*)(lds + L_RED3))[w * 64 + lane] = yy;
    BAR();
    if (w == 0) {
      const float* r3 = (const float*)(lds + L_RED3) + lane;
      const float sum = ((r3[0] + r3[64]) + (r3[128] + r3[192])) + ((r3[256] + r3[320]) + (r3[384] + r3[448]));
      const float sn = dpp<ROR8>(sum);
      const u32 pr = safe(pk(sum, sn));
      const int u2 = 4 * c8 + (lane >> 4);
      const int pos = (u2 & ~3) | ((u2 - rb) & 3);
      if (p == 0) __builtin_amdgcn_raw_buffer_store_b32(pr, rs, WS_BOX + cu * BOX + L * BOXL + BOX_X + pos * 4, 0, 16);
      ST(L * 8 + 5);
    }
  }
}

__global__ void __launch_bounds__(512, 2) __attribute__((amdgpu_num_vgpr(64)))
fused_kernel(const char* __restrict__ xin, const char* kA, const char* vA, const char* wqA, const char* woA,
             const char* kB, const char* vB, const char* wqB, const char* woB, unsigned short* out, char* ws) {
  extern __shared__ __attribute__((aligned(16))) char lds[];
  const int id = blockIdx.x, g = id & 7, b = id >> 6, s = (id >> 3) & 7;
  const int tid = threadIdx.x, lane = tid & 63, w = __builtin_amdgcn_readfirstlane(tid >> 6);
  const int c8 = lane & 7, p = (lane >> 3) & 1, kq = lane >> 4;
  const int rb = ROT ? b : 0;
  int stampv = 0;
  const __amdgpu_buffer_rsrc_t rs = __builtin_amdgcn_make_buffer_rsrc(ws, 0, WS_END, 0x00020000);
  const u32 cu = (u32)((b * 8 + g) * 8 + s);
  ST(40); STC(50);
  u32 ep; v4u xraw;
  asm volatile("global_load_dword %0, %2, %3 sc1\n\tglobal_load_dwordx4 %1, %4, %5"
               : "=&v"(ep), "=&v"(xraw) : "v"(WS_EPOCH + b * 256), "s"(ws), "v"((u32)((b * 512 + 64 * w + 16 * kq + 8 * p) * 2)), "s"(xin) : "memory");
  {
    const v4u sv = {SENT, SENT, SENT, SENT};
    if (w < 5) __builtin_amdgcn_raw_buffer_store_b128(sv, rs, WS_BOX + cu * BOX + w * 1024 + lane * 16, 0, 16);
    else if (w < 7) {
      const int i = (w - 5) * 64 + lane;
      __builtin_amdgcn_raw_buffer_store_b32(SENT, rs, WS_LSR + (u32)((((i >> 5) * 32 + b * 8 + g) * 8 + ((i >> 2) & 7)) * 128 + (i & 3) * 32 + s * 4), 0, 16);
    }
  }
  asm volatile("" ::: "v128", "v255");
  const size_t kvoff = ((size_t)(b * 8 + g) * SK + (SK - WIN) + s * 128) * 256;
  {
    const u32 vq = (u32)(lane >> 3) * 65536 + c8 * 16;
    const char* bq = wqA + (size_t)(64 * w) * 8192 + (512 * g + 64 * s) * 2;
    bulk8<R_WQA, 8192>(bq, vq, rb);
    const int r8 = (lane & 31) >> 2;
#pragma unroll
    for (int i = 0; i < 4; ++i) {
      const u32 ch = 8 * (i & 1) + 4 * (lane >> 5) + ((lane & 3) ^ ((2 * (i >> 1) + (r8 >> 2)) & 3));
      const u32 vo = r8 * 256 + ch * 16;
      const size_t rowb = (size_t)(16 * w + 8 * (i >> 1)) * 256;
      dma16(vo, kA + kvoff + rowb, L_KV + w * 4096 + i * 1024);
    }
#pragma unroll
    for (int i = 0; i < 4; ++i) {
      const u32 ch = 8 * (i & 1) + 4 * (lane >> 5) + ((lane & 3) ^ ((2 * (i >> 1) + (r8 >> 2)) & 3));
      const u32 vo = r8 * 256 + ch * 16;
      const size_t rowb = (size_t)(16 * w + 8 * (i >> 1)) * 256;
      dma16(vo, vA + kvoff + rowb, L_KV + 32768 + w * 4096 + i * 1024);
    }
  }
  ST(42);
  asm volatile("s_waitcnt vmcnt(16)" : "+v"(xraw), "+v"(ep) :: "memory");
  ST(43);
  const u32 tag = (u32)__builtin_amdgcn_readfirstlane((int)ep) + 1u;
  float xo[8];
  {
    v4u xr;
    if (rb == 0) xr = xraw; else if (rb == 1) xr = v4u{xraw[1], xraw[2], xraw[3], xraw[0]}; else if (rb == 2) xr = v4u{xraw[2], xraw[3], xraw[0], xraw[1]}; else xr = v4u{xraw[3], xraw[0], xraw[1], xraw[2]};
#pragma unroll
    for (int m = 0; m < 4; ++m) { xo[2 * m] = bflo(xr[m]); xo[2 * m + 1] = bfhi(xr[m]); }
  }
  for (int rep = 0; rep < 2; ++rep) {
    layer<0>(rep, ws, lds, rs, b, g, s, w, lane, tid, xo, kB + kvoff, vB + kvoff, wqB, woA, tag, stampv);
    layer<1>(rep, ws, lds, rs, b, g, s, w, lane, tid, xo, nullptr, nullptr, nullptr, woB, tag, stampv);
  }
  if (g == 0 && w == s) {
    if (DLY_B) __builtin_amdgcn_s_sleep(DLY_B);
    const u32 voff = WS_BOX + (u32)((b * 8 + c8) * 8 + w) * BOX + 3u * BOXL + BOX_X + kq * 32 + p * 16;
    v4u x = poll128(ws, voff);
    float v[8];
#pragma unroll
    for (int m = 0; m < 4; ++m) { v[2 * m] = bflo(x[m]); v[2 * m + 1] = bfhi(x[m]); }
#pragma unroll
    for (int j = 0; j < 8; ++j) v[j] += dpp<XOR1>(v[j]);
#pragma unroll
    for (int j = 0; j < 8; ++j) v[j] += dpp<XOR2>(v[j]);
#pragma unroll
    for (int j = 0; j < 8; ++j) v[j] += dpp<HMIR>(v[j]);
    if (c8 == 0) {
      u32* op = (u32*)(out + b * 512 + 64 * w + 16 * kq + 8 * p);
#pragma unroll
      for (int m = 0; m < 4; ++m) op[(m + rb) & 3] = pk(xo[2 * m] + v[2 * m], xo[2 * m + 1] + v[2 * m + 1]);
    }
    ST(41);
  }
}

extern "C" void kernel_launch(void* const* d_in, const int* in_sizes, int n_in, void* d_out, int out_size, void* d_ws, size_t ws_size,
                              hipStream_t stream) {
  (void)hipFuncSetAttribute((const void*)fused_kernel, hipFuncAttributeMaxDynamicSharedMemorySize, L_END);
  fused_kernel<<<256, 512, L_END, stream>>>((const char*)d_in[0], (const char*)d_in[1], (const char*)d_in[2], (const char*)d_in[3], (const char*)d_in[4],
                                            (const char*)d_in[6], (const char*)d_in[7], (const char*)d_in[8], (const char*)d_in[9],
                                            (unsigned short*)d_out, (char*)d_ws);
}
```

```cpp
#include <hip/hip_runtime.h>
#include <stdint.h>

typedef unsigned int u32;
typedef unsigned long long u64;
typedef u32 v4u __attribute__((ext_vector_type(4)));
typedef u32 v2u __attribute__((ext_vector_type(2)));
typedef float v4f __attribute__((ext_vector_type(4)));
typedef float v2f __attribute__((ext_vector_type(2)));
typedef __bf16 v8b __attribute__((ext_vector_type(8)));
typedef __bf16 v2b __attribute__((ext_vector_type(2)));
#define GAS __attribute__((address_space(1)))

#define DLY_C 2
#define DLY_A 8
#define DLY_B 10
#define SCHED 0
#define NTKV " nt"
#define NTW ""

namespace {
constexpr int SK = 4096, WIN = 1024;
constexpr u32 WS_EPOCH = 0;
constexpr u32 WS_RDY = 4096;
constexpr u32 WS_LSR = 8192;
constexpr u32 WS_BOX = WS_LSR + 131072;
constexpr u32 BOXL = 1280, BOX_Q = 1024, BOX_X = 1152, BOX_CS = 5120, BOX = 5376;
constexpr u32 WS_END = WS_BOX + 256 * BOX;
constexpr u32 WS_DBG = 4u << 20;
constexpr u32 L_KV = 0;
constexpr u32 L_K = 0, L_V = 65536;
constexpr u32 L_OM = 131072;
constexpr u32 L_ML = L_OM + 8192;
constexpr u32 L_Q = L_ML + 128;
constexpr u32 L_CSI = L_Q + 1088;
constexpr u32 L_RED = L_CSI + 2176;
constexpr u32 L_REDC = L_RED + 2048;
constexpr u32 L_ST = L_REDC + 2048;
constexpr u32 L_RED3 = L_ST + 64;
constexpr u32 L_XS = L_RED3 + 2048;
constexpr u32 L_END = L_XS + 256;
static_assert(L_END <= 160 * 1024, "lds");
static_assert(L_Q % 16 == 0 && L_CSI % 16 == 0 && L_RED % 16 == 0 && L_ST % 16 == 0 && L_RED3 % 16 == 0 && L_XS % 16 == 0, "align");
constexpr u32 SENT = 0xFFFFFFFFu;
constexpr u32 SPIN_CAP = 1u << 15;
constexpr float C2 = 0.08838834764831845f * 1.4426950408889634f;
constexpr u32 ONES = 0x3F803F80u;
constexpr u64 PMASK = 0xFF00FF00FF00FF00ull;
constexpr u64 C8Z = 0x0101010101010101ull;

__device__ __forceinline__ float bflo(u32 w) { return __uint_as_float(w << 16); }
__device__ __forceinline__ float bfhi(u32 w) { return __uint_as_float(w & 0xffff0000u); }
__device__ __forceinline__ u32 pk(float lo, float hi) { v2f f = {lo, hi}; return __builtin_bit_cast(u32, __builtin_convertvector(f, v2b)); }
__device__ __forceinline__ u32 safe(u32 v) { return v < 0xFFFFFFFEu ? v : 0xFFFFFFFEu; }
template <int CTRL> __device__ __forceinline__ float dpp(float x) {
  return __builtin_bit_cast(float, __builtin_amdgcn_mov_dpp(__builtin_bit_cast(int, x), CTRL, 0xf, 0xf, true));
}
template <int CTRL> __device__ __forceinline__ u32 dppu(u32 x) { return (u32)__builtin_amdgcn_mov_dpp((int)x, CTRL, 0xf, 0xf, true); }
constexpr int XOR1 = 0xB1, XOR2 = 0x4E, HMIR = 0x141, ROR8 = 0x128;
__device__ __forceinline__ float x16_add(float x) { auto s = __builtin_amdgcn_permlane16_swap(__float_as_uint(x), __float_as_uint(x), false, false); return __uint_as_float(s[0]) + __uint_as_float(s[1]); }
__device__ __forceinline__ float x32_add(float x) { auto s = __builtin_amdgcn_permlane32_swap(__float_as_uint(x), __float_as_uint(x), false, false); return __uint_as_float(s[0]) + __uint_as_float(s[1]); }
__device__ __forceinline__ v4f mfma(v4u a, v4u b, v4f c) {
  return __builtin_amdgcn_mfma_f32_16x16x32_bf16(__builtin_bit_cast(v8b, a), __builtin_bit_cast(v8b, b), c, 0, 0, 0);
}
#define BAR() asm volatile("s_waitcnt lgkmcnt(0)\n\ts_barrier" ::: "memory")
#define VMWAIT(n) asm volatile("s_waitcnt vmcnt(" #n ")" ::: "memory")

constexpr int R_WQA = 128, R_WOA = 160, R_WQB = 192, R_WOB = 224;
template <int BASE, u32 STRIDE>
__device__ __forceinline__ void bulk8(const char* base, u32 voff) {
  asm volatile("" : "+s"(base));
  const __amdgpu_buffer_rsrc_t r = __builtin_amdgcn_make_buffer_rsrc((void*)base, 0, 0x7fffffff, 0x00020000);
  asm volatile("s_nop 4\n\t"
               "buffer_load_dwordx4 v[%c10:%c11], %0, %1, 0 offen" NTW "\n\tbuffer_load_dwordx4 v[%c12:%c13], %0, %1, %3 offen" NTW "\n\t"
               "buffer_load_dwordx4 v[%c14:%c15], %0, %1, %4 offen" NTW "\n\tbuffer_load_dwordx4 v[%c16:%c17], %0, %1, %5 offen" NTW "\n\t"
               "buffer_load_dwordx4 v[%c18:%c19], %0, %1, %6 offen" NTW "\n\tbuffer_load_dwordx4 v[%c20:%c21], %0, %1, %7 offen" NTW "\n\t"
               "buffer_load_dwordx4 v[%c22:%c23], %0, %1, %8 offen" NTW "\n\tbuffer_load_dwordx4 v[%c24:%c25], %0, %1, %9 offen" NTW
               :: "v"(voff), "s"(r), "s"(0), "s"(STRIDE), "s"(2 * STRIDE), "s"(3 * STRIDE), "s"(4 * STRIDE), "s"(5 * STRIDE), "s"(6 * STRIDE), "s"(7 * STRIDE),
                  "i"(BASE), "i"(BASE + 3), "i"(BASE + 4), "i"(BASE + 7), "i"(BASE + 8), "i"(BASE + 11), "i"(BASE + 12), "i"(BASE + 15),
                  "i"(BASE + 16), "i"(BASE + 19), "i"(BASE + 20), "i"(BASE + 23), "i"(BASE + 24), "i"(BASE + 27), "i"(BASE + 28), "i"(BASE + 31)
               : "memory");
}
template <int B, int M, int R>
__device__ __forceinline__ void tp_pair(u32 slo, u32 shi) {
  if constexpr (M == R) {
    asm volatile("v_perm_b32 v48, v%c3, v%c2, %0\n\tv_perm_b32 v%c3, v%c3, v%c2, %1\n\tv_mov_b32 v%c2, v48"
                 :: "s"(slo), "s"(shi), "i"(B + 8 * M + R), "i"(B + 8 * M + 4 + R));
  } else {
    asm volatile("v_perm_b32 v48, v%c3, v%c2, %0\n\tv_perm_b32 v49, v%c3, v%c2, %1\n\t"
                 "v_perm_b32 v%c2, v%c5, v%c4, %0\n\tv_perm_b32 v%c3, v%c5, v%c4, %1\n\t"
                 "v_mov_b32 v%c4, v48\n\tv_mov_b32 v%c5, v49"
                 :: "s"(slo), "s"(shi), "i"(B + 8 * M + R), "i"(B + 8 * M + 4 + R), "i"(B + 8 * R + M), "i"(B + 8 * R + 4 + M));
  }
}
template <int B>
__device__ __forceinline__ void transpose8() {
  const u32 slo = 0x05040100u, shi = 0x07060302u;
  tp_pair<B, 0, 0>(slo, shi); tp_pair<B, 1, 1>(slo, shi); tp_pair<B, 2, 2>(slo, shi); tp_pair<B, 3, 3>(slo, shi);
  tp_pair<B, 0, 1>(slo, shi); tp_pair<B, 0, 2>(slo, shi); tp_pair<B, 0, 3>(slo, shi);
  tp_pair<B, 1, 2>(slo, shi); tp_pair<B, 1, 3>(slo, shi); tp_pair<B, 2, 3>(slo, shi);
  asm volatile("s_nop 1");
}
#define SNOP
#define MF(d, b) "v_mfma_f32_16x16x32_bf16 v[" #d ":" #d "+3], v[48:51], v[%c[wb]+" #b ":%c[wb]+" #b "+3], 0\n\t"
#define FOLD_TAIL \
  "v_permlane32_swap_b32 v52, v68\n\t" \
  "v_permlane32_swap_b32 v56, v72\n\t" \
  "v_permlane32_swap_b32 v60, v76\n\t" \
  "v_permlane32_swap_b32 v64, v80\n\t" \
  "v_cndmask_b32_e64 v52, v52, v68, %[pm]\n\t" \
  "v_cndmask_b32_e64 v60, v60, v76, %[pm]\n\t" \
  "v_cndmask_b32_e64 v56, v56, v72, %[pm]\n\t" \
  "v_cndmask_b32_e64 v64, v64, v80, %[pm]\n\t" \
  "v_permlane16_swap_b32 v52, v60\n\t" \
  "s_nop 0\n\t" \
  "v_permlane16_swap_b32 v56, v64\n\t" \
  "v_add_f32 v52, v52, v60\n\t" \
  "v_add_f32 v56, v56, v64\n\t" \
  "s_nop 0\n\t" \
  "v_add_f32_dpp v52, v52, v52 row_ror:8 row_mask:0xf bank_mask:0xf\n\t" \
  "v_add_f32_dpp v56, v56, v56 row_ror:8 row_mask:0xf bank_mask:0xf\n\t" \
  "v_cndmask_b32_e64 v52, v52, v56, %[pm]\n\t"

template <int WB, bool POLL, int LOFF>
__device__ __forceinline__ void seg_p1(u32 voff, const char* ws, u32 ldsw, u32& spins) {
  if constexpr (POLL) {
    asm volatile(
      SNOP "Lp%=:\n\t"
      "global_load_dwordx4 v[48:51], %[voff], %[ws] sc1\n\t"
      "s_add_u32 %[sp], %[sp], 1\n\t"
      "s_waitcnt vmcnt(0)\n\t"
      MF(52, 0) "v_max3_u32 v84, v48, v49, v50\n\t"
      MF(56, 4) "v_max_u32 v84, v84, v51\n\t"
      MF(60, 8) "v_cmp_eq_u32 vcc, -1, v84\n\t"
      MF(64, 12)
      "s_cbranch_vccz Lk%=\n\t"
      "s_cmp_lt_u32 %[sp], %[cap]\n\t"
      "s_cbranch_scc1 Lp%=\n\t"
      "Lk%=:\n\t"
      : [sp] "+s"(spins) : [voff] "v"(voff), [ws] "s"(ws), [wb] "i"(WB), [cap] "i"(SPIN_CAP) : "memory", "vcc", "scc");
  } else {
    asm volatile("s_nop 1\n\t" MF(52, 0) "s_nop 0\n\t" MF(56, 4) "s_nop 0\n\t" MF(60, 8) "s_nop 0\n\t" MF(64, 12) "s_nop 0\n\t" :: [wb] "i"(WB));
  }
  asm volatile(
    MF(68, 16) "v_pk_add_f32 v[52:53], v[52:53], v[54:55]\n\t"
    MF(72, 20) "v_pk_add_f32 v[56:57], v[56:57], v[58:59]\n\t"
    MF(76, 24) "v_pk_add_f32 v[60:61], v[60:61], v[62:63]\n\t"
    MF(80, 28) "v_pk_add_f32 v[64:65], v[64:65], v[66:67]\n\t"
    "v_add_f32 v52, v52, v53\n\t"
    "v_pk_add_f32 v[68:69], v[68:69], v[70:71]\n\t"
    "v_add_f32 v56, v56, v57\n\t"
    "v_pk_add_f32 v[72:73], v[72:73], v[74:75]\n\t"
    "v_add_f32 v60, v60, v61\n\t"
    "v_pk_add_f32 v[76:77], v[76:77], v[78:79]\n\t"
    "v_add_f32 v64, v64, v65\n\t"
    "v_pk_add_f32 v[80:81], v[80:81], v[82:83]\n\t"
    "v_add_f32 v68, v68, v69\n\t"
    "v_add_f32 v72, v72, v73\n\t"
    "v_add_f32 v76, v76, v77\n\t"
    "v_add_f32 v80, v80, v81\n\t"
    "s_nop 0\n\t"
    FOLD_TAIL
    "ds_write_b32 %[lw], v52 offset:%c[lo]\n\t"
    :: [wb] "i"(WB), [pm] "s"(PMASK), [lw] "v"(ldsw), [lo] "i"(LOFF) : "memory");
}
template <int WB>
__device__ __forceinline__ void seg_p3(u32 voff, u32 voff2, const char* ws, const char* ws2, u32 ldsw, u32& spins) {
  asm volatile(
    SNOP "Lp%=:\n\t"
    "global_load_dwordx4 v[48:51], %[voff], %[ws] sc1\n\t"
    "global_load_dword v84, %[voff2], %[ws2] sc1\n\t"
    "s_add_u32 %[sp], %[sp], 1\n\t"
    "s_waitcnt vmcnt(0)\n\t"
    MF(52, 0) "v_max3_u32 v85, v48, v49, v50\n\t"
    MF(56, 4) "v_max3_u32 v85, v85, v51, v84\n\t"
    MF(60, 8) "v_cmp_eq_u32 vcc, -1, v85\n\t"
    MF(64, 12)
    "s_cbranch_vccz Lk%=\n\t"
    "s_cmp_lt_u32 %[sp], %[cap]\n\t"
    "s_cbranch_scc1 Lp%=\n\t"
    "Lk%=:\n\t"
    "v_max_f32_dpp v85, v84, v84 quad_perm:[1,0,3,2] row_mask:0xf bank_mask:0xf\n\t"
    MF(68, 16)
    "s_nop 0\n\t"
    "v_max_f32_dpp v85, v85, v85 quad_perm:[2,3,0,1] row_mask:0xf bank_mask:0xf\n\t"
    MF(72, 20)
    "s_nop 0\n\t"
    "v_max_f32_dpp v85, v85, v85 row_half_mirror row_mask:0xf bank_mask:0xf\n\t"
    MF(76, 24)
    "v_sub_f32 v86, v84, v85\n\t"
    MF(80, 28)
    "v_exp_f32 v86, v86\n\t"
    "s_nop 1\n\t"
    "v_mov_b32_dpp v90, v86 row_newbcast:0 row_mask:0x5 bank_mask:0xf\n\t"
    "v_mov_b32_dpp v91, v86 row_newbcast:1 row_mask:0x5 bank_mask:0xf\n\t"
    "v_mov_b32_dpp v92, v86 row_newbcast:2 row_mask:0x5 bank_mask:0xf\n\t"
    "v_mov_b32_dpp v93, v86 row_newbcast:3 row_mask:0x5 bank_mask:0xf\n\t"
    "v_mov_b32_dpp v90, v86 row_newbcast:4 row_mask:0xa bank_mask:0xf\n\t"
    "v_mov_b32_dpp v91, v86 row_newbcast:5 row_mask:0xa bank_mask:0xf\n\t"
    "v_mov_b32_dpp v92, v86 row_newbcast:6 row_mask:0xa bank_mask:0xf\n\t"
    "v_mov_b32_dpp v93, v86 row_newbcast:7 row_mask:0xa bank_mask:0xf\n\t"
    "v_add_f32_dpp v87, v86, v86 quad_perm:[1,0,3,2] row_mask:0xf bank_mask:0xf\n\t"
    "v_pk_mul_f32 v[52:53], v[52:53], v[90:91]\n\t"
    "v_pk_mul_f32 v[56:57], v[56:57], v[90:91]\n\t"
    "v_add_f32_dpp v87, v87, v87 quad_perm:[2,3,0,1] row_mask:0xf bank_mask:0xf\n\t"
    "v_pk_mul_f32 v[60:61], v[60:61], v[90:91]\n\t"
    "v_pk_mul_f32 v[64:65], v[64:65], v[90:91]\n\t"
    "v_add_f32_dpp v87, v87, v87 row_half_mirror row_mask:0xf bank_mask:0xf\n\t"
    "v_pk_mul_f32 v[68:69], v[68:69], v[90:91]\n\t"
    "v_pk_mul_f32 v[72:73], v[72:73], v[90:91]\n\t"
    "v_rcp_f32 v87, v87\n\t"
    "v_pk_mul_f32 v[76:77], v[76:77], v[90:91]\n\t"
    "v_pk_mul_f32 v[80:81], v[80:81], v[90:91]\n\t"
    "v_pk_fma_f32 v[52:53], v[54:55], v[92:93], v[52:53]\n\t"
    "v_pk_fma_f32 v[68:69], v[70:71], v[92:93], v[68:69]\n\t"
    "v_pk_fma_f32 v[56:57], v[58:59], v[92:93], v[56:57]\n\t"
    "v_pk_fma_f32 v[72:73], v[74:75], v[92:93], v[72:73]\n\t"
    "v_pk_fma_f32 v[60:61], v[62:63], v[92:93], v[60:61]\n\t"
    "v_pk_fma_f32 v[76:77], v[78:79], v[92:93], v[76:77]\n\t"
    "v_pk_fma_f32 v[64:65], v[66:67], v[92:93], v[64:65]\n\t"
    "v_pk_fma_f32 v[80:81], v[82:83], v[92:93], v[80:81]\n\t"
    "v_add_f32 v52, v52, v53\n\t"
    "v_add_f32 v68, v68, v69\n\t"
    "v_add_f32 v56, v56, v57\n\t"
    "v_add_f32 v72, v72, v73\n\t"
    "v_add_f32 v60, v60, v61\n\t"
    "v_add_f32 v76, v76, v77\n\t"
    "v_add_f32 v64, v64, v65\n\t"
    "v_add_f32 v80, v80, v81\n\t"
    "s_nop 1\n\t"
    FOLD_TAIL
    "v_mul_f32 v52, v52, v87\n\t"
    "ds_write_b32 %[lw], v52 offset:%c[lo]\n\t"
    : [sp] "+s"(spins)
    : [voff] "v"(voff), [voff2] "v"(voff2), [ws] "s"(ws), [ws2] "s"(ws2), [wb] "i"(WB), [cap] "i"(SPIN_CAP), [pm] "s"(PMASK), [lw] "v"(ldsw), [lo] "i"(L_RED3 - L_RED) : "memory", "vcc", "scc");
}
#define PACK_STORE \
  "s_nop 1\n\t" \
  "v_mov_b32_dpp v53, v52 quad_perm:[1,0,3,2] row_mask:0xf bank_mask:0xf\n\t" \
  "v_cvt_pk_bf16_f32 v54, v52, v53\n\t" \
  "v_min_u32 v54, -2, v54\n\t" \
  "s_nop 1\n\t" \
  "v_mov_b32_dpp v55, v54 row_shl:2 row_mask:0xf bank_mask:0xf\n\t" \
  "v_mov_b32_dpp v56, v54 row_shl:4 row_mask:0xf bank_mask:0xf\n\t" \
  "v_mov_b32_dpp v57, v54 row_shl:6 row_mask:0xf bank_mask:0xf\n\t" \
  "s_mov_b64 exec, %[c8z]\n\t" \
  "global_store_dwordx4 %[so], v[54:57], %[ws] sc1\n\t" \
  "s_mov_b64 exec, -1\n\t" \
  "s_nop 1\n\t"
template <bool XS>
__device__ __forceinline__ void seg_final2(u32 ldsr, u32 xsr, u32 so, const char* ws) {
  constexpr u32 LO = XS ? L_RED3 - L_RED : 0;
  if constexpr (XS) {
    asm volatile(
      "ds_read_b128 v[52:55], %[lr] offset:%c[lo]\n\t"
      "ds_read_b128 v[56:59], %[lr] offset:%c[lo]+16\n\t"
      "ds_read_b32 v60, %[xr]\n\t"
      "s_waitcnt lgkmcnt(1)\n\t"
      "v_pk_add_f32 v[52:53], v[52:53], v[56:57]\n\t"
      "v_pk_add_f32 v[54:55], v[54:55], v[58:59]\n\t"
      "v_pk_add_f32 v[52:53], v[52:53], v[54:55]\n\t"
      "s_waitcnt lgkmcnt(0)\n\t"
      "v_add_f32 v52, v52, v53\n\t"
      "v_add_f32 v52, v52, v60\n\t"
      PACK_STORE
      :: [lr] "v"(ldsr), [xr] "v"(xsr), [so] "v"(so), [ws] "s"(ws), [c8z] "s"(C8Z), [lo] "i"(LO) : "memory");
  } else {
    asm volatile(
      "ds_read_b128 v[52:55], %[lr]\n\t"
      "ds_read_b128 v[56:59], %[lr] offset:16\n\t"
      "s_waitcnt lgkmcnt(0)\n\t"
      "v_pk_add_f32 v[52:53], v[52:53], v[56:57]\n\t"
      "v_pk_add_f32 v[54:55], v[54:55], v[58:59]\n\t"
      "v_pk_add_f32 v[52:53], v[52:53], v[54:55]\n\t"
      "v_add_f32 v52, v52, v53\n\t"
      PACK_STORE
      :: [lr] "v"(ldsr), [so] "v"(so), [ws] "s"(ws), [c8z] "s"(C8Z) : "memory");
  }
}
template <int LOFF>
__device__ __forceinline__ void seg_qpoll(u32 voff, const char* ws, u32 ldsw, u32& spins) {
  asm volatile(
    SNOP "Lp%=:\n\t"
    "global_load_dwordx4 v[52:55], %[voff], %[ws] sc1\n\t"
    "s_add_u32 %[sp], %[sp], 1\n\t"
    "s_waitcnt vmcnt(0)\n\t"
    "v_max3_u32 v56, v52, v53, v54\n\t"
    "v_max_u32 v56, v56, v55\n\t"
    "v_cmp_eq_u32 vcc, -1, v56\n\t"
    "ds_write_b128 %[lw], v[52:55] offset:%c[lo]\n\t"
    "s_cbranch_vccz Lk%=\n\t"
    "s_cmp_lt_u32 %[sp], %[cap]\n\t"
    "s_cbranch_scc1 Lp%=\n\t"
    "Lk%=:\n\t"
    : [sp] "+s"(spins) : [voff] "v"(voff), [ws] "s"(ws), [lw] "v"(ldsw), [cap] "i"(SPIN_CAP), [lo] "i"(LOFF) : "memory", "vcc", "scc");
}
template <int TO>
__device__ __forceinline__ void seg_kpre(u32 ka0, u32 ka1) {
  asm volatile(
    "ds_read_b128 v[48:51], %0 offset:%c2\n\tds_read_b128 v[64:67], %1 offset:%c2\n\t"
    "ds_read_b128 v[52:55], %0 offset:%c2+512\n\tds_read_b128 v[68:71], %1 offset:%c2+512\n\t"
    "ds_read_b128 v[56:59], %0 offset:%c2+1024\n\tds_read_b128 v[72:75], %1 offset:%c2+1024\n\t"
    "ds_read_b128 v[60:63], %0 offset:%c2+1536\n\tds_read_b128 v[76:79], %1 offset:%c2+1536\n\t"
    "v_mov_b32 v96, 0\n\tv_mov_b32 v97, 0\n\tv_mov_b32 v98, 0\n\tv_mov_b32 v99, 0\n\t"
    "v_mov_b32 v100, 0\n\tv_mov_b32 v101, 0\n\tv_mov_b32 v102, 0\n\tv_mov_b32 v103, 0\n\t"
    "v_mov_b32 v104, 0\n\tv_mov_b32 v105, 0\n\tv_mov_b32 v106, 0\n\tv_mov_b32 v107, 0\n\t"
    "v_mov_b32 v108, 0\n\tv_mov_b32 v109, 0\n\tv_mov_b32 v110, 0\n\tv_mov_b32 v111, 0\n\t"
    :: "v"(ka0), "v"(ka1), "i"(TO) : "memory");
}
#define QK(s, k, q, c) "v_mfma_f32_16x16x32_bf16 v[" #s ":" #s "+3], v[" #k ":" #k "+3], v[" #q ":" #q "+3], " c "\n\t"
#define TR(d, a, off) "ds_read_b64_tr_b16 v[" #d ":" #d "+1], %[" #a "] offset:%c[to]+" #off "\n\t"
#define PV(r) "v_mfma_f32_16x16x32_bf16 v[" #r ":" #r "+3], v[124:127], v[" #r ":" #r "+3], 0\n\t"
#define SM(r) "v_fma_f32 v" #r ", v" #r ", v99, v121\n\tv_exp_f32 v" #r ", v" #r "\n\t"
#define PVA(acc, a, v, c) "v_mfma_f32_16x16x32_bf16 v[" #acc ":" #acc "+3], v[" #a ":" #a "+3], v[" #v ":" #v "+3], " c "\n\t"
#define PCP(d, s, ctl, bank) "v_mov_b32_dpp v" #d ", v" #s " " ctl " row_mask:0xf bank_mask:" bank "\n\t"
template <int TO>
__device__ __forceinline__ void seg_attn(u32 qa, u32 sta, u32 va0, u32 va1, u32 va2, u32 va3, u32 oa, u32 mla) {
  asm volatile(
    "ds_read_b128 v[80:83], %[qa]\n\t"
    "ds_read_b128 v[84:87], %[qa] offset:64\n\t"
    "ds_read_b128 v[88:91], %[qa] offset:128\n\t"
    "ds_read_b128 v[92:95], %[qa] offset:192\n\t"
    "ds_read_b64 v[120:121], %[sta]\n\t"
    "s_waitcnt lgkmcnt(4)\n\t"
    QK(112, 48, 80, "0") QK(116, 64, 80, "0")
    "s_waitcnt lgkmcnt(3)\n\t"
    QK(112, 52, 84, "v[112:115]") QK(116, 68, 84, "v[116:119]")
    "s_waitcnt lgkmcnt(2)\n\t"
    QK(112, 56, 88, "v[112:115]") QK(116, 72, 88, "v[116:119]")
    "s_waitcnt lgkmcnt(0)\n\t"
    QK(112, 60, 92, "v[112:115]")
    "v_add_f32_dpp v120, v120, v120 quad_perm:[1,0,3,2] row_mask:0xf bank_mask:0xf\n\t"
    "v_add_f32_dpp v121, v121, v121 quad_perm:[1,0,3,2] row_mask:0xf bank_mask:0xf\n\t"
    QK(116, 76, 92, "v[116:119]")
    TR(48, va0, 0)
    "v_add_f32_dpp v120, v120, v120 quad_perm:[2,3,0,1] row_mask:0xf bank_mask:0xf\n\t"
    "v_add_f32_dpp v121, v121, v121 quad_perm:[2,3,0,1] row_mask:0xf bank_mask:0xf\n\t"
    TR(50, va1, 0) TR(52, va2, 0)
    "v_add_f32_dpp v120, v120, v120 row_half_mirror row_mask:0xf bank_mask:0xf\n\t"
    "v_add_f32_dpp v121, v121, v121 row_half_mirror row_mask:0xf bank_mask:0xf\n\t"
    TR(54, va3, 0)
    "v_mul_f32 v122, 0xbb000000, v120\n\t"
    "v_mul_f32 v123, 0x3b000000, v121\n\t"
    "v_fma_f32 v123, -v122, v122, v123\n\t"
    "v_fmac_f32_dpp v112, v112, v122 row_shl:4 row_mask:0xf bank_mask:0xf bound_ctrl:1\n\t"
    "v_max_f32 v123, 0, v123\n\t"
    "v_fmac_f32_dpp v113, v113, v122 row_shl:4 row_mask:0xf bank_mask:0xf bound_ctrl:1\n\t"
    "v_add_f32 v123, 0x3727c5ac, v123\n\t"
    "v_fmac_f32_dpp v114, v114, v122 row_shl:4 row_mask:0xf bank_mask:0xf bound_ctrl:1\n\t"
    "v_rsq_f32 v123, v123\n\t"
    "v_fmac_f32_dpp v115, v115, v122 row_shl:4 row_mask:0xf bank_mask:0xf bound_ctrl:1\n\t"
    "v_fmac_f32_dpp v116, v116, v122 row_shl:4 row_mask:0xf bank_mask:0xf bound_ctrl:1\n\t"
    "v_max3_f32 v120, v112, v113, v114\n\t"
    "v_fmac_f32_dpp v117, v117, v122 row_shl:4 row_mask:0xf bank_mask:0xf bound_ctrl:1\n\t"
    "v_fmac_f32_dpp v118, v118, v122 row_shl:4 row_mask:0xf bank_mask:0xf bound_ctrl:1\n\t"
    "v_fmac_f32_dpp v119, v119, v122 row_shl:4 row_mask:0xf bank_mask:0xf bound_ctrl:1\n\t"
    "v_max3_f32 v120, v120, v115, v116\n\t"
    "v_mul_f32 v123, %[c2], v123\n\t"
    "v_max3_f32 v120, v120, v117, v118\n\t"
    "v_max_f32 v120, v120, v119\n\t"
    "v_mov_b32 v121, v120\n\t"
    TR(56, va0, 512) TR(58, va1, 512)
    "v_permlane16_swap_b32 v120, v121\n\t"
    "v_max_f32 v120, v120, v121\n\t"
    "v_mov_b32 v121, v120\n\t"
    TR(60, va2, 512) TR(62, va3, 512)
    "v_permlane32_swap_b32 v120, v121\n\t"
    "v_max_f32 v120, v120, v121\n\t"
    "v_mul_f32_e64 v121, -v120, v123\n\t"
    "v_mul_f32 v122, v120, v123\n\t"
    "v_fma_f32 v112, v112, v123, v121\n\t"
    "v_fma_f32 v113, v113, v123, v121\n\t"
    "v_exp_f32 v112, v112\n\t"
    TR(64, va0, 1024)
    "v_fma_f32 v114, v114, v123, v121\n\t"
    "v_exp_f32 v113, v113\n\t"
    TR(66, va1, 1024)
    "v_fma_f32 v115, v115, v123, v121\n\t"
    "v_exp_f32 v114, v114\n\t"
    TR(68, va2, 1024)
    "v_fma_f32 v116, v116, v123, v121\n\t"
    "v_exp_f32 v115, v115\n\t"
    TR(70, va3, 1024)
    "v_fma_f32 v117, v117, v123, v121\n\t"
    "v_exp_f32 v116, v116\n\t"
    "v_cvt_pk_bf16_f32 v124, v112, v113\n\t"
    "v_fma_f32 v118, v118, v123, v121\n\t"
    "v_exp_f32 v117, v117\n\t"
    TR(72, va0, 1536)
    "v_fma_f32 v119, v119, v123, v121\n\t"
    "v_exp_f32 v118, v118\n\t"
    "v_cvt_pk_bf16_f32 v125, v114, v115\n\t"
    "v_exp_f32 v119, v119\n\t"
    TR(74, va1, 1536) TR(76, va2, 1536) TR(78, va3, 1536)
    "v_cvt_pk_bf16_f32 v126, v116, v117\n\t"
    "v_cvt_pk_bf16_f32 v127, v118, v119\n\t"
    PCP(96, 124, "quad_perm:[0,1,2,3]", "0x1") PCP(100, 124, "row_shr:4", "0x2") PCP(104, 124, "quad_perm:[0,1,2,3]", "0x4") PCP(108, 124, "row_shr:4", "0x8")
    PCP(97, 125, "quad_perm:[0,1,2,3]", "0x1") PCP(101, 125, "row_shr:4", "0x2") PCP(105, 125, "quad_perm:[0,1,2,3]", "0x4") PCP(109, 125, "row_shr:4", "0x8")
    PCP(98, 126, "quad_perm:[0,1,2,3]", "0x1") PCP(102, 126, "row_shr:4", "0x2") PCP(106, 126, "quad_perm:[0,1,2,3]", "0x4") PCP(110, 126, "row_shr:4", "0x8")
    PCP(99, 127, "quad_perm:[0,1,2,3]", "0x1") PCP(103, 127, "row_shr:4", "0x2") PCP(107, 127, "quad_perm:[0,1,2,3]", "0x4") PCP(111, 127, "row_shr:4", "0x8")
    "v_mov_b32 v84, %[ones]\n\tv_mov_b32 v85, %[ones]\n\tv_mov_b32 v86, %[ones]\n\tv_mov_b32 v87, %[ones]\n\t"
    "s_waitcnt lgkmcnt(0)\n\t"
    PVA(112, 96, 48, "0") PVA(116, 96, 64, "0")
    PVA(112, 100, 52, "v[112:115]") PVA(116, 100, 68, "v[116:119]")
    PVA(112, 104, 56, "v[112:115]") PVA(116, 104, 72, "v[116:119]")
    PVA(112, 108, 60, "v[112:115]") PVA(116, 108, 76, "v[116:119]")
    "v_mfma_f32_16x16x32_bf16 v[80:83], v[84:87], v[124:127], 0\n\t"
    "s_nop 6\n\t"
    "ds_write_b128 %[oa], v[112:115]\n\t"
    "ds_write_b128 %[oa], v[116:119] offset:1024\n\t"
    "s_mov_b64 exec, 0xf\n\t"
    "ds_write2_b32 %[mla], v122, v80 offset0:0 offset1:16\n\t"
    "s_mov_b64 exec, -1\n\t"
    :: [qa] "v"(qa), [sta] "v"(sta), [va0] "v"(va0), [va1] "v"(va1), [va2] "v"(va2), [va3] "v"(va3), [oa] "v"(oa), [mla] "v"(mla),
       [c2] "s"(C2), [ones] "s"(ONES), [to] "i"(TO) : "memory");
}
__device__ __forceinline__ void seg_merge(u32 mla, u32 oa, u32 so, const char* ws) {
  asm volatile(
    "ds_read_b128 v[48:51], %[mla]\n\t"
    "ds_read_b128 v[60:63], %[mla] offset:64\n\t"
    "ds_read2st64_b32 v[56:57], %[oa] offset0:0 offset1:8\n\t"
    "ds_read2st64_b32 v[58:59], %[oa] offset0:16 offset1:24\n\t"
    "s_waitcnt lgkmcnt(3)\n\t"
    "v_max3_f32 v52, v48, v49, v50\n\t"
    "v_max_f32 v52, v52, v51\n\t"
    "v_sub_f32 v48, v48, v52\n\t"
    "v_sub_f32 v49, v49, v52\n\t"
    "v_exp_f32 v48, v48\n\t"
    "v_sub_f32 v50, v50, v52\n\t"
    "v_exp_f32 v49, v49\n\t"
    "v_sub_f32 v51, v51, v52\n\t"
    "v_exp_f32 v50, v50\n\t"
    "v_exp_f32 v51, v51\n\t"
    "s_waitcnt lgkmcnt(0)\n\t"
    "v_pk_mul_f32 v[60:61], v[60:61], v[48:49]\n\t"
    "v_pk_mul_f32 v[56:57], v[56:57], v[48:49]\n\t"
    "v_pk_fma_f32 v[60:61], v[62:63], v[50:51], v[60:61]\n\t"
    "v_pk_fma_f32 v[56:57], v[58:59], v[50:51], v[56:57]\n\t"
    "v_add_f32 v60, v60, v61\n\t"
    "v_add_f32 v56, v56, v57\n\t"
    "v_rcp_f32 v60, v60\n\t"
    "s_nop 0\n\t"
    "v_mul_f32 v52, v56, v60\n\t"
    PACK_STORE
    :: [mla] "v"(mla), [oa] "v"(oa), [so] "v"(so), [ws] "s"(ws), [c8z] "s"(C8Z) : "memory");
}

__device__ __forceinline__ void dma16(u32 voff, const char* sbase, u32 lds_dst) {
  u32 keep;
  asm volatile(SNOP "s_mov_b32 %0, m0\n\ts_mov_b32 m0, %3\n\ts_nop 0\n\tglobal_load_lds_dwordx4 %1, %2" NTKV "\n\ts_mov_b32 m0, %0"
               : "=&s"(keep) : "v"(voff), "s"(sbase), "s"(lds_dst) : "memory");
}
__device__ __forceinline__ void kv_load(const char* base, int w, int lane, u32 ldsb) {
  asm volatile("" : "+s"(base));
  const int r8 = (lane & 31) >> 2;
#pragma unroll
  for (int i = 0; i < 4; ++i) {
    const u32 ch = 8 * (i & 1) + 4 * (lane >> 5) + ((lane & 3) ^ ((2 * (i >> 1) + (r8 >> 2)) & 3));
    dma16(r8 * 256 + ch * 16, base + (size_t)(16 * w + 8 * (i >> 1)) * 256, ldsb + w * 4096 + i * 1024);
  }
}
__device__ __forceinline__ bool has_sent(v4u x) {
  const u32 m = __builtin_elementwise_max(__builtin_elementwise_max(x[0], x[1]), __builtin_elementwise_max(x[2], x[3]));
  return __builtin_amdgcn_ballot_w64(m == SENT) != 0ull;
}
__device__ __forceinline__ v4u poll128(const char* base, u32 voff) {
  v4u x;
  for (u32 spins = 0;; ++spins) {
    asm volatile("s_nop 4\n\tglobal_load_dwordx4 %0, %1, %2 sc1\n\ts_waitcnt vmcnt(0)" : "=&v"(x) : "v"(voff), "s"(base) : "memory");
    if (!has_sent(x) || spins > SPIN_CAP) break;
  }
  return x;
}
__device__ __forceinline__ v4u pack8(float v) {
  const u32 x = safe(pk(v, dpp<XOR1>(v)));
  return v4u{x, dppu<0x102>(x), dppu<0x104>(x), dppu<0x106>(x)};
}
__device__ __forceinline__ void sum_partials(v4u x, float (&v)[8]) {
#pragma unroll
  for (int m = 0; m < 4; ++m) { v[2 * m] = bflo(x[m]); v[2 * m + 1] = bfhi(x[m]); }
#pragma unroll
  for (int j = 0; j < 8; ++j) v[j] += dpp<XOR1>(v[j]);
#pragma unroll
  for (int j = 0; j < 8; ++j) v[j] += dpp<XOR2>(v[j]);
#pragma unroll
  for (int j = 0; j < 8; ++j) v[j] += dpp<HMIR>(v[j]);
}
__device__ __forceinline__ void x_stats(v4u xa, char* lds, int w, int lane) {
  const bool p = (lane >> 3) & 1; const int kq = lane >> 4;
  const v4f gm = mfma(xa, xa, v4f{0.f, 0.f, 0.f, 0.f});
  const v4f sm = mfma(xa, v4u{ONES, ONES, ONES, ONES}, v4f{0.f, 0.f, 0.f, 0.f});
  float s2 = (gm[0] + gm[1]) + (gm[2] + gm[3]);
  s2 = (p == (bool)(kq >> 1)) ? s2 : 0.f;
  s2 += dpp<XOR1>(s2); s2 += dpp<XOR2>(s2); s2 += dpp<HMIR>(s2); s2 += dpp<ROR8>(s2);
  s2 = x32_add(x16_add(s2));
  float s1 = (sm[0] + sm[1]) + (sm[2] + sm[3]);
  s1 = x32_add(x16_add(s1));
  if (lane == 0) *(v2f*)(lds + L_ST + w * 8) = v2f{s1, s2};
}
}

#define ST(i) do {} while (0)
#define STFLUSH(sv, base) do {} while (0)
#define SGPR_ATTR

__device__ __forceinline__ u32 lb_st() { return L_ST; }
#define OPAQUE_LANE() int lane = lane0; asm volatile("" : "+v"(lane)); const int c8 = lane & 7, kq = lane >> 4; const bool p = (lane >> 3) & 1; (void)c8; (void)p; (void)kq
#define OPAQUE_S(x) asm volatile("" : "+s"(x))

constexpr int FW = 4;

struct Addr {
  u32 vB, vA, vL, lw, so;
  u32 mr, orr;
  u32 r0, r1, r2, r3, r4, r5, r6, r7, r8;
};
#define PIN(x) asm volatile("" : "+v"(x))

template <int TYPE>
__device__ __forceinline__ void layer(const int rep, char* ws, char* lds, int b, int g, int s, int w, const int lane0, v4u xraw, Addr& A, u32 qaT,
                                      const char* nkc, const char* nvc, const char* nwq, const char* cwo, const char* nwo, const bool lead,
                                      const u32 tag, int& stampv, u32 (&cnt)[3]) {
  constexpr int RQ = TYPE ? R_WQB : R_WQA, RO = TYPE ? R_WOB : R_WOA;
  const int L = rep * 2 + TYPE;
  OPAQUE_S(b); OPAQUE_S(g); OPAQUE_S(s); OPAQUE_S(w);
  const u32 grp = (u32)(b * 8 + g), cu = grp * 8 + s;
#define LD_WQ(R, ptr) do { OPAQUE_LANE(); OPAQUE_S(ptr); bulk8<R, 8192>(ptr + (size_t)(64 * w) * 8192 + (512 * g + 64 * s) * 2, (u32)(lane >> 3) * 65536 + c8 * 16); } while (0)
#define LD_WO(R, ptr) do { OPAQUE_LANE(); OPAQUE_S(ptr); bulk8<R, 1024>(ptr + (size_t)(512 * g + 64 * w) * 1024 + 128 * s, (u32)(lane >> 3) * 8192 + c8 * 16); } while (0)
  const char* wsL = ws + L * BOXL;
  const char* box = ws + WS_BOX + cu * BOX + L * BOXL;
  {
    if (rep == 0) {
      if (SCHED == 0) { if (TYPE == 0) VMWAIT(8); else VMWAIT(0); }
      else { if (TYPE == 0) { if (!lead) VMWAIT(4); } else { VMWAIT(4); } }
      transpose8<RQ>();
    }
    if (L > 0) {
      const char* wsP = wsL - BOXL;
      if (DLY_B && rep) __builtin_amdgcn_s_sleep(DLY_B);
      ST(0);
      u32 sp = 0;
      seg_p1<RQ, true, 0>(A.vB, wsP, A.lw, sp);
      cnt[0] += sp;
    } else {
      const v4u xa = (lane0 & 7) == 0 ? xraw : v4u{0u, 0u, 0u, 0u};
      asm volatile("v_mov_b32 v48, %0\n\tv_mov_b32 v49, %1\n\tv_mov_b32 v50, %2\n\tv_mov_b32 v51, %3" :: "v"(xa[0]), "v"(xa[1]), "v"(xa[2]), "v"(xa[3]));
      u32 sp = 0;
      seg_p1<RQ, false, 0>(0, ws, A.lw, sp);
    }
    ST(2);
    if (rep == 0 && TYPE == 1) {
      OPAQUE_LANE();
      if (SCHED == 0) kv_load(nkc, w, lane, L_K + 32768);
      kv_load(nvc, w, lane, L_V + 32768);
    }
    v4u xa;
    asm volatile("v_mov_b32 %0, v48\n\tv_mov_b32 %1, v49\n\tv_mov_b32 %2, v50\n\tv_mov_b32 %3, v51" : "=v"(xa[0]), "=v"(xa[1]), "=v"(xa[2]), "=v"(xa[3]));
    if (rep == 0) {
      asm volatile("v_mov_b32 v48, %0\n\tv_mov_b32 v49, %0\n\tv_mov_b32 v50, %0\n\tv_mov_b32 v51, %0" :: "s"(ONES));
      u32 sp = 0;
      seg_p1<RQ, false, L_REDC - L_RED>(0, ws, A.lw, sp);
    }
    BAR();
    ST(3);
    if (w == FW) {
      seg_final2<false>(A.r2, 0, A.so, box + BOX_Q);
      ST(4);
      OPAQUE_LANE();
      if (rep == 0) {
        const __amdgpu_buffer_rsrc_t rs = __builtin_amdgcn_make_buffer_rsrc(ws, 0, WS_END, 0x00020000);
        const v4f r0 = *(const v4f*)(lds + L_REDC + lane * 32), r1 = *(const v4f*)(lds + L_REDC + lane * 32 + 16);
        const float cs = 0.125f * (((r0[0] + r0[1]) + (r0[2] + r0[3])) + ((r1[0] + r1[1]) + (r1[2] + r1[3])));
        const v4u pc = pack8(cs);
        if (c8 == 0) __builtin_amdgcn_raw_buffer_store_b128(pc, rs, WS_BOX + cu * BOX + BOX_CS + TYPE * 128 + lane * 2, 0, 16);
      }
      x_stats(xa, lds, w, lane);
      if (L == 0) {
        const u64 flag = (u64)tag | ((u64)(~tag) << 32);
        for (u32 spins = 0;; ++spins) {
          const u64 f = __hip_atomic_load((GAS u64*)(ws + WS_RDY + b * 512 + lane * 8), __ATOMIC_RELAXED, __HIP_MEMORY_SCOPE_AGENT);
          if (__all(f == flag) || spins > SPIN_CAP) break;
        }
        if (g == 0 && s == 0 && lane == 0) __hip_atomic_store((GAS u32*)(ws + WS_EPOCH + b * 256), tag, __ATOMIC_RELAXED, __HIP_MEMORY_SCOPE_AGENT);
      }
      if (DLY_C) __builtin_amdgcn_s_sleep(DLY_C);
      u32 sp = 0;
      seg_qpoll<0>(A.r0, wsL, A.r1, sp);
      cnt[1] += sp;
      if (rep == 0) { sp = 0; seg_qpoll<L_CSI + TYPE * 1088 - L_Q>(A.r0, ws + (BOX_CS + TYPE * 128 - BOX_Q), A.r1, sp); }
      ST(5);
    } else {
      OPAQUE_LANE();
      x_stats(xa, lds, w, lane);
    }
    if (g == 0 && w == s) {
      OPAQUE_LANE();
      float v[8];
      sum_partials(xa, v);
      if (c8 == 0) {
        float* xs = (float*)(lds + L_XS) + 16 * kq + (p ? 8 : 0);
        *(v4f*)xs = v4f{v[0], v[1], v[2], v[3]};
        *(v4f*)(xs + 4) = v4f{v[4], v[5], v[6], v[7]};
      }
    }
    if (rep == 0) {
      if (SCHED == 1 && TYPE == 0 && lead) VMWAIT(16); else VMWAIT(0);
      BAR();
      if (SCHED == 0) LD_WO(RO, cwo);
      else {
        if (!lead) LD_WO(RO, cwo);
        else if (TYPE == 0) LD_WO(R_WOB, nwo);
      }
    }
  }
  if (w < 4) {
    seg_kpre<TYPE * 32768>(A.r0, A.r1);
    ST(6);
    BAR();
    ST(7);
    seg_attn<TYPE * 32768>(qaT, A.r6, A.r2, A.r3, A.r4, A.r5, A.r7, A.r8);
    ST(8);
  } else {
    BAR();
  }
  {
    BAR();
    seg_merge(A.mr, A.orr, A.so, box + w * 128);
    if (w == 7) {
      OPAQUE_LANE();
      const __amdgpu_buffer_rsrc_t rs = __builtin_amdgcn_make_buffer_rsrc(ws, 0, WS_END, 0x00020000);
      const int hh = (lane >> 2) & 3, aa = lane & 3;
      const float m1 = ((const float*)(lds + L_ML))[hh * 4 + aa], l1 = ((const float*)(lds + L_ML))[16 + hh * 4 + aa];
      float M1 = __builtin_fmaxf(m1, dpp<XOR1>(m1)); M1 = __builtin_fmaxf(M1, dpp<XOR2>(M1));
      float L1 = __builtin_amdgcn_exp2f(m1 - M1) * l1; L1 += dpp<XOR1>(L1); L1 += dpp<XOR2>(L1);
      const u32 e = safe(__float_as_uint(M1 + __builtin_amdgcn_logf(L1)));
      const v4u ev = {dppu<0x150>(e), dppu<0x154>(e), dppu<0x158>(e), dppu<0x15C>(e)};
      if (lane < 8) __builtin_amdgcn_raw_buffer_store_b128(ev, rs, WS_LSR + ((L * 32 + grp) * 8 + lane) * 128 + s * 16, 0, 16);
    }
    ST(9);
  }
  {
    const char* wsS = ws + L * 32768;
    if (rep == 0) {
      if (SCHED == 1 && TYPE == 0 && lead) VMWAIT(8); else VMWAIT(0);
      transpose8<RO>();
    } else {
      if (DLY_A) __builtin_amdgcn_s_sleep(DLY_A);
    }
    ST(1);
    u32 sp = 0;
    seg_p3<RO>(A.vA, A.vL, wsL, wsS, A.lw, sp);
    cnt[2] += sp;
    ST(10);
    if (rep == 0 && TYPE == 0) {
      if (SCHED == 0) LD_WQ(R_WQB, nwq);
      else {
        if (!lead) LD_WQ(R_WQB, nwq);
        OPAQUE_LANE();
        kv_load(nkc, w, lane, L_K + 32768);
      }
    }
    BAR();
    if (w == FW) seg_final2<true>(A.r2, A.r3, A.so, box + BOX_X);
    ST(11);
  }
  STFLUSH(stampv, L * 12);
}

__global__ void __launch_bounds__(512, 2) __attribute__((amdgpu_num_vgpr(24) SGPR_ATTR))
fused_kernel(const char* __restrict__ xin, const char* kA, const char* vA, const char* wqA, const char* woA,
             const char* kB, const char* vB, const char* wqB, const char* woB, unsigned short* out, char* ws) {
  extern __shared__ __attribute__((aligned(16))) char lds[];
  const int id = blockIdx.x, g = id & 7, b = id >> 6, s = (id >> 3) & 7;
  const int tid = threadIdx.x, lane = tid & 63, w = __builtin_amdgcn_readfirstlane(tid >> 6);
  const int c8 = lane & 7, kq = lane >> 4;
  const bool p = (lane >> 3) & 1;
  int stampv = 0;
  const __amdgpu_buffer_rsrc_t rs = __builtin_amdgcn_make_buffer_rsrc(ws, 0, WS_END, 0x00020000);
  const u32 cu = (u32)((b * 8 + g) * 8 + s);
  asm volatile("" ::: "v127", "v255");
  u32 ep; v4u xraw;
  asm volatile("s_nop 4\n\tglobal_load_dword v56, %0, %1 sc1\n\tglobal_load_dwordx4 v[52:55], %2, %3"
               :: "v"(WS_EPOCH + b * 256), "s"(ws), "v"((u32)((b * 512 + 64 * w + 16 * kq + (p ? 8 : 0)) * 2)), "s"(xin) : "memory");
  if (w == 5) {
    const v4u sv = {SENT, SENT, SENT, SENT};
#pragma unroll
    for (int i = 0; i < 5; ++i) __builtin_amdgcn_raw_buffer_store_b128(sv, rs, WS_BOX + cu * BOX + i * 1024 + lane * 16, 0, 16);
    const u32 o6 = lane < 16 ? WS_BOX + cu * BOX + 5120 + lane * 16
                             : WS_LSR + (u32)(((((lane - 16) >> 3) * 32 + b * 8 + g) * 8 + (lane & 7)) * 128 + s * 16);
    if (lane < 48) __builtin_amdgcn_raw_buffer_store_b128(sv, rs, o6, 0, 16);
  }
  if (w == 6) ((float*)(lds + L_XS))[lane] = 0.f;
  const size_t kvoff = ((size_t)(b * 8 + g) * SK + (SK - WIN) + s * 128) * 256;
  const bool lead = (w >> 1) == b;
  const size_t qoff = (size_t)(64 * w) * 8192 + (512 * g + 64 * s) * 2, ooff = (size_t)(512 * g + 64 * w) * 1024 + 128 * s;
  const u32 vq = (u32)(lane >> 3) * 65536 + c8 * 16, vo = (u32)(lane >> 3) * 8192 + c8 * 16;
  if (SCHED == 0 || lead) {
    bulk8<R_WQA, 8192>(wqA + qoff, vq);
    VMWAIT(8);
  } else {
    VMWAIT(0);
  }
  asm volatile("v_mov_b32 %0, v52\n\tv_mov_b32 %1, v53\n\tv_mov_b32 %2, v54\n\tv_mov_b32 %3, v55\n\tv_mov_b32 %4, v56"
               : "=v"(xraw[0]), "=v"(xraw[1]), "=v"(xraw[2]), "=v"(xraw[3]), "=v"(ep));
  const u32 tag = (u32)__builtin_amdgcn_readfirstlane((int)ep) + 1u;
  if (w == 5 && lane == 0) {
    const u64 flag = (u64)tag | ((u64)(~tag) << 32);
    __hip_atomic_store((GAS u64*)(ws + WS_RDY + b * 512 + (s * 8 + g) * 8), flag, __ATOMIC_RELAXED, __HIP_MEMORY_SCOPE_AGENT);
  }
  if (SCHED == 0) {
    asm volatile("s_barrier" ::: "memory");
    kv_load(kA + kvoff, w, lane, L_K);
    kv_load(vA + kvoff, w, lane, L_V);
  } else {
    kv_load(kA + kvoff, w, lane, L_K);
    if (lead) VMWAIT(4);
    asm volatile("s_barrier" ::: "memory");
    if (!lead) bulk8<R_WQA, 8192>(wqA + qoff, vq);
    kv_load(vA + kvoff, w, lane, L_V);
    if (lead) { bulk8<R_WOA, 1024>(woA + ooff, vo); bulk8<R_WQB, 8192>(wqB + qoff, vq); }
  }
  Addr A; u32 qa0, qa1;
  {
    const u32 grp = (u32)(b * 8 + g);
    const u32 oct = kq * 32 + (p ? 16 : 0);
    A.vB = WS_BOX + (u32)((b * 8 + c8) * 8 + w) * BOX + BOX_X + oct;
    A.vA = WS_BOX + (grp * 8 + c8) * BOX + w * 128 + oct;
    A.vL = WS_LSR + (grp * 8 + s) * 128 + c8 * 16 + (w >> 1) * 4;
    A.lw = L_RED + (8 * c8 + (lane >> 3)) * 32 + w * 4;
    A.so = lane * 2;
    A.mr = L_ML + (w >> 1) * 16;
    A.orr = L_OM + (w & 1) * 1024 + lane * 16 + (w >> 1) * 4;
    const int r16 = lane & 15, q4 = r16 >> 2, p2 = lane & 3;
    qa0 = L_Q + (lane & 3) * 272 + kq * 16;
    qa1 = qa0;
    if (lane & 4) { qa0 += L_CSI - L_Q; qa1 += L_CSI + 1088 - L_Q; }
    if (w < 4) {
      const u32 kb = L_K + 2048 * (4 * w + (r16 >> 2)) + 64 * (r16 & 3);
      A.r0 = kb + 16 * (kq ^ ((2 * (r16 >> 2)) & 3));
      A.r1 = kb + 256 + 16 * (kq ^ ((2 * (r16 >> 2) + 1) & 3));
      const u32 vb = L_V + 2048 * (4 * w + kq) + 64 * q4 + 8 * (p2 & 1);
      A.r2 = vb + 16 * (((p2 >> 1)) ^ ((2 * kq) & 3));
      A.r3 = vb + 256 + 16 * (((p2 >> 1)) ^ ((2 * kq + 1) & 3));
      A.r4 = vb + 16 * ((2 + (p2 >> 1)) ^ ((2 * kq) & 3));
      A.r5 = vb + 256 + 16 * ((2 + (p2 >> 1)) ^ ((2 * kq + 1) & 3));
      A.r7 = L_OM + w * 2048 + lane * 16;
      A.r8 = L_ML + (lane * 4 + w) * 4;
    } else {
      A.r0 = WS_BOX + (grp * 8 + (lane >> 3)) * BOX + BOX_Q + c8 * 16;
      A.r1 = L_Q + kq * 272 + (lane & 15) * 16;
      A.r2 = L_RED + lane * 32;
      A.r3 = L_XS + lane * 4;
      A.r4 = A.r5 = A.r7 = A.r8 = 0;
    }
    A.r6 = L_ST + c8 * 8;
    PIN(A.r6);
    PIN(A.vB); PIN(A.vA); PIN(A.vL); PIN(A.lw); PIN(A.so); PIN(A.mr); PIN(A.orr); PIN(A.r0); PIN(A.r1); PIN(A.r2); PIN(A.r3); PIN(A.r4); PIN(A.r5); PIN(A.r7); PIN(A.r8);
    PIN(qa0); PIN(qa1);
  }
  u32 cnt[3] = {0, 0, 0};
  for (int rep = 0; rep < 2; ++rep) {
    layer<0>(rep, ws, lds, b, g, s, w, lane, xraw, A, qa0, kB + kvoff, nullptr, wqB, woA, woB, lead, tag, stampv, cnt);
    layer<1>(rep, ws, lds, b, g, s, w, lane, xraw, A, qa1, kB + kvoff, vB + kvoff, nullptr, woB, nullptr, lead, tag, stampv, cnt);
  }
  if (g == 0 && w == s) {
    const u32 voff = WS_BOX + (u32)((b * 8 + c8) * 8 + w) * BOX + 3u * BOXL + BOX_X + kq * 32 + (p ? 16 : 0);
    if (DLY_B) __builtin_amdgcn_s_sleep(DLY_B);
    const v4u x = poll128(ws, voff);
    float v[8];
    sum_partials(x, v);
    if (c8 == 0) *(v4u*)(out + b * 512 + 64 * w + 16 * kq + (p ? 8 : 0)) = v4u{pk(v[0], v[1]), pk(v[2], v[3]), pk(v[4], v[5]), pk(v[6], v[7])};
  }
}

extern "C" void kernel_launch(void* const* d_in, const int* in_sizes, int n_in, void* d_out, int out_size, void* d_ws, size_t ws_size,
                              hipStream_t stream) {
  (void)hipFuncSetAttribute((const void*)fused_kernel, hipFuncAttributeMaxDynamicSharedMemorySize, L_END);
  fused_kernel<<<256, 512, L_END, stream>>>((const char*)d_in[0], (const char*)d_in[1], (const char*)d_in[2], (const char*)d_in[3], (const char*)d_in[4],
                                            (const char*)d_in[6], (const char*)d_in[7], (const char*)d_in[8], (const char*)d_in[9],
                                            (unsigned short*)d_out, (char*)d_ws);
}
```

```cpp
#include <hip/hip_runtime.h>
#include <stdint.h>

typedef unsigned int u32;
typedef unsigned long long u64;
typedef u32 v4u __attribute__((ext_vector_type(4)));
typedef u32 v2u __attribute__((ext_vector_type(2)));
typedef float v4f __attribute__((ext_vector_type(4)));
typedef float v2f __attribute__((ext_vector_type(2)));
typedef __bf16 v8b __attribute__((ext_vector_type(8)));
typedef __bf16 v2b __attribute__((ext_vector_type(2)));
#define GAS __attribute__((address_space(1)))

#define DLY_C 2
#define DLY_A 8
#define DLY_B 10
#define SCHED 0
#define NTKV " nt"
#define NTW " nt"

namespace {
constexpr int SK = 4096, WIN = 1024;
constexpr u32 WS_EPOCH = 0;
constexpr u32 WS_RDY = 4096;
constexpr u32 WS_LSR = 8192;
constexpr u32 WS_BOX = WS_LSR + 131072;
constexpr u32 BOXL = 1280, BOX_Q = 1024, BOX_X = 1152, BOX_CS = 5120, BOX = 5376;
constexpr u32 WS_END = WS_BOX + 256 * BOX;
constexpr u32 WS_DBG = 4u << 20;
constexpr u32 L_KV = 0;
constexpr u32 L_K = 0, L_V = 65536;
constexpr u32 L_OM = 131072;
constexpr u32 L_ML = L_OM + 8192;
constexpr u32 L_Q = L_ML + 128;
constexpr u32 L_CSI = L_Q + 1088;
constexpr u32 L_RED = L_CSI + 2176;
constexpr u32 L_REDC = L_RED + 2048;
constexpr u32 L_ST = L_REDC + 2048;
constexpr u32 L_RED3 = L_ST + 64;
constexpr u32 L_XS = L_RED3 + 2048;
constexpr u32 L_END = L_XS + 256;
static_assert(L_END <= 160 * 1024, "lds");
static_assert(L_Q % 16 == 0 && L_CSI % 16 == 0 && L_RED % 16 == 0 && L_ST % 16 == 0 && L_RED3 % 16 == 0 && L_XS % 16 == 0, "align");
constexpr u32 SENT = 0xFFFFFFFFu;
constexpr u32 SPIN_CAP = 1u << 15;
constexpr float C2 = 0.08838834764831845f * 1.4426950408889634f;
constexpr u32 ONES = 0x3F803F80u;
constexpr u64 PMASK = 0xFF00FF00FF00FF00ull;
constexpr u64 C8Z = 0x0101010101010101ull;

__device__ __forceinline__ float bflo(u32 w) { return __uint_as_float(w << 16); }
__device__ __forceinline__ float bfhi(u32 w) { return __uint_as_float(w & 0xffff0000u); }
__device__ __forceinline__ u32 pk(float lo, float hi) { v2f f = {lo, hi}; return __builtin_bit_cast(u32, __builtin_convertvector(f, v2b)); }
__device__ __forceinline__ u32 safe(u32 v) { return v < 0xFFFFFFFEu ? v : 0xFFFFFFFEu; }
template <int CTRL> __device__ __forceinline__ float dpp(float x) {
  return __builtin_bit_cast(float, __builtin_amdgcn_mov_dpp(__builtin_bit_cast(int, x), CTRL, 0xf, 0xf, true));
}
template <int CTRL> __device__ __forceinline__ u32 dppu(u32 x) { return (u32)__builtin_amdgcn_mov_dpp((int)x, CTRL, 0xf, 0xf, true); }
constexpr int XOR1 = 0xB1, XOR2 = 0x4E, HMIR = 0x141, ROR8 = 0x128;
__device__ __forceinline__ float x16_add(float x) { auto s = __builtin_amdgcn_permlane16_swap(__float_as_uint(x), __float_as_uint(x), false, false); return __uint_as_float(s[0]) + __uint_as_float(s[1]); }
__device__ __forceinline__ float x32_add(float x) { auto s = __builtin_amdgcn_permlane32_swap(__float_as_uint(x), __float_as_uint(x), false, false); return __uint_as_float(s[0]) + __uint_as_float(s[1]); }
__device__ __forceinline__ v4f mfma(v4u a, v4u b, v4f c) {
  return __builtin_amdgcn_mfma_f32_16x16x32_bf16(__builtin_bit_cast(v8b, a), __builtin_bit_cast(v8b, b), c, 0, 0, 0);
}
#define BAR() asm volatile("s_waitcnt lgkmcnt(0)\n\ts_barrier" ::: "memory")
#define VMWAIT(n) asm volatile("s_waitcnt vmcnt(" #n ")" ::: "memory")

constexpr int R_WQA = 128, R_WOA = 160, R_WQB = 192, R_WOB = 224;
template <int BASE, u32 STRIDE>
__device__ __forceinline__ void bulk8(const char* base, u32 voff) {
  asm volatile("" : "+s"(base));
  const __amdgpu_buffer_rsrc_t r = __builtin_amdgcn_make_buffer_rsrc((void*)base, 0, 0x7fffffff, 0x00020000);
  asm volatile("s_nop 4\n\t"
               "buffer_load_dwordx4 v[%c10:%c11], %0, %1, 0 offen" NTW "\n\tbuffer_load_dwordx4 v[%c12:%c13], %0, %1, %3 offen" NTW "\n\t"
               "buffer_load_dwordx4 v[%c14:%c15], %0, %1, %4 offen" NTW "\n\tbuffer_load_dwordx4 v[%c16:%c17], %0, %1, %5 offen" NTW "\n\t"
               "buffer_load_dwordx4 v[%c18:%c19], %0, %1, %6 offen" NTW "\n\tbuffer_load_dwordx4 v[%c20:%c21], %0, %1, %7 offen" NTW "\n\t"
               "buffer_load_dwordx4 v[%c22:%c23], %0, %1, %8 offen" NTW "\n\tbuffer_load_dwordx4 v[%c24:%c25], %0, %1, %9 offen" NTW
               :: "v"(voff), "s"(r), "s"(0), "s"(STRIDE), "s"(2 * STRIDE), "s"(3 * STRIDE), "s"(4 * STRIDE), "s"(5 * STRIDE), "s"(6 * STRIDE), "s"(7 * STRIDE),
                  "i"(BASE), "i"(BASE + 3), "i"(BASE + 4), "i"(BASE + 7), "i"(BASE + 8), "i"(BASE + 11), "i"(BASE + 12), "i"(BASE + 15),
                  "i"(BASE + 16), "i"(BASE + 19), "i"(BASE + 20), "i"(BASE + 23), "i"(BASE + 24), "i"(BASE + 27), "i"(BASE + 28), "i"(BASE + 31)
               : "memory");
}
template <int B, int M, int R>
__device__ __forceinline__ void tp_pair(u32 slo, u32 shi) {
  if constexpr (M == R) {
    asm volatile("v_perm_b32 v48, v%c3, v%c2, %0\n\tv_perm_b32 v%c3, v%c3, v%c2, %1\n\tv_mov_b32 v%c2, v48"
                 :: "s"(slo), "s"(shi), "i"(B + 8 * M + R), "i"(B + 8 * M + 4 + R));
  } else {
    asm volatile("v_perm_b32 v48, v%c3, v%c2, %0\n\tv_perm_b32 v49, v%c3, v%c2, %1\n\t"
                 "v_perm_b32 v%c2, v%c5, v%c4, %0\n\tv_perm_b32 v%c3, v%c5, v%c4, %1\n\t"
                 "v_mov_b32 v%c4, v48\n\tv_mov_b32 v%c5, v49"
                 :: "s"(slo), "s"(shi), "i"(B + 8 * M + R), "i"(B + 8 * M + 4 + R), "i"(B + 8 * R + M), "i"(B + 8 * R + 4 + M));
  }
}
template <int B>
__device__ __forceinline__ void transpose8() {
  const u32 slo = 0x05040100u, shi = 0x07060302u;
  tp_pair<B, 0, 0>(slo, shi); tp_pair<B, 1, 1>(slo, shi); tp_pair<B, 2, 2>(slo, shi); tp_pair<B, 3, 3>(slo, shi);
  tp_pair<B, 0, 1>(slo, shi); tp_pair<B, 0, 2>(slo, shi); tp_pair<B, 0, 3>(slo, shi);
  tp_pair<B, 1, 2>(slo, shi); tp_pair<B, 1, 3>(slo, shi); tp_pair<B, 2, 3>(slo, shi);
  asm volatile("s_nop 1");
}
#define SNOP
#define MF(d, b) "v_mfma_f32_16x16x32_bf16 v[" #d ":" #d "+3], v[48:51], v[%c[wb]+" #b ":%c[wb]+" #b "+3], 0\n\t"
#define FOLD_TAIL \
  "v_permlane32_swap_b32 v52, v68\n\t" \
  "v_permlane32_swap_b32 v56, v72\n\t" \
  "v_permlane32_swap_b32 v60, v76\n\t" \
  "v_permlane32_swap_b32 v64, v80\n\t" \
  "v_cndmask_b32_e64 v52, v52, v68, %[pm]\n\t" \
  "v_cndmask_b32_e64 v60, v60, v76, %[pm]\n\t" \
  "v_cndmask_b32_e64 v56, v56, v72, %[pm]\n\t" \
  "v_cndmask_b32_e64 v64, v64, v80, %[pm]\n\t" \
  "v_permlane16_swap_b32 v52, v60\n\t" \
  "s_nop 0\n\t" \
  "v_permlane16_swap_b32 v56, v64\n\t" \
  "v_add_f32 v52, v52, v60\n\t" \
  "v_add_f32 v56, v56, v64\n\t" \
  "s_nop 0\n\t" \
  "v_add_f32_dpp v52, v52, v52 row_ror:8 row_mask:0xf bank_mask:0xf\n\t" \
  "v_add_f32_dpp v56, v56, v56 row_ror:8 row_mask:0xf bank_mask:0xf\n\t" \
  "v_cndmask_b32_e64 v52, v52, v56, %[pm]\n\t"

template <int WB, bool POLL, int LOFF>
__device__ __forceinline__ void seg_p1(u32 voff, const char* ws, u32 ldsw, u32& spins) {
  if constexpr (POLL) {
    asm volatile(
      SNOP "Lp%=:\n\t"
      "global_load_dwordx4 v[48:51], %[voff], %[ws] sc1\n\t"
      "s_add_u32 %[sp], %[sp], 1\n\t"
      "s_waitcnt vmcnt(0)\n\t"
      MF(52, 0) "v_max3_u32 v84, v48, v49, v50\n\t"
      MF(56, 4) "v_max_u32 v84, v84, v51\n\t"
      MF(60, 8) "v_cmp_eq_u32 vcc, -1, v84\n\t"
      MF(64, 12)
      "s_cbranch_vccz Lk%=\n\t"
      "s_cmp_lt_u32 %[sp], %[cap]\n\t"
      "s_cbranch_scc1 Lp%=\n\t"
      "Lk%=:\n\t"
      : [sp] "+s"(spins) : [voff] "v"(voff), [ws] "s"(ws), [wb] "i"(WB), [cap] "i"(SPIN_CAP) : "memory", "vcc", "scc");
  } else {
    asm volatile("s_nop 1\n\t" MF(52, 0) "s_nop 0\n\t" MF(56, 4) "s_nop 0\n\t" MF(60, 8) "s_nop 0\n\t" MF(64, 12) "s_nop 0\n\t" :: [wb] "i"(WB));
  }
  asm volatile(
    MF(68, 16) "v_pk_add_f32 v[52:53], v[52:53], v[54:55]\n\t"
    MF(72, 20) "v_pk_add_f32 v[56:57], v[56:57], v[58:59]\n\t"
    MF(76, 24) "v_pk_add_f32 v[60:61], v[60:61], v[62:63]\n\t"
    MF(80, 28) "v_pk_add_f32 v[64:65], v[64:65], v[66:67]\n\t"
    "v_add_f32 v52, v52, v53\n\t"
    "v_pk_add_f32 v[68:69], v[68:69], v[70:71]\n\t"
    "v_add_f32 v56, v56, v57\n\t"
    "v_pk_add_f32 v[72:73], v[72:73], v[74:75]\n\t"
    "v_add_f32 v60, v60, v61\n\t"
    "v_pk_add_f32 v[76:77], v[76:77], v[78:79]\n\t"
    "v_add_f32 v64, v64, v65\n\t"
    "v_pk_add_f32 v[80:81], v[80:81], v[82:83]\n\t"
    "v_add_f32 v68, v68, v69\n\t"
    "v_add_f32 v72, v72, v73\n\t"
    "v_add_f32 v76, v76, v77\n\t"
    "v_add_f32 v80, v80, v81\n\t"
    "s_nop 0\n\t"
    FOLD_TAIL
    "ds_write_b32 %[lw], v52 offset:%c[lo]\n\t"
    :: [wb] "i"(WB), [pm] "s"(PMASK), [lw] "v"(ldsw), [lo] "i"(LOFF) : "memory");
}
template <int WB>
__device__ __forceinline__ void seg_p3(u32 voff, u32 voff2, const char* ws, const char* ws2, u32 ldsw, u32& spins) {
  asm volatile(
    SNOP "Lp%=:\n\t"
    "global_load_dwordx4 v[48:51], %[voff], %[ws] sc1\n\t"
    "global_load_dword v84, %[voff2], %[ws2] sc1\n\t"
    "s_add_u32 %[sp], %[sp], 1\n\t"
    "s_waitcnt vmcnt(0)\n\t"
    MF(52, 0) "v_max3_u32 v85, v48, v49, v50\n\t"
    MF(56, 4) "v_max3_u32 v85, v85, v51, v84\n\t"
    MF(60, 8) "v_cmp_eq_u32 vcc, -1, v85\n\t"
    MF(64, 12)
    "s_cbranch_vccz Lk%=\n\t"
    "s_cmp_lt_u32 %[sp], %[cap]\n\t"
    "s_cbranch_scc1 Lp%=\n\t"
    "Lk%=:\n\t"
    "v_max_f32_dpp v85, v84, v84 quad_perm:[1,0,3,2] row_mask:0xf bank_mask:0xf\n\t"
    MF(68, 16)
    "s_nop 0\n\t"
    "v_max_f32_dpp v85, v85, v85 quad_perm:[2,3,0,1] row_mask:0xf bank_mask:0xf\n\t"
    MF(72, 20)
    "s_nop 0\n\t"
    "v_max_f32_dpp v85, v85, v85 row_half_mirror row_mask:0xf bank_mask:0xf\n\t"
    MF(76, 24)
    "v_sub_f32 v86, v84, v85\n\t"
    MF(80, 28)
    "v_exp_f32 v86, v86\n\t"
    "s_nop 1\n\t"
    "v_mov_b32_dpp v90, v86 row_newbcast:0 row_mask:0x5 bank_mask:0xf\n\t"
    "v_mov_b32_dpp v91, v86 row_newbcast:1 row_mask:0x5 bank_mask:0xf\n\t"
    "v_mov_b32_dpp v92, v86 row_newbcast:2 row_mask:0x5 bank_mask:0xf\n\t"
    "v_mov_b32_dpp v93, v86 row_newbcast:3 row_mask:0x5 bank_mask:0xf\n\t"
    "v_mov_b32_dpp v90, v86 row_newbcast:4 row_mask:0xa bank_mask:0xf\n\t"
    "v_mov_b32_dpp v91, v86 row_newbcast:5 row_mask:0xa bank_mask:0xf\n\t"
    "v_mov_b32_dpp v92, v86 row_newbcast:6 row_mask:0xa bank_mask:0xf\n\t"
    "v_mov_b32_dpp v93, v86 row_newbcast:7 row_mask:0xa bank_mask:0xf\n\t"
    "v_add_f32_dpp v87, v86, v86 quad_perm:[1,0,3,2] row_mask:0xf bank_mask:0xf\n\t"
    "v_pk_mul_f32 v[52:53], v[52:53], v[90:91]\n\t"
    "v_pk_mul_f32 v[56:57], v[56:57], v[90:91]\n\t"
    "v_add_f32_dpp v87, v87, v87 quad_perm:[2,3,0,1] row_mask:0xf bank_mask:0xf\n\t"
    "v_pk_mul_f32 v[60:61], v[60:61], v[90:91]\n\t"
    "v_pk_mul_f32 v[64:65], v[64:65], v[90:91]\n\t"
    "v_add_f32_dpp v87, v87, v87 row_half_mirror row_mask:0xf bank_mask:0xf\n\t"
    "v_pk_mul_f32 v[68:69], v[68:69], v[90:91]\n\t"
    "v_pk_mul_f32 v[72:73], v[72:73], v[90:91]\n\t"
    "v_rcp_f32 v87, v87\n\t"
    "v_pk_mul_f32 v[76:77], v[76:77], v[90:91]\n\t"
    "v_pk_mul_f32 v[80:81], v[80:81], v[90:91]\n\t"
    "v_pk_fma_f32 v[52:53], v[54:55], v[92:93], v[52:53]\n\t"
    "v_pk_fma_f32 v[68:69], v[70:71], v[92:93], v[68:69]\n\t"
    "v_pk_fma_f32 v[56:57], v[58:59], v[92:93], v[56:57]\n\t"
    "v_pk_fma_f32 v[72:73], v[74:75], v[92:93], v[72:73]\n\t"
    "v_pk_fma_f32 v[60:61], v[62:63], v[92:93], v[60:61]\n\t"
    "v_pk_fma_f32 v[76:77], v[78:79], v[92:93], v[76:77]\n\t"
    "v_pk_fma_f32 v[64:65], v[66:67], v[92:93], v[64:65]\n\t"
    "v_pk_fma_f32 v[80:81], v[82:83], v[92:93], v[80:81]\n\t"
    "v_add_f32 v52, v52, v53\n\t"
    "v_add_f32 v68, v68, v69\n\t"
    "v_add_f32 v56, v56, v57\n\t"
    "v_add_f32 v72, v72, v73\n\t"
    "v_add_f32 v60, v60, v61\n\t"
    "v_add_f32 v76, v76, v77\n\t"
    "v_add_f32 v64, v64, v65\n\t"
    "v_add_f32 v80, v80, v81\n\t"
    "s_nop 1\n\t"
    FOLD_TAIL
    "v_mul_f32 v52, v52, v87\n\t"
    "ds_write_b32 %[lw], v52 offset:%c[lo]\n\t"
    : [sp] "+s"(spins)
    : [voff] "v"(voff), [voff2] "v"(voff2), [ws] "s"(ws), [ws2] "s"(ws2), [wb] "i"(WB), [cap] "i"(SPIN_CAP), [pm] "s"(PMASK), [lw] "v"(ldsw), [lo] "i"(L_RED3 - L_RED) : "memory", "vcc", "scc");
}
#define PACK_STORE \
  "s_nop 1\n\t" \
  "v_mov_b32_dpp v53, v52 quad_perm:[1,0,3,2] row_mask:0xf bank_mask:0xf\n\t" \
  "v_cvt_pk_bf16_f32 v54, v52, v53\n\t" \
  "v_min_u32 v54, -2, v54\n\t" \
  "s_nop 1\n\t" \
  "v_mov_b32_dpp v55, v54 row_shl:2 row_mask:0xf bank_mask:0xf\n\t" \
  "v_mov_b32_dpp v56, v54 row_shl:4 row_mask:0xf bank_mask:0xf\n\t" \
  "v_mov_b32_dpp v57, v54 row_shl:6 row_mask:0xf bank_mask:0xf\n\t" \
  "s_mov_b64 exec, %[c8z]\n\t" \
  "global_store_dwordx4 %[so], v[54:57], %[ws] sc1\n\t" \
  "s_mov_b64 exec, -1\n\t" \
  "s_nop 1\n\t"
template <bool XS>
__device__ __forceinline__ void seg_final2(u32 ldsr, u32 xsr, u32 so, const char* ws) {
  constexpr u32 LO = XS ? L_RED3 - L_RED : 0;
  if constexpr (XS) {
    asm volatile(
      "ds_read_b128 v[52:55], %[lr] offset:%c[lo]\n\t"
      "ds_read_b128 v[56:59], %[lr] offset:%c[lo]+16\n\t"
      "ds_read_b32 v60, %[xr]\n\t"
      "s_waitcnt lgkmcnt(1)\n\t"
      "v_pk_add_f32 v[52:53], v[52:53], v[56:57]\n\t"
      "v_pk_add_f32 v[54:55], v[54:55], v[58:59]\n\t"
      "v_pk_add_f32 v[52:53], v[52:53], v[54:55]\n\t"
      "s_waitcnt lgkmcnt(0)\n\t"
      "v_add_f32 v52, v52, v53\n\t"
      "v_add_f32 v52, v52, v60\n\t"
      PACK_STORE
      :: [lr] "v"(ldsr), [xr] "v"(xsr), [so] "v"(so), [ws] "s"(ws), [c8z] "s"(C8Z), [lo] "i"(LO) : "memory");
  } else {
    asm volatile(
      "ds_read_b128 v[52:55], %[lr]\n\t"
      "ds_read_b128 v[56:59], %[lr] offset:16\n\t"
      "s_waitcnt lgkmcnt(0)\n\t"
      "v_pk_add_f32 v[52:53], v[52:53], v[56:57]\n\t"
      "v_pk_add_f32 v[54:55], v[54:55], v[58:59]\n\t"
      "v_pk_add_f32 v[52:53], v[52:53], v[54:55]\n\t"
      "v_add_f32 v52, v52, v53\n\t"
      PACK_STORE
      :: [lr] "v"(ldsr), [so] "v"(so), [ws] "s"(ws), [c8z] "s"(C8Z) : "memory");
  }
}
template <int LOFF>
__device__ __forceinline__ void seg_qpoll(u32 voff, const char* ws, u32 ldsw, u32& spins) {
  asm volatile(
    SNOP "Lp%=:\n\t"
    "global_load_dwordx4 v[52:55], %[voff], %[ws] sc1\n\t"
    "s_add_u32 %[sp], %[sp], 1\n\t"
    "s_waitcnt vmcnt(0)\n\t"
    "v_max3_u32 v56, v52, v53, v54\n\t"
    "v_max_u32 v56, v56, v55\n\t"
    "v_cmp_eq_u32 vcc, -1, v56\n\t"
    "ds_write_b128 %[lw], v[52:55] offset:%c[lo]\n\t"
    "s_cbranch_vccz Lk%=\n\t"
    "s_cmp_lt_u32 %[sp], %[cap]\n\t"
    "s_cbranch_scc1 Lp%=\n\t"
    "Lk%=:\n\t"
    : [sp] "+s"(spins) : [voff] "v"(voff), [ws] "s"(ws), [lw] "v"(ldsw), [cap] "i"(SPIN_CAP), [lo] "i"(LOFF) : "memory", "vcc", "scc");
}
template <int TO>
__device__ __forceinline__ void seg_kpre(u32 ka0, u32 ka1) {
  asm volatile(
    "ds_read_b128 v[48:51], %0 offset:%c2\n\tds_read_b128 v[64:67], %1 offset:%c2\n\t"
    "ds_read_b128 v[52:55], %0 offset:%c2+512\n\tds_read_b128 v[68:71], %1 offset:%c2+512\n\t"
    "ds_read_b128 v[56:59], %0 offset:%c2+1024\n\tds_read_b128 v[72:75], %1 offset:%c2+1024\n\t"
    "ds_read_b128 v[60:63], %0 offset:%c2+1536\n\tds_read_b128 v[76:79], %1 offset:%c2+1536\n\t"
    "v_mov_b32 v96, 0\n\tv_mov_b32 v97, 0\n\tv_mov_b32 v98, 0\n\tv_mov_b32 v99, 0\n\t"
    "v_mov_b32 v100, 0\n\tv_mov_b32 v101, 0\n\tv_mov_b32 v102, 0\n\tv_mov_b32 v103, 0\n\t"
    "v_mov_b32 v104, 0\n\tv_mov_b32 v105, 0\n\tv_mov_b32 v106, 0\n\tv_mov_b32 v107, 0\n\t"
    "v_mov_b32 v108, 0\n\tv_mov_b32 v109, 0\n\tv_mov_b32 v110, 0\n\tv_mov_b32 v111, 0\n\t"
    :: "v"(ka0), "v"(ka1), "i"(TO) : "memory");
}
#define QK(s, k, q, c) "v_mfma_f32_16x16x32_bf16 v[" #s ":" #s "+3], v[" #k ":" #k "+3], v[" #q ":" #q "+3], " c "\n\t"
#define TR(d, a, off) "ds_read_b64_tr_b16 v[" #d ":" #d "+1], %[" #a "] offset:%c[to]+" #off "\n\t"
#define PV(r) "v_mfma_f32_16x16x32_bf16 v[" #r ":" #r "+3], v[124:127], v[" #r ":" #r "+3], 0\n\t"
#define SM(r) "v_fma_f32 v" #r ", v" #r ", v99, v121\n\tv_exp_f32 v" #r ", v" #r "\n\t"
#define PVA(acc, a, v, c) "v_mfma_f32_16x16x32_bf16 v[" #acc ":" #acc "+3], v[" #a ":" #a "+3], v[" #v ":" #v "+3], " c "\n\t"
#define PCP(d, s, ctl, bank) "v_mov_b32_dpp v" #d ", v" #s " " ctl " row_mask:0xf bank_mask:" bank "\n\t"
template <int TO>
__device__ __forceinline__ void seg_attn(u32 qa, u32 sta, u32 va0, u32 va1, u32 va2, u32 va3, u32 oa, u32 mla) {
  asm volatile(
    "ds_read_b128 v[80:83], %[qa]\n\t"
    "ds_read_b128 v[84:87], %[qa] offset:64\n\t"
    "ds_read_b128 v[88:91], %[qa] offset:128\n\t"
    "ds_read_b128 v[92:95], %[qa] offset:192\n\t"
    "ds_read_b64 v[120:121], %[sta]\n\t"
    "s_waitcnt lgkmcnt(4)\n\t"
    QK(112, 48, 80, "0") QK(116, 64, 80, "0")
    "s_waitcnt lgkmcnt(3)\n\t"
    QK(112, 52, 84, "v[112:115]") QK(116, 68, 84, "v[116:119]")
    "s_waitcnt lgkmcnt(2)\n\t"
    QK(112, 56, 88, "v[112:115]") QK(116, 72, 88, "v[116:119]")
    "s_waitcnt lgkmcnt(0)\n\t"
    QK(112, 60, 92, "v[112:115]")
    "v_add_f32_dpp v120, v120, v120 quad_perm:[1,0,3,2] row_mask:0xf bank_mask:0xf\n\t"
    "v_add_f32_dpp v121, v121, v121 quad_perm:[1,0,3,2] row_mask:0xf bank_mask:0xf\n\t"
    QK(116, 76, 92, "v[116:119]")
    TR(48, va0, 0)
    "v_add_f32_dpp v120, v120, v120 quad_perm:[2,3,0,1] row_mask:0xf bank_mask:0xf\n\t"
    "v_add_f32_dpp v121, v121, v121 quad_perm:[2,3,0,1] row_mask:0xf bank_mask:0xf\n\t"
    TR(50, va1, 0) TR(52, va2, 0)
    "v_add_f32_dpp v120, v120, v120 row_half_mirror row_mask:0xf bank_mask:0xf\n\t"
    "v_add_f32_dpp v121, v121, v121 row_half_mirror row_mask:0xf bank_mask:0xf\n\t"
    TR(54, va3, 0)
    "v_mul_f32 v122, 0xbb000000, v120\n\t"
    "v_mul_f32 v123, 0x3b000000, v121\n\t"
    "v_fma_f32 v123, -v122, v122, v123\n\t"
    "v_fmac_f32_dpp v112, v112, v122 row_shl:4 row_mask:0xf bank_mask:0xf bound_ctrl:1\n\t"
    "v_max_f32 v123, 0, v123\n\t"
    "v_fmac_f32_dpp v113, v113, v122 row_shl:4 row_mask:0xf bank_mask:0xf bound_ctrl:1\n\t"
    "v_add_f32 v123, 0x3727c5ac, v123\n\t"
    "v_fmac_f32_dpp v114, v114, v122 row_shl:4 row_mask:0xf bank_mask:0xf bound_ctrl:1\n\t"
    "v_rsq_f32 v123, v123\n\t"
    "v_fmac_f32_dpp v115, v115, v122 row_shl:4 row_mask:0xf bank_mask:0xf bound_ctrl:1\n\t"
    "v_fmac_f32_dpp v116, v116, v122 row_shl:4 row_mask:0xf bank_mask:0xf bound_ctrl:1\n\t"
    "v_max3_f32 v120, v112, v113, v114\n\t"
    "v_fmac_f32_dpp v117, v117, v122 row_shl:4 row_mask:0xf bank_mask:0xf bound_ctrl:1\n\t"
    "v_fmac_f32_dpp v118, v118, v122 row_shl:4 row_mask:0xf bank_mask:0xf bound_ctrl:1\n\t"
    "v_fmac_f32_dpp v119, v119, v122 row_shl:4 row_mask:0xf bank_mask:0xf bound_ctrl:1\n\t"
    "v_max3_f32 v120, v120, v115, v116\n\t"
    "v_mul_f32 v123, %[c2], v123\n\t"
    "v_max3_f32 v120, v120, v117, v118\n\t"
    "v_max_f32 v120, v120, v119\n\t"
    "v_mov_b32 v121, v120\n\t"
    TR(56, va0, 512) TR(58, va1, 512)
    "v_permlane16_swap_b32 v120, v121\n\t"
    "v_max_f32 v120, v120, v121\n\t"
    "v_mov_b32 v121, v120\n\t"
    TR(60, va2, 512) TR(62, va3, 512)
    "v_permlane32_swap_b32 v120, v121\n\t"
    "v_max_f32 v120, v120, v121\n\t"
    "v_mul_f32_e64 v121, -v120, v123\n\t"
    "v_mul_f32 v122, v120, v123\n\t"
    "v_fma_f32 v112, v112, v123, v121\n\t"
    "v_fma_f32 v113, v113, v123, v121\n\t"
    "v_exp_f32 v112, v112\n\t"
    TR(64, va0, 1024)
    "v_fma_f32 v114, v114, v123, v121\n\t"
    "v_exp_f32 v113, v113\n\t"
    TR(66, va1, 1024)
    "v_fma_f32 v115, v115, v123, v121\n\t"
    "v_exp_f32 v114, v114\n\t"
    TR(68, va2, 1024)
    "v_fma_f32 v116, v116, v123, v121\n\t"
    "v_exp_f32 v115, v115\n\t"
    TR(70, va3, 1024)
    "v_fma_f32 v117, v117, v123, v121\n\t"
    "v_exp_f32 v116, v116\n\t"
    "v_cvt_pk_bf16_f32 v124, v112, v113\n\t"
    "v_fma_f32 v118, v118, v123, v121\n\t"
    "v_exp_f32 v117, v117\n\t"
    TR(72, va0, 1536)
    "v_fma_f32 v119, v119, v123, v121\n\t"
    "v_exp_f32 v118, v118\n\t"
    "v_cvt_pk_bf16_f32 v125, v114, v115\n\t"
    "v_exp_f32 v119, v119\n\t"
    TR(74, va1, 1536) TR(76, va2, 1536) TR(78, va3, 1536)
    "v_cvt_pk_bf16_f32 v126, v116, v117\n\t"
    "v_cvt_pk_bf16_f32 v127, v118, v119\n\t"
    PCP(96, 124, "quad_perm:[0,1,2,3]", "0x1") PCP(100, 124, "row_shr:4", "0x2") PCP(104, 124, "quad_perm:[0,1,2,3]", "0x4") PCP(108, 124, "row_shr:4", "0x8")
    PCP(97, 125, "quad_perm:[0,1,2,3]", "0x1") PCP(101, 125, "row_shr:4", "0x2") PCP(105, 125, "quad_perm:[0,1,2,3]", "0x4") PCP(109, 125, "row_shr:4", "0x8")
    PCP(98, 126, "quad_perm:[0,1,2,3]", "0x1") PCP(102, 126, "row_shr:4", "0x2") PCP(106, 126, "quad_perm:[0,1,2,3]", "0x4") PCP(110, 126, "row_shr:4", "0x8")
    PCP(99, 127, "quad_perm:[0,1,2,3]", "0x1") PCP(103, 127, "row_shr:4", "0x2") PCP(107, 127, "quad_perm:[0,1,2,3]", "0x4") PCP(111, 127, "row_shr:4", "0x8")
    "v_mov_b32 v84, %[ones]\n\tv_mov_b32 v85, %[ones]\n\tv_mov_b32 v86, %[ones]\n\tv_mov_b32 v87, %[ones]\n\t"
    "s_waitcnt lgkmcnt(0)\n\t"
    PVA(112, 96, 48, "0") PVA(116, 96, 64, "0")
    PVA(112, 100, 52, "v[112:115]") PVA(116, 100, 68, "v[116:119]")
    PVA(112, 104, 56, "v[112:115]") PVA(116, 104, 72, "v[116:119]")
    PVA(112, 108, 60, "v[112:115]") PVA(116, 108, 76, "v[116:119]")
    "v_mfma_f32_16x16x32_bf16 v[80:83], v[84:87], v[124:127], 0\n\t"
    "s_nop 6\n\t"
    "ds_write_b128 %[oa], v[112:115]\n\t"
    "ds_write_b128 %[oa], v[116:119] offset:1024\n\t"
    "s_mov_b64 exec, 0xf\n\t"
    "ds_write2_b32 %[mla], v122, v80 offset0:0 offset1:16\n\t"
    "s_mov_b64 exec, -1\n\t"
    :: [qa] "v"(qa), [sta] "v"(sta), [va0] "v"(va0), [va1] "v"(va1), [va2] "v"(va2), [va3] "v"(va3), [oa] "v"(oa), [mla] "v"(mla),
       [c2] "s"(C2), [ones] "s"(ONES), [to] "i"(TO) : "memory");
}
__device__ __forceinline__ void seg_merge(u32 mla, u32 oa, u32 so, const char* ws) {
  asm volatile(
    "ds_read_b128 v[48:51], %[mla]\n\t"
    "ds_read_b128 v[60:63], %[mla] offset:64\n\t"
    "ds_read2st64_b32 v[56:57], %[oa] offset0:0 offset1:8\n\t"
    "ds_read2st64_b32 v[58:59], %[oa] offset0:16 offset1:24\n\t"
    "s_waitcnt lgkmcnt(3)\n\t"
    "v_max3_f32 v52, v48, v49, v50\n\t"
    "v_max_f32 v52, v52, v51\n\t"
    "v_sub_f32 v48, v48, v52\n\t"
    "v_sub_f32 v49, v49, v52\n\t"
    "v_exp_f32 v48, v48\n\t"
    "v_sub_f32 v50, v50, v52\n\t"
    "v_exp_f32 v49, v49\n\t"
    "v_sub_f32 v51, v51, v52\n\t"
    "v_exp_f32 v50, v50\n\t"
    "v_exp_f32 v51, v51\n\t"
    "s_waitcnt lgkmcnt(0)\n\t"
    "v_pk_mul_f32 v[60:61], v[60:61], v[48:49]\n\t"
    "v_pk_mul_f32 v[56:57], v[56:57], v[48:49]\n\t"
    "v_pk_fma_f32 v[60:61], v[62:63], v[50:51], v[60:61]\n\t"
    "v_pk_fma_f32 v[56:57], v[58:59], v[50:51], v[56:57]\n\t"
    "v_add_f32 v60, v60, v61\n\t"
    "v_add_f32 v56, v56, v57\n\t"
    "v_rcp_f32 v60, v60\n\t"
    "s_nop 0\n\t"
    "v_mul_f32 v52, v56, v60\n\t"
    PACK_STORE
    :: [mla] "v"(mla), [oa] "v"(oa), [so] "v"(so), [ws] "s"(ws), [c8z] "s"(C8Z) : "memory");
}

__device__ __forceinline__ void dma16(u32 voff, const char* sbase, u32 lds_dst) {
  u32 keep;
  asm volatile(SNOP "s_mov_b32 %0, m0\n\ts_mov_b32 m0, %3\n\ts_nop 0\n\tglobal_load_lds_dwordx4 %1, %2" NTKV "\n\ts_mov_b32 m0, %0"
               : "=&s"(keep) : "v"(voff), "s"(sbase), "s"(lds_dst) : "memory");
}
__device__ __forceinline__ void kv_load(const char* base, int w, int lane, u32 ldsb) {
  asm volatile("" : "+s"(base));
  const int r8 = (lane & 31) >> 2;
#pragma unroll
  for (int i = 0; i < 4; ++i) {
    const u32 ch = 8 * (i & 1) + 4 * (lane >> 5) + ((lane & 3) ^ ((2 * (i >> 1) + (r8 >> 2)) & 3));
    dma16(r8 * 256 + ch * 16, base + (size_t)(16 * w + 8 * (i >> 1)) * 256, ldsb + w * 4096 + i * 1024);
  }
}
__device__ __forceinline__ bool has_sent(v4u x) {
  const u32 m = __builtin_elementwise_max(__builtin_elementwise_max(x[0], x[1]), __builtin_elementwise_max(x[2], x[3]));
  return __builtin_amdgcn_ballot_w64(m == SENT) != 0ull;
}
__device__ __forceinline__ v4u poll128(const char* base, u32 voff) {
  v4u x;
  for (u32 spins = 0;; ++spins) {
    asm volatile("s_nop 4\n\tglobal_load_dwordx4 %0, %1, %2 sc1\n\ts_waitcnt vmcnt(0)" : "=&v"(x) : "v"(voff), "s"(base) : "memory");
    if (!has_sent(x) || spins > SPIN_CAP) break;
  }
  return x;
}
__device__ __forceinline__ v4u pack8(float v) {
  const u32 x = safe(pk(v, dpp<XOR1>(v)));
  return v4u{x, dppu<0x102>(x), dppu<0x104>(x), dppu<0x106>(x)};
}
__device__ __forceinline__ void sum_partials(v4u x, float (&v)[8]) {
#pragma unroll
  for (int m = 0; m < 4; ++m) { v[2 * m] = bflo(x[m]); v[2 * m + 1] = bfhi(x[m]); }
#pragma unroll
  for (int j = 0; j < 8; ++j) v[j] += dpp<XOR1>(v[j]);
#pragma unroll
  for (int j = 0; j < 8; ++j) v[j] += dpp<XOR2>(v[j]);
#pragma unroll
  for (int j = 0; j < 8; ++j) v[j] += dpp<HMIR>(v[j]);
}
__device__ __forceinline__ void x_stats(v4u xa, char* lds, int w, int lane) {
  const bool p = (lane >> 3) & 1; const int kq = lane >> 4;
  const v4f gm = mfma(xa, xa, v4f{0.f, 0.f, 0.f, 0.f});
  const v4f sm = mfma(xa, v4u{ONES, ONES, ONES, ONES}, v4f{0.f, 0.f, 0.f, 0.f});
  float s2 = (gm[0] + gm[1]) + (gm[2] + gm[3]);
  s2 = (p == (bool)(kq >> 1)) ? s2 : 0.f;
  s2 += dpp<XOR1>(s2); s2 += dpp<XOR2>(s2); s2 += dpp<HMIR>(s2); s2 += dpp<ROR8>(s2);
  s2 = x32_add(x16_add(s2));
  float s1 = (sm[0] + sm[1]) + (sm[2] + sm[3]);
  s1 = x32_add(x16_add(s1));
  if (lane == 0) *(v2f*)(lds + L_ST + w * 8) = v2f{s1, s2};
}
}

#define ST(i) do {} while (0)
#define STFLUSH(sv, base) do {} while (0)
#define SGPR_ATTR

__device__ __forceinline__ u32 lb_st() { return L_ST; }
#define OPAQUE_LANE() int lane = lane0; asm volatile("" : "+v"(lane)); const int c8 = lane & 7, kq = lane >> 4; const bool p = (lane >> 3) & 1; (void)c8; (void)p; (void)kq
#define OPAQUE_S(x) asm volatile("" : "+s"(x))

constexpr int FW = 4;

struct Addr {
  u32 vB, vA, vL, lw, so;
  u32 mr, orr;
  u32 r0, r1, r2, r3, r4, r5, r6, r7, r8;
};
#define PIN(x) asm volatile("" : "+v"(x))

template <int TYPE>
__device__ __forceinline__ void layer(const int rep, char* ws, char* lds, int b, int g, int s, int w, const int lane0, v4u xraw, Addr& A, u32 qaT,
                                      const char* nkc, const char* nvc, const char* nwq, const char* cwo, const char* nwo, const bool lead,
                                      const u32 tag, int& stampv, u32 (&cnt)[3]) {
  constexpr int RQ = TYPE ? R_WQB : R_WQA, RO = TYPE ? R_WOB : R_WOA;
  const int L = rep * 2 + TYPE;
  OPAQUE_S(b); OPAQUE_S(g); OPAQUE_S(s); OPAQUE_S(w);
  const u32 grp = (u32)(b * 8 + g), cu = grp * 8 + s;
#define LD_WQ(R, ptr) do { OPAQUE_LANE(); OPAQUE_S(ptr); bulk8<R, 8192>(ptr + (size_t)(64 * w) * 8192 + (512 * g + 64 * s) * 2, (u32)(lane >> 3) * 65536 + c8 * 16); } while (0)
#define LD_WO(R, ptr) do { OPAQUE_LANE(); OPAQUE_S(ptr); bulk8<R, 1024>(ptr + (size_t)(512 * g + 64 * w) * 1024 + 128 * s, (u32)(lane >> 3) * 8192 + c8 * 16); } while (0)
  const char* wsL = ws + L * BOXL;
  const char* box = ws + WS_BOX + cu * BOX + L * BOXL;
  {
    if (rep == 0) {
      if (SCHED == 0) { if (TYPE == 0) VMWAIT(8); else VMWAIT(0); }
      else { if (TYPE == 0) { if (!lead) VMWAIT(4); } else { VMWAIT(4); } }
      if (w >= 4) ST(7);
      transpose8<RQ>();
    }
    if (L > 0) {
      const char* wsP = wsL - BOXL;
      if (DLY_B && rep) __builtin_amdgcn_s_sleep(DLY_B);
      ST(0);
      u32 sp = 0;
      seg_p1<RQ, true, 0>(A.vB, wsP, A.lw, sp);
      cnt[0] += sp;
    } else {
      const v4u xa = (lane0 & 7) == 0 ? xraw : v4u{0u, 0u, 0u, 0u};
      asm volatile("v_mov_b32 v48, %0\n\tv_mov_b32 v49, %1\n\tv_mov_b32 v50, %2\n\tv_mov_b32 v51, %3" :: "v"(xa[0]), "v"(xa[1]), "v"(xa[2]), "v"(xa[3]));
      u32 sp = 0;
      seg_p1<RQ, false, 0>(0, ws, A.lw, sp);
    }
    ST(2);
    if (rep == 0 && TYPE == 1) {
      OPAQUE_LANE();
      if (SCHED == 0) kv_load(nkc, w, lane, L_K + 32768);
      kv_load(nvc, w, lane, L_V + 32768);
    }
    v4u xa;
    asm volatile("v_mov_b32 %0, v48\n\tv_mov_b32 %1, v49\n\tv_mov_b32 %2, v50\n\tv_mov_b32 %3, v51" : "=v"(xa[0]), "=v"(xa[1]), "=v"(xa[2]), "=v"(xa[3]));
    if (rep == 0) {
      asm volatile("v_mov_b32 v48, %0\n\tv_mov_b32 v49, %0\n\tv_mov_b32 v50, %0\n\tv_mov_b32 v51, %0" :: "s"(ONES));
      u32 sp = 0;
      seg_p1<RQ, false, L_REDC - L_RED>(0, ws, A.lw, sp);
    }
    BAR();
    ST(3);
    if (w == FW) {
      seg_final2<false>(A.r2, 0, A.so, box + BOX_Q);
      ST(4);
      OPAQUE_LANE();
      if (rep == 0) {
        const __amdgpu_buffer_rsrc_t rs = __builtin_amdgcn_make_buffer_rsrc(ws, 0, WS_END, 0x00020000);
        const v4f r0 = *(const v4f*)(lds + L_REDC + lane * 32), r1 = *(const v4f*)(lds + L_REDC + lane * 32 + 16);
        const float cs = 0.125f * (((r0[0] + r0[1]) + (r0[2] + r0[3])) + ((r1[0] + r1[1]) + (r1[2] + r1[3])));
        const v4u pc = pack8(cs);
        if (c8 == 0) __builtin_amdgcn_raw_buffer_store_b128(pc, rs, WS_BOX + cu * BOX + BOX_CS + TYPE * 128 + lane * 2, 0, 16);
      }
      x_stats(xa, lds, w, lane);
      if (L == 0) {
        const u64 flag = (u64)tag | ((u64)(~tag) << 32);
        for (u32 spins = 0;; ++spins) {
          const u64 f = __hip_atomic_load((GAS u64*)(ws + WS_RDY + b * 512 + lane * 8), __ATOMIC_RELAXED, __HIP_MEMORY_SCOPE_AGENT);
          if (__all(f == flag) || spins > SPIN_CAP) break;
        }
        if (g == 0 && s == 0 && lane == 0) __hip_atomic_store((GAS u32*)(ws + WS_EPOCH + b * 256), tag, __ATOMIC_RELAXED, __HIP_MEMORY_SCOPE_AGENT);
      }
      if (DLY_C) __builtin_amdgcn_s_sleep(DLY_C);
      u32 sp = 0;
      seg_qpoll<0>(A.r0, wsL, A.r1, sp);
      cnt[1] += sp;
      if (rep == 0) { sp = 0; seg_qpoll<L_CSI + TYPE * 1088 - L_Q>(A.r0, ws + (BOX_CS + TYPE * 128 - BOX_Q), A.r1, sp); }
      ST(5);
    } else {
      OPAQUE_LANE();
      x_stats(xa, lds, w, lane);
    }
    if (g == 0 && w == s) {
      OPAQUE_LANE();
      float v[8];
      sum_partials(xa, v);
      if (c8 == 0) {
        float* xs = (float*)(lds + L_XS) + 16 * kq + (p ? 8 : 0);
        *(v4f*)xs = v4f{v[0], v[1], v[2], v[3]};
        *(v4f*)(xs + 4) = v4f{v[4], v[5], v[6], v[7]};
      }
    }
    if (rep == 0) {
      if (SCHED == 1 && TYPE == 0 && lead) VMWAIT(16); else VMWAIT(0);
      if (w >= 4) ST(8);
      BAR();
      if (SCHED == 0) LD_WO(RO, cwo);
      else {
        if (!lead) LD_WO(RO, cwo);
        else if (TYPE == 0) LD_WO(R_WOB, nwo);
      }
    }
  }
  if (w < 4) {
    seg_kpre<TYPE * 32768>(A.r0, A.r1);
    ST(6);
    BAR();
    ST(7);
    seg_attn<TYPE * 32768>(qaT, A.r6, A.r2, A.r3, A.r4, A.r5, A.r7, A.r8);
    ST(8);
  } else {
    BAR();
  }
  {
    BAR();
    seg_merge(A.mr, A.orr, A.so, box + w * 128);
    if (w == 7) {
      OPAQUE_LANE();
      const __amdgpu_buffer_rsrc_t rs = __builtin_amdgcn_make_buffer_rsrc(ws, 0, WS_END, 0x00020000);
      const int hh = (lane >> 2) & 3, aa = lane & 3;
      const float m1 = ((const float*)(lds + L_ML))[hh * 4 + aa], l1 = ((const float*)(lds + L_ML))[16 + hh * 4 + aa];
      float M1 = __builtin_fmaxf(m1, dpp<XOR1>(m1)); M1 = __builtin_fmaxf(M1, dpp<XOR2>(M1));
      float L1 = __builtin_amdgcn_exp2f(m1 - M1) * l1; L1 += dpp<XOR1>(L1); L1 += dpp<XOR2>(L1);
      const u32 e = safe(__float_as_uint(M1 + __builtin_amdgcn_logf(L1)));
      const v4u ev = {dppu<0x150>(e), dppu<0x154>(e), dppu<0x158>(e), dppu<0x15C>(e)};
      if (lane < 8) __builtin_amdgcn_raw_buffer_store_b128(ev, rs, WS_LSR + ((L * 32 + grp) * 8 + lane) * 128 + s * 16, 0, 16);
    }
    ST(9);
  }
  {
    const char* wsS = ws + L * 32768;
    if (rep == 0) {
      if (SCHED == 1 && TYPE == 0 && lead) VMWAIT(8); else VMWAIT(0);
      transpose8<RO>();
    } else {
      if (DLY_A) __builtin_amdgcn_s_sleep(DLY_A);
    }
    ST(1);
    u32 sp = 0;
    seg_p3<RO>(A.vA, A.vL, wsL, wsS, A.lw, sp);
    cnt[2] += sp;
    ST(10);
    if (rep == 0 && TYPE == 0) {
      if (SCHED == 0) LD_WQ(R_WQB, nwq);
      else {
        if (!lead) LD_WQ(R_WQB, nwq);
        OPAQUE_LANE();
        kv_load(nkc, w, lane, L_K + 32768);
      }
    }
    BAR();
    if (w == FW) seg_final2<true>(A.r2, A.r3, A.so, box + BOX_X);
    ST(11);
  }
  STFLUSH(stampv, L * 12);
}

__global__ void __launch_bounds__(512, 2) __attribute__((amdgpu_num_vgpr(24) SGPR_ATTR))
fused_kernel(const char* __restrict__ xin, const char* kA, const char* vA, const char* wqA, const char* woA,
             const char* kB, const char* vB, const char* wqB, const char* woB, unsigned short* out, char* ws) {
  extern __shared__ __attribute__((aligned(16))) char lds[];
  const int id = blockIdx.x, g = id & 7, b = id >> 6, s = (id >> 3) & 7;
  const int tid = threadIdx.x, lane = tid & 63, w = __builtin_amdgcn_readfirstlane(tid >> 6);
  const int c8 = lane & 7, kq = lane >> 4;
  const bool p = (lane >> 3) & 1;
  int stampv = 0;
  const __amdgpu_buffer_rsrc_t rs = __builtin_amdgcn_make_buffer_rsrc(ws, 0, WS_END, 0x00020000);
  const u32 cu = (u32)((b * 8 + g) * 8 + s);
  asm volatile("" ::: "v127", "v255");
  u32 ep; v4u xraw;
  asm volatile("s_nop 4\n\tglobal_load_dword v56, %0, %1 sc1\n\tglobal_load_dwordx4 v[52:55], %2, %3"
               :: "v"(WS_EPOCH + b * 256), "s"(ws), "v"((u32)((b * 512 + 64 * w + 16 * kq + (p ? 8 : 0)) * 2)), "s"(xin) : "memory");
  if (w == 5) {
    const v4u sv = {SENT, SENT, SENT, SENT};
#pragma unroll
    for (int i = 0; i < 5; ++i) __builtin_amdgcn_raw_buffer_store_b128(sv, rs, WS_BOX + cu * BOX + i * 1024 + lane * 16, 0, 16);
    const u32 o6 = lane < 16 ? WS_BOX + cu * BOX + 5120 + lane * 16
                             : WS_LSR + (u32)(((((lane - 16) >> 3) * 32 + b * 8 + g) * 8 + (lane & 7)) * 128 + s * 16);
    if (lane < 48) __builtin_amdgcn_raw_buffer_store_b128(sv, rs, o6, 0, 16);
  }
  if (w == 6) ((float*)(lds + L_XS))[lane] = 0.f;
  const size_t kvoff = ((size_t)(b * 8 + g) * SK + (SK - WIN) + s * 128) * 256;
  const bool lead = (w >> 1) == b;
  const size_t qoff = (size_t)(64 * w) * 8192 + (512 * g + 64 * s) * 2, ooff = (size_t)(512 * g + 64 * w) * 1024 + 128 * s;
  const u32 vq = (u32)(lane >> 3) * 65536 + c8 * 16, vo = (u32)(lane >> 3) * 8192 + c8 * 16;
  if (SCHED == 0 || lead) {
    bulk8<R_WQA, 8192>(wqA + qoff, vq);
    VMWAIT(8);
  } else {
    VMWAIT(0);
  }
  asm volatile("v_mov_b32 %0, v52\n\tv_mov_b32 %1, v53\n\tv_mov_b32 %2, v54\n\tv_mov_b32 %3, v55\n\tv_mov_b32 %4, v56"
               : "=v"(xraw[0]), "=v"(xraw[1]), "=v"(xraw[2]), "=v"(xraw[3]), "=v"(ep));
  ST(6);
  const u32 tag = (u32)__builtin_amdgcn_readfirstlane((int)ep) + 1u;
  if (w == 5 && lane == 0) {
    const u64 flag = (u64)tag | ((u64)(~tag) << 32);
    __hip_atomic_store((GAS u64*)(ws + WS_RDY + b * 512 + (s * 8 + g) * 8), flag, __ATOMIC_RELAXED, __HIP_MEMORY_SCOPE_AGENT);
  }
  if (SCHED == 0) {
    asm volatile("s_barrier" ::: "memory");
    kv_load(kA + kvoff, w, lane, L_K);
    kv_load(vA + kvoff, w, lane, L_V);
  } else {
    kv_load(kA + kvoff, w, lane, L_K);
    if (lead) VMWAIT(4);
    asm volatile("s_barrier" ::: "memory");
    if (!lead) bulk8<R_WQA, 8192>(wqA + qoff, vq);
    kv_load(vA + kvoff, w, lane, L_V);
    if (lead) { bulk8<R_WOA, 1024>(woA + ooff, vo); bulk8<R_WQB, 8192>(wqB + qoff, vq); }
  }
  Addr A; u32 qa0, qa1;
  {
    const u32 grp = (u32)(b * 8 + g);
    const u32 oct = kq * 32 + (p ? 16 : 0);
    A.vB = WS_BOX + (u32)((b * 8 + c8) * 8 + w) * BOX + BOX_X + oct;
    A.vA = WS_BOX + (grp * 8 + c8) * BOX + w * 128 + oct;
    A.vL = WS_LSR + (grp * 8 + s) * 128 + c8 * 16 + (w >> 1) * 4;
    A.lw = L_RED + (8 * c8 + (lane >> 3)) * 32 + w * 4;
    A.so = lane * 2;
    A.mr = L_ML + (w >> 1) * 16;
    A.orr = L_OM + (w & 1) * 1024 + lane * 16 + (w >> 1) * 4;
    const int r16 = lane & 15, q4 = r16 >> 2, p2 = lane & 3;
    qa0 = L_Q + (lane & 3) * 272 + kq * 16;
    qa1 = qa0;
    if (lane & 4) { qa0 += L_CSI - L_Q; qa1 += L_CSI + 1088 - L_Q; }
    if (w < 4) {
      const u32 kb = L_K + 2048 * (4 * w + (r16 >> 2)) + 64 * (r16 & 3);
      A.r0 = kb + 16 * (kq ^ ((2 * (r16 >> 2)) & 3));
      A.r1 = kb + 256 + 16 * (kq ^ ((2 * (r16 >> 2) + 1) & 3));
      const u32 vb = L_V + 2048 * (4 * w + kq) + 64 * q4 + 8 * (p2 & 1);
      A.r2 = vb + 16 * (((p2 >> 1)) ^ ((2 * kq) & 3));
      A.r3 = vb + 256 + 16 * (((p2 >> 1)) ^ ((2 * kq + 1) & 3));
      A.r4 = vb + 16 * ((2 + (p2 >> 1)) ^ ((2 * kq) & 3));
      A.r5 = vb + 256 + 16 * ((2 + (p2 >> 1)) ^ ((2 * kq + 1) & 3));
      A.r7 = L_OM + w * 2048 + lane * 16;
      A.r8 = L_ML + (lane * 4 + w) * 4;
    } else {
      A.r0 = WS_BOX + (grp * 8 + (lane >> 3)) * BOX + BOX_Q + c8 * 16;
      A.r1 = L_Q + kq * 272 + (lane & 15) * 16;
      A.r2 = L_RED + lane * 32;
      A.r3 = L_XS + lane * 4;
      A.r4 = A.r5 = A.r7 = A.r8 = 0;
    }
    A.r6 = L_ST + c8 * 8;
    PIN(A.r6);
    PIN(A.vB); PIN(A.vA); PIN(A.vL); PIN(A.lw); PIN(A.so); PIN(A.mr); PIN(A.orr); PIN(A.r0); PIN(A.r1); PIN(A.r2); PIN(A.r3); PIN(A.r4); PIN(A.r5); PIN(A.r7); PIN(A.r8);
    PIN(qa0); PIN(qa1);
  }
  u32 cnt[3] = {0, 0, 0};
  for (int rep = 0; rep < 2; ++rep) {
    layer<0>(rep, ws, lds, b, g, s, w, lane, xraw, A, qa0, kB + kvoff, nullptr, wqB, woA, woB, lead, tag, stampv, cnt);
    layer<1>(rep, ws, lds, b, g, s, w, lane, xraw, A, qa1, kB + kvoff, vB + kvoff, nullptr, woB, nullptr, lead, tag, stampv, cnt);
  }
  if (g == 0 && w == s) {
    const u32 voff = WS_BOX + (u32)((b * 8 + c8) * 8 + w) * BOX + 3u * BOXL + BOX_X + kq * 32 + (p ? 16 : 0);
    if (DLY_B) __builtin_amdgcn_s_sleep(DLY_B);
    const v4u x = poll128(ws, voff);
    float v[8];
    sum_partials(x, v);
    if (c8 == 0) *(v4u*)(out + b * 512 + 64 * w + 16 * kq + (p ? 8 : 0)) = v4u{pk(v[0], v[1]), pk(v[2], v[3]), pk(v[4], v[5]), pk(v[6], v[7])};
  }
}

extern "C" void kernel_launch(void* const* d_in, const int* in_sizes, int n_in, void* d_out, int out_size, void* d_ws, size_t ws_size,
                              hipStream_t stream) {
  (void)hipFuncSetAttribute((const void*)fused_kernel, hipFuncAttributeMaxDynamicSharedMemorySize, L_END);
  fused_kernel<<<256, 512, L_END, stream>>>((const char*)d_in[0], (const char*)d_in[1], (const char*)d_in[2], (const char*)d_in[3], (const char*)d_in[4],
                                            (const char*)d_in[6], (const char*)d_in[7], (const char*)d_in[8], (const char*)d_in[9],
                                            (unsigned short*)d_out, (char*)d_ws);
}
```

```cpp
#include <hip/hip_runtime.h>
#include <stdint.h>

typedef unsigned int u32;
typedef unsigned long long u64;
typedef u32 v4u __attribute__((ext_vector_type(4)));
typedef u32 v2u __attribute__((ext_vector_type(2)));
typedef float v4f __attribute__((ext_vector_type(4)));
typedef float v2f __attribute__((ext_vector_type(2)));
typedef __bf16 v8b __attribute__((ext_vector_type(8)));
typedef __bf16 v2b __attribute__((ext_vector_type(2)));
#define GAS __attribute__((address_space(1)))

#define DLY_C 2
#define DLY_A 8
#define DLY_B 10
#define SCHED 0
#define PRETOUCH 0
#define NTKV ""
#define NTW ""

namespace {
constexpr int SK = 4096, WIN = 1024;
constexpr u32 WS_EPOCH = 0;
constexpr u32 WS_RDY = 4096;
constexpr u32 WS_LSR = 8192;
constexpr u32 WS_BOX = WS_LSR + 131072;
constexpr u32 BOXL = 1280, BOX_Q = 1024, BOX_X = 1152, BOX_CS = 5120, BOX = 5376;
constexpr u32 WS_END = WS_BOX + 256 * BOX;
constexpr u32 WS_DBG = 4u << 20;
constexpr u32 L_KV = 0;
constexpr u32 L_K = 0, L_V = 65536;
constexpr u32 L_OM = 131072;
constexpr u32 L_ML = L_OM + 8192;
constexpr u32 L_Q = L_ML + 128;
constexpr u32 L_CSI = L_Q + 1088;
constexpr u32 L_RED = L_CSI + 2176;
constexpr u32 L_REDC = L_RED + 2048;
constexpr u32 L_ST = L_REDC + 2048;
constexpr u32 L_RED3 = L_ST + 64;
constexpr u32 L_XS = L_RED3 + 2048;
constexpr u32 L_END = L_XS + 256;
static_assert(L_END <= 160 * 1024, "lds");
static_assert(L_Q % 16 == 0 && L_CSI % 16 == 0 && L_RED % 16 == 0 && L_ST % 16 == 0 && L_RED3 % 16 == 0 && L_XS % 16 == 0, "align");
constexpr u32 SENT = 0xFFFFFFFFu;
constexpr u32 SPIN_CAP = 1u << 15;
constexpr float C2 = 0.08838834764831845f * 1.4426950408889634f;
constexpr u32 ONES = 0x3F803F80u;
constexpr u64 PMASK = 0xFF00FF00FF00FF00ull;
constexpr u64 C8Z = 0x0101010101010101ull;

__device__ __forceinline__ float bflo(u32 w) { return __uint_as_float(w << 16); }
__device__ __forceinline__ float bfhi(u32 w) { return __uint_as_float(w & 0xffff0000u); }
__device__ __forceinline__ u32 pk(float lo, float hi) { v2f f = {lo, hi}; return __builtin_bit_cast(u32, __builtin_convertvector(f, v2b)); }
__device__ __forceinline__ u32 safe(u32 v) { return v < 0xFFFFFFFEu ? v : 0xFFFFFFFEu; }
template <int CTRL> __device__ __forceinline__ float dpp(float x) {
  return __builtin_bit_cast(float, __builtin_amdgcn_mov_dpp(__builtin_bit_cast(int, x), CTRL, 0xf, 0xf, true));
}
template <int CTRL> __device__ __forceinline__ u32 dppu(u32 x) { return (u32)__builtin_amdgcn_mov_dpp((int)x, CTRL, 0xf, 0xf, true); }
constexpr int XOR1 = 0xB1, XOR2 = 0x4E, HMIR = 0x141, ROR8 = 0x128;
__device__ __forceinline__ float x16_add(float x) { auto s = __builtin_amdgcn_permlane16_swap(__float_as_uint(x), __float_as_uint(x), false, false); return __uint_as_float(s[0]) + __uint_as_float(s[1]); }
__device__ __forceinline__ float x32_add(float x) { auto s = __builtin_amdgcn_permlane32_swap(__float_as_uint(x), __float_as_uint(x), false, false); return __uint_as_float(s[0]) + __uint_as_float(s[1]); }
__device__ __forceinline__ v4f mfma(v4u a, v4u b, v4f c) {
  return __builtin_amdgcn_mfma_f32_16x16x32_bf16(__builtin_bit_cast(v8b, a), __builtin_bit_cast(v8b, b), c, 0, 0, 0);
}
#define BAR() asm volatile("s_waitcnt lgkmcnt(0)\n\ts_barrier" ::: "memory")
#define VMWAIT(n) asm volatile("s_waitcnt vmcnt(" #n ")" ::: "memory")

constexpr int R_WQA = 128, R_WOA = 160, R_WQB = 192, R_WOB = 224;
template <int BASE, u32 STRIDE>
__device__ __forceinline__ void bulk8(const char* base, u32 voff) {
  asm volatile("" : "+s"(base));
  const __amdgpu_buffer_rsrc_t r = __builtin_amdgcn_make_buffer_rsrc((void*)base, 0, 0x7fffffff, 0x00020000);
  asm volatile("s_nop 4\n\t"
               "buffer_load_dwordx4 v[%c10:%c11], %0, %1, 0 offen" NTW "\n\tbuffer_load_dwordx4 v[%c12:%c13], %0, %1, %3 offen" NTW "\n\t"
               "buffer_load_dwordx4 v[%c14:%c15], %0, %1, %4 offen" NTW "\n\tbuffer_load_dwordx4 v[%c16:%c17], %0, %1, %5 offen" NTW "\n\t"
               "buffer_load_dwordx4 v[%c18:%c19], %0, %1, %6 offen" NTW "\n\tbuffer_load_dwordx4 v[%c20:%c21], %0, %1, %7 offen" NTW "\n\t"
               "buffer_load_dwordx4 v[%c22:%c23], %0, %1, %8 offen" NTW "\n\tbuffer_load_dwordx4 v[%c24:%c25], %0, %1, %9 offen" NTW
               :: "v"(voff), "s"(r), "s"(0), "s"(STRIDE), "s"(2 * STRIDE), "s"(3 * STRIDE), "s"(4 * STRIDE), "s"(5 * STRIDE), "s"(6 * STRIDE), "s"(7 * STRIDE),
                  "i"(BASE), "i"(BASE + 3), "i"(BASE + 4), "i"(BASE + 7), "i"(BASE + 8), "i"(BASE + 11), "i"(BASE + 12), "i"(BASE + 15),
                  "i"(BASE + 16), "i"(BASE + 19), "i"(BASE + 20), "i"(BASE + 23), "i"(BASE + 24), "i"(BASE + 27), "i"(BASE + 28), "i"(BASE + 31)
               : "memory");
}
template <int B, int M, int R>
__device__ __forceinline__ void tp_pair(u32 slo, u32 shi) {
  if constexpr (M == R) {
    asm volatile("v_perm_b32 v48, v%c3, v%c2, %0\n\tv_perm_b32 v%c3, v%c3, v%c2, %1\n\tv_mov_b32 v%c2, v48"
                 :: "s"(slo), "s"(shi), "i"(B + 8 * M + R), "i"(B + 8 * M + 4 + R));
  } else {
    asm volatile("v_perm_b32 v48, v%c3, v%c2, %0\n\tv_perm_b32 v49, v%c3, v%c2, %1\n\t"
                 "v_perm_b32 v%c2, v%c5, v%c4, %0\n\tv_perm_b32 v%c3, v%c5, v%c4, %1\n\t"
                 "v_mov_b32 v%c4, v48\n\tv_mov_b32 v%c5, v49"
                 :: "s"(slo), "s"(shi), "i"(B + 8 * M + R), "i"(B + 8 * M + 4 + R), "i"(B + 8 * R + M), "i"(B + 8 * R + 4 + M));
  }
}
template <int B>
__device__ __forceinline__ void transpose8() {
  const u32 slo = 0x05040100u, shi = 0x07060302u;
  tp_pair<B, 0, 0>(slo, shi); tp_pair<B, 1, 1>(slo, shi); tp_pair<B, 2, 2>(slo, shi); tp_pair<B, 3, 3>(slo, shi);
  tp_pair<B, 0, 1>(slo, shi); tp_pair<B, 0, 2>(slo, shi); tp_pair<B, 0, 3>(slo, shi);
  tp_pair<B, 1, 2>(slo, shi); tp_pair<B, 1, 3>(slo, shi); tp_pair<B, 2, 3>(slo, shi);
  asm volatile("s_nop 1");
}
#define SNOP
#define MF(d, b) "v_mfma_f32_16x16x32_bf16 v[" #d ":" #d "+3], v[48:51], v[%c[wb]+" #b ":%c[wb]+" #b "+3], 0\n\t"
#define FOLD_TAIL \
  "v_permlane32_swap_b32 v52, v68\n\t" \
  "v_permlane32_swap_b32 v56, v72\n\t" \
  "v_permlane32_swap_b32 v60, v76\n\t" \
  "v_permlane32_swap_b32 v64, v80\n\t" \
  "v_cndmask_b32_e64 v52, v52, v68, %[pm]\n\t" \
  "v_cndmask_b32_e64 v60, v60, v76, %[pm]\n\t" \
  "v_cndmask_b32_e64 v56, v56, v72, %[pm]\n\t" \
  "v_cndmask_b32_e64 v64, v64, v80, %[pm]\n\t" \
  "v_permlane16_swap_b32 v52, v60\n\t" \
  "s_nop 0\n\t" \
  "v_permlane16_swap_b32 v56, v64\n\t" \
  "v_add_f32 v52, v52, v60\n\t" \
  "v_add_f32 v56, v56, v64\n\t" \
  "s_nop 0\n\t" \
  "v_add_f32_dpp v52, v52, v52 row_ror:8 row_mask:0xf bank_mask:0xf\n\t" \
  "v_add_f32_dpp v56, v56, v56 row_ror:8 row_mask:0xf bank_mask:0xf\n\t" \
  "v_cndmask_b32_e64 v52, v52, v56, %[pm]\n\t"

template <int WB, bool POLL, int LOFF>
__device__ __forceinline__ void seg_p1(u32 voff, const char* ws, u32 ldsw, u32& spins) {
  if constexpr (POLL) {
    asm volatile(
      SNOP "Lp%=:\n\t"
      "global_load_dwordx4 v[48:51], %[voff], %[ws] sc1\n\t"
      "s_add_u32 %[sp], %[sp], 1\n\t"
      "s_waitcnt vmcnt(0)\n\t"
      MF(52, 0) "v_max3_u32 v84, v48, v49, v50\n\t"
      MF(56, 4) "v_max_u32 v84, v84, v51\n\t"
      MF(60, 8) "v_cmp_eq_u32 vcc, -1, v84\n\t"
      MF(64, 12)
      "s_cbranch_vccz Lk%=\n\t"
      "s_cmp_lt_u32 %[sp], %[cap]\n\t"
      "s_cbranch_scc1 Lp%=\n\t"
      "Lk%=:\n\t"
      : [sp] "+s"(spins) : [voff] "v"(voff), [ws] "s"(ws), [wb] "i"(WB), [cap] "i"(SPIN_CAP) : "memory", "vcc", "scc");
  } else {
    asm volatile("s_nop 1\n\t" MF(52, 0) "s_nop 0\n\t" MF(56, 4) "s_nop 0\n\t" MF(60, 8) "s_nop 0\n\t" MF(64, 12) "s_nop 0\n\t" :: [wb] "i"(WB));
  }
  asm volatile(
    MF(68, 16) "v_pk_add_f32 v[52:53], v[52:53], v[54:55]\n\t"
    MF(72, 20) "v_pk_add_f32 v[56:57], v[56:57], v[58:59]\n\t"
    MF(76, 24) "v_pk_add_f32 v[60:61], v[60:61], v[62:63]\n\t"
    MF(80, 28) "v_pk_add_f32 v[64:65], v[64:65], v[66:67]\n\t"
    "v_add_f32 v52, v52, v53\n\t"
    "v_pk_add_f32 v[68:69], v[68:69], v[70:71]\n\t"
    "v_add_f32 v56, v56, v57\n\t"
    "v_pk_add_f32 v[72:73], v[72:73], v[74:75]\n\t"
    "v_add_f32 v60, v60, v61\n\t"
    "v_pk_add_f32 v[76:77], v[76:77], v[78:79]\n\t"
    "v_add_f32 v64, v64, v65\n\t"
    "v_pk_add_f32 v[80:81], v[80:81], v[82:83]\n\t"
    "v_add_f32 v68, v68, v69\n\t"
    "v_add_f32 v72, v72, v73\n\t"
    "v_add_f32 v76, v76, v77\n\t"
    "v_add_f32 v80, v80, v81\n\t"
    "s_nop 0\n\t"
    FOLD_TAIL
    "ds_write_b32 %[lw], v52 offset:%c[lo]\n\t"
    :: [wb] "i"(WB), [pm] "s"(PMASK), [lw] "v"(ldsw), [lo] "i"(LOFF) : "memory");
}
template <int WB>
__device__ __forceinline__ void seg_p3(u32 voff, u32 voff2, const char* ws, const char* ws2, u32 ldsw, u32& spins) {
  asm volatile(
    SNOP "Lp%=:\n\t"
    "global_load_dwordx4 v[48:51], %[voff], %[ws] sc1\n\t"
    "global_load_dword v84, %[voff2], %[ws2] sc1\n\t"
    "s_add_u32 %[sp], %[sp], 1\n\t"
    "s_waitcnt vmcnt(0)\n\t"
    MF(52, 0) "v_max3_u32 v85, v48, v49, v50\n\t"
    MF(56, 4) "v_max3_u32 v85, v85, v51, v84\n\t"
    MF(60, 8) "v_cmp_eq_u32 vcc, -1, v85\n\t"
    MF(64, 12)
    "s_cbranch_vccz Lk%=\n\t"
    "s_cmp_lt_u32 %[sp], %[cap]\n\t"
    "s_cbranch_scc1 Lp%=\n\t"
    "Lk%=:\n\t"
    "v_max_f32_dpp v85, v84, v84 quad_perm:[1,0,3,2] row_mask:0xf bank_mask:0xf\n\t"
    MF(68, 16)
    "s_nop 0\n\t"
    "v_max_f32_dpp v85, v85, v85 quad_perm:[2,3,0,1] row_mask:0xf bank_mask:0xf\n\t"
    MF(72, 20)
    "s_nop 0\n\t"
    "v_max_f32_dpp v85, v85, v85 row_half_mirror row_mask:0xf bank_mask:0xf\n\t"
    MF(76, 24)
    "v_sub_f32 v86, v84, v85\n\t"
    MF(80, 28)
    "v_exp_f32 v86, v86\n\t"
    "s_nop 1\n\t"
    "v_mov_b32_dpp v90, v86 row_newbcast:0 row_mask:0x5 bank_mask:0xf\n\t"
    "v_mov_b32_dpp v91, v86 row_newbcast:1 row_mask:0x5 bank_mask:0xf\n\t"
    "v_mov_b32_dpp v92, v86 row_newbcast:2 row_mask:0x5 bank_mask:0xf\n\t"
    "v_mov_b32_dpp v93, v86 row_newbcast:3 row_mask:0x5 bank_mask:0xf\n\t"
    "v_mov_b32_dpp v90, v86 row_newbcast:4 row_mask:0xa bank_mask:0xf\n\t"
    "v_mov_b32_dpp v91, v86 row_newbcast:5 row_mask:0xa bank_mask:0xf\n\t"
    "v_mov_b32_dpp v92, v86 row_newbcast:6 row_mask:0xa bank_mask:0xf\n\t"
    "v_mov_b32_dpp v93, v86 row_newbcast:7 row_mask:0xa bank_mask:0xf\n\t"
    "v_add_f32_dpp v87, v86, v86 quad_perm:[1,0,3,2] row_mask:0xf bank_mask:0xf\n\t"
    "v_pk_mul_f32 v[52:53], v[52:53], v[90:91]\n\t"
    "v_pk_mul_f32 v[56:57], v[56:57], v[90:91]\n\t"
    "v_add_f32_dpp v87, v87, v87 quad_perm:[2,3,0,1] row_mask:0xf bank_mask:0xf\n\t"
    "v_pk_mul_f32 v[60:61], v[60:61], v[90:91]\n\t"
    "v_pk_mul_f32 v[64:65], v[64:65], v[90:91]\n\t"
    "v_add_f32_dpp v87, v87, v87 row_half_mirror row_mask:0xf bank_mask:0xf\n\t"
    "v_pk_mul_f32 v[68:69], v[68:69], v[90:91]\n\t"
    "v_pk_mul_f32 v[72:73], v[72:73], v[90:91]\n\t"
    "v_rcp_f32 v87, v87\n\t"
    "v_pk_mul_f32 v[76:77], v[76:77], v[90:91]\n\t"
    "v_pk_mul_f32 v[80:81], v[80:81], v[90:91]\n\t"
    "v_pk_fma_f32 v[52:53], v[54:55], v[92:93], v[52:53]\n\t"
    "v_pk_fma_f32 v[68:69], v[70:71], v[92:93], v[68:69]\n\t"
    "v_pk_fma_f32 v[56:57], v[58:59], v[92:93], v[56:57]\n\t"
    "v_pk_fma_f32 v[72:73], v[74:75], v[92:93], v[72:73]\n\t"
    "v_pk_fma_f32 v[60:61], v[62:63], v[92:93], v[60:61]\n\t"
    "v_pk_fma_f32 v[76:77], v[78:79], v[92:93], v[76:77]\n\t"
    "v_pk_fma_f32 v[64:65], v[66:67], v[92:93], v[64:65]\n\t"
    "v_pk_fma_f32 v[80:81], v[82:83], v[92:93], v[80:81]\n\t"
    "v_add_f32 v52, v52, v53\n\t"
    "v_add_f32 v68, v68, v69\n\t"
    "v_add_f32 v56, v56, v57\n\t"
    "v_add_f32 v72, v72, v73\n\t"
    "v_add_f32 v60, v60, v61\n\t"
    "v_add_f32 v76, v76, v77\n\t"
    "v_add_f32 v64, v64, v65\n\t"
    "v_add_f32 v80, v80, v81\n\t"
    "s_nop 1\n\t"
    FOLD_TAIL
    "v_mul_f32 v52, v52, v87\n\t"
    "ds_write_b32 %[lw], v52 offset:%c[lo]\n\t"
    : [sp] "+s"(spins)
    : [voff] "v"(voff), [voff2] "v"(voff2), [ws] "s"(ws), [ws2] "s"(ws2), [wb] "i"(WB), [cap] "i"(SPIN_CAP), [pm] "s"(PMASK), [lw] "v"(ldsw), [lo] "i"(L_RED3 - L_RED) : "memory", "vcc", "scc");
}
#define PACK_STORE \
  "s_nop 1\n\t" \
  "v_mov_b32_dpp v53, v52 quad_perm:[1,0,3,2] row_mask:0xf bank_mask:0xf\n\t" \
  "v_cvt_pk_bf16_f32 v54, v52, v53\n\t" \
  "v_min_u32 v54, -2, v54\n\t" \
  "s_nop 1\n\t" \
  "v_mov_b32_dpp v55, v54 row_shl:2 row_mask:0xf bank_mask:0xf\n\t" \
  "v_mov_b32_dpp v56, v54 row_shl:4 row_mask:0xf bank_mask:0xf\n\t" \
  "v_mov_b32_dpp v57, v54 row_shl:6 row_mask:0xf bank_mask:0xf\n\t" \
  "s_mov_b64 exec, %[c8z]\n\t" \
  "global_store_dwordx4 %[so], v[54:57], %[ws] sc1\n\t" \
  "s_mov_b64 exec, -1\n\t" \
  "s_nop 1\n\t"
template <bool XS>
__device__ __forceinline__ void seg_final2(u32 ldsr, u32 xsr, u32 so, const char* ws) {
  constexpr u32 LO = XS ? L_RED3 - L_RED : 0;
  if constexpr (XS) {
    asm volatile(
      "ds_read_b128 v[52:55], %[lr] offset:%c[lo]\n\t"
      "ds_read_b128 v[56:59], %[lr] offset:%c[lo]+16\n\t"
      "ds_read_b32 v60, %[xr]\n\t"
      "s_waitcnt lgkmcnt(1)\n\t"
      "v_pk_add_f32 v[52:53], v[52:53], v[56:57]\n\t"
      "v_pk_add_f32 v[54:55], v[54:55], v[58:59]\n\t"
      "v_pk_add_f32 v[52:53], v[52:53], v[54:55]\n\t"
      "s_waitcnt lgkmcnt(0)\n\t"
      "v_add_f32 v52, v52, v53\n\t"
      "v_add_f32 v52, v52, v60\n\t"
      PACK_STORE
      :: [lr] "v"(ldsr), [xr] "v"(xsr), [so] "v"(so), [ws] "s"(ws), [c8z] "s"(C8Z), [lo] "i"(LO) : "memory");
  } else {
    asm volatile(
      "ds_read_b128 v[52:55], %[lr]\n\t"
      "ds_read_b128 v[56:59], %[lr] offset:16\n\t"
      "s_waitcnt lgkmcnt(0)\n\t"
      "v_pk_add_f32 v[52:53], v[52:53], v[56:57]\n\t"
      "v_pk_add_f32 v[54:55], v[54:55], v[58:59]\n\t"
      "v_pk_add_f32 v[52:53], v[52:53], v[54:55]\n\t"
      "v_add_f32 v52, v52, v53\n\t"
      PACK_STORE
      :: [lr] "v"(ldsr), [so] "v"(so), [ws] "s"(ws), [c8z] "s"(C8Z) : "memory");
  }
}
template <int LOFF>
__device__ __forceinline__ void seg_qpoll(u32 voff, const char* ws, u32 ldsw, u32& spins) {
  asm volatile(
    SNOP "Lp%=:\n\t"
    "global_load_dwordx4 v[52:55], %[voff], %[ws] sc1\n\t"
    "s_add_u32 %[sp], %[sp], 1\n\t"
    "s_waitcnt vmcnt(0)\n\t"
    "v_max3_u32 v56, v52, v53, v54\n\t"
    "v_max_u32 v56, v56, v55\n\t"
    "v_cmp_eq_u32 vcc, -1, v56\n\t"
    "ds_write_b128 %[lw], v[52:55] offset:%c[lo]\n\t"
    "s_cbranch_vccz Lk%=\n\t"
    "s_cmp_lt_u32 %[sp], %[cap]\n\t"
    "s_cbranch_scc1 Lp%=\n\t"
    "Lk%=:\n\t"
    : [sp] "+s"(spins) : [voff] "v"(voff), [ws] "s"(ws), [lw] "v"(ldsw), [cap] "i"(SPIN_CAP), [lo] "i"(LOFF) : "memory", "vcc", "scc");
}
template <int TO>
__device__ __forceinline__ void seg_kpre(u32 ka0, u32 ka1) {
  asm volatile(
    "ds_read_b128 v[48:51], %0 offset:%c2\n\tds_read_b128 v[64:67], %1 offset:%c2\n\t"
    "ds_read_b128 v[52:55], %0 offset:%c2+512\n\tds_read_b128 v[68:71], %1 offset:%c2+512\n\t"
    "ds_read_b128 v[56:59], %0 offset:%c2+1024\n\tds_read_b128 v[72:75], %1 offset:%c2+1024\n\t"
    "ds_read_b128 v[60:63], %0 offset:%c2+1536\n\tds_read_b128 v[76:79], %1 offset:%c2+1536\n\t"
    "v_mov_b32 v96, 0\n\tv_mov_b32 v97, 0\n\tv_mov_b32 v98, 0\n\tv_mov_b32 v99, 0\n\t"
    "v_mov_b32 v100, 0\n\tv_mov_b32 v101, 0\n\tv_mov_b32 v102, 0\n\tv_mov_b32 v103, 0\n\t"
    "v_mov_b32 v104, 0\n\tv_mov_b32 v105, 0\n\tv_mov_b32 v106, 0\n\tv_mov_b32 v107, 0\n\t"
    "v_mov_b32 v108, 0\n\tv_mov_b32 v109, 0\n\tv_mov_b32 v110, 0\n\tv_mov_b32 v111, 0\n\t"
    :: "v"(ka0), "v"(ka1), "i"(TO) : "memory");
}
#define QK(s, k, q, c) "v_mfma_f32_16x16x32_bf16 v[" #s ":" #s "+3], v[" #k ":" #k "+3], v[" #q ":" #q "+3], " c "\n\t"
#define TR(d, a, off) "ds_read_b64_tr_b16 v[" #d ":" #d "+1], %[" #a "] offset:%c[to]+" #off "\n\t"
#define PV(r) "v_mfma_f32_16x16x32_bf16 v[" #r ":" #r "+3], v[124:127], v[" #r ":" #r "+3], 0\n\t"
#define SM(r) "v_fma_f32 v" #r ", v" #r ", v99, v121\n\tv_exp_f32 v" #r ", v" #r "\n\t"
#define PVA(acc, a, v, c) "v_mfma_f32_16x16x32_bf16 v[" #acc ":" #acc "+3], v[" #a ":" #a "+3], v[" #v ":" #v "+3], " c "\n\t"
#define PCP(d, s, ctl, bank) "v_mov_b32_dpp v" #d ", v" #s " " ctl " row_mask:0xf bank_mask:" bank "\n\t"
template <int TO>
__device__ __forceinline__ void seg_attn(u32 qa, u32 sta, u32 va0, u32 va1, u32 va2, u32 va3, u32 oa, u32 mla) {
  asm volatile(
    "ds_read_b128 v[80:83], %[qa]\n\t"
    "ds_read_b128 v[84:87], %[qa] offset:64\n\t"
    "ds_read_b128 v[88:91], %[qa] offset:128\n\t"
    "ds_read_b128 v[92:95], %[qa] offset:192\n\t"
    "ds_read_b64 v[120:121], %[sta]\n\t"
    "s_waitcnt lgkmcnt(4)\n\t"
    QK(112, 48, 80, "0") QK(116, 64, 80, "0")
    "s_waitcnt lgkmcnt(3)\n\t"
    QK(112, 52, 84, "v[112:115]") QK(116, 68, 84, "v[116:119]")
    "s_waitcnt lgkmcnt(2)\n\t"
    QK(112, 56, 88, "v[112:115]") QK(116, 72, 88, "v[116:119]")
    "s_waitcnt lgkmcnt(0)\n\t"
    QK(112, 60, 92, "v[112:115]")
    "v_add_f32_dpp v120, v120, v120 quad_perm:[1,0,3,2] row_mask:0xf bank_mask:0xf\n\t"
    "v_add_f32_dpp v121, v121, v121 quad_perm:[1,0,3,2] row_mask:0xf bank_mask:0xf\n\t"
    QK(116, 76, 92, "v[116:119]")
    TR(48, va0, 0)
    "v_add_f32_dpp v120, v120, v120 quad_perm:[2,3,0,1] row_mask:0xf bank_mask:0xf\n\t"
    "v_add_f32_dpp v121, v121, v121 quad_perm:[2,3,0,1] row_mask:0xf bank_mask:0xf\n\t"
    TR(50, va1, 0) TR(52, va2, 0)
    "v_add_f32_dpp v120, v120, v120 row_half_mirror row_mask:0xf bank_mask:0xf\n\t"
    "v_add_f32_dpp v121, v121, v121 row_half_mirror row_mask:0xf bank_mask:0xf\n\t"
    TR(54, va3, 0)
    "v_mul_f32 v122, 0xbb000000, v120\n\t"
    "v_mul_f32 v123, 0x3b000000, v121\n\t"
    "v_fma_f32 v123, -v122, v122, v123\n\t"
    "v_fmac_f32_dpp v112, v112, v122 row_shl:4 row_mask:0xf bank_mask:0xf bound_ctrl:1\n\t"
    "v_max_f32 v123, 0, v123\n\t"
    "v_fmac_f32_dpp v113, v113, v122 row_shl:4 row_mask:0xf bank_mask:0xf bound_ctrl:1\n\t"
    "v_add_f32 v123, 0x3727c5ac, v123\n\t"
    "v_fmac_f32_dpp v114, v114, v122 row_shl:4 row_mask:0xf bank_mask:0xf bound_ctrl:1\n\t"
    "v_rsq_f32 v123, v123\n\t"
    "v_fmac_f32_dpp v115, v115, v122 row_shl:4 row_mask:0xf bank_mask:0xf bound_ctrl:1\n\t"
    "v_fmac_f32_dpp v116, v116, v122 row_shl:4 row_mask:0xf bank_mask:0xf bound_ctrl:1\n\t"
    "v_max3_f32 v120, v112, v113, v114\n\t"
    "v_fmac_f32_dpp v117, v117, v122 row_shl:4 row_mask:0xf bank_mask:0xf bound_ctrl:1\n\t"
    "v_fmac_f32_dpp v118, v118, v122 row_shl:4 row_mask:0xf bank_mask:0xf bound_ctrl:1\n\t"
    "v_fmac_f32_dpp v119, v119, v122 row_shl:4 row_mask:0xf bank_mask:0xf bound_ctrl:1\n\t"
    "v_max3_f32 v120, v120, v115, v116\n\t"
    "v_mul_f32 v123, %[c2], v123\n\t"
    "v_max3_f32 v120, v120, v117, v118\n\t"
    "v_max_f32 v120, v120, v119\n\t"
    "v_mov_b32 v121, v120\n\t"
    TR(56, va0, 512) TR(58, va1, 512)
    "v_permlane16_swap_b32 v120, v121\n\t"
    "v_max_f32 v120, v120, v121\n\t"
    "v_mov_b32 v121, v120\n\t"
    TR(60, va2, 512) TR(62, va3, 512)
    "v_permlane32_swap_b32 v120, v121\n\t"
    "v_max_f32 v120, v120, v121\n\t"
    "v_mul_f32_e64 v121, -v120, v123\n\t"
    "v_mul_f32 v122, v120, v123\n\t"
    "v_fma_f32 v112, v112, v123, v121\n\t"
    "v_fma_f32 v113, v113, v123, v121\n\t"
    "v_exp_f32 v112, v112\n\t"
    TR(64, va0, 1024)
    "v_fma_f32 v114, v114, v123, v121\n\t"
    "v_exp_f32 v113, v113\n\t"
    TR(66, va1, 1024)
    "v_fma_f32 v115, v115, v123, v121\n\t"
    "v_exp_f32 v114, v114\n\t"
    TR(68, va2, 1024)
    "v_fma_f32 v116, v116, v123, v121\n\t"
    "v_exp_f32 v115, v115\n\t"
    TR(70, va3, 1024)
    "v_fma_f32 v117, v117, v123, v121\n\t"
    "v_exp_f32 v116, v116\n\t"
    "v_cvt_pk_bf16_f32 v124, v112, v113\n\t"
    "v_fma_f32 v118, v118, v123, v121\n\t"
    "v_exp_f32 v117, v117\n\t"
    TR(72, va0, 1536)
    "v_fma_f32 v119, v119, v123, v121\n\t"
    "v_exp_f32 v118, v118\n\t"
    "v_cvt_pk_bf16_f32 v125, v114, v115\n\t"
    "v_exp_f32 v119, v119\n\t"
    TR(74, va1, 1536) TR(76, va2, 1536) TR(78, va3, 1536)
    "v_cvt_pk_bf16_f32 v126, v116, v117\n\t"
    "v_cvt_pk_bf16_f32 v127, v118, v119\n\t"
    PCP(96, 124, "quad_perm:[0,1,2,3]", "0x1") PCP(100, 124, "row_shr:4", "0x2") PCP(104, 124, "quad_perm:[0,1,2,3]", "0x4") PCP(108, 124, "row_shr:4", "0x8")
    PCP(97, 125, "quad_perm:[0,1,2,3]", "0x1") PCP(101, 125, "row_shr:4", "0x2") PCP(105, 125, "quad_perm:[0,1,2,3]", "0x4") PCP(109, 125, "row_shr:4", "0x8")
    PCP(98, 126, "quad_perm:[0,1,2,3]", "0x1") PCP(102, 126, "row_shr:4", "0x2") PCP(106, 126, "quad_perm:[0,1,2,3]", "0x4") PCP(110, 126, "row_shr:4", "0x8")
    PCP(99, 127, "quad_perm:[0,1,2,3]", "0x1") PCP(103, 127, "row_shr:4", "0x2") PCP(107, 127, "quad_perm:[0,1,2,3]", "0x4") PCP(111, 127, "row_shr:4", "0x8")
    "v_mov_b32 v84, %[ones]\n\tv_mov_b32 v85, %[ones]\n\tv_mov_b32 v86, %[ones]\n\tv_mov_b32 v87, %[ones]\n\t"
    "s_waitcnt lgkmcnt(0)\n\t"
    PVA(112, 96, 48, "0") PVA(116, 96, 64, "0")
    PVA(112, 100, 52, "v[112:115]") PVA(116, 100, 68, "v[116:119]")
    PVA(112, 104, 56, "v[112:115]") PVA(116, 104, 72, "v[116:119]")
    PVA(112, 108, 60, "v[112:115]") PVA(116, 108, 76, "v[116:119]")
    "v_mfma_f32_16x16x32_bf16 v[80:83], v[84:87], v[124:127], 0\n\t"
    "s_nop 6\n\t"
    "ds_write_b128 %[oa], v[112:115]\n\t"
    "ds_write_b128 %[oa], v[116:119] offset:1024\n\t"
    "s_mov_b64 exec, 0xf\n\t"
    "ds_write2_b32 %[mla], v122, v80 offset0:0 offset1:16\n\t"
    "s_mov_b64 exec, -1\n\t"
    :: [qa] "v"(qa), [sta] "v"(sta), [va0] "v"(va0), [va1] "v"(va1), [va2] "v"(va2), [va3] "v"(va3), [oa] "v"(oa), [mla] "v"(mla),
       [c2] "s"(C2), [ones] "s"(ONES), [to] "i"(TO) : "memory");
}
__device__ __forceinline__ void seg_merge(u32 mla, u32 oa, u32 so, const char* ws) {
  asm volatile(
    "ds_read_b128 v[48:51], %[mla]\n\t"
    "ds_read_b128 v[60:63], %[mla] offset:64\n\t"
    "ds_read2st64_b32 v[56:57], %[oa] offset0:0 offset1:8\n\t"
    "ds_read2st64_b32 v[58:59], %[oa] offset0:16 offset1:24\n\t"
    "s_waitcnt lgkmcnt(3)\n\t"
    "v_max3_f32 v52, v48, v49, v50\n\t"
    "v_max_f32 v52, v52, v51\n\t"
    "v_sub_f32 v48, v48, v52\n\t"
    "v_sub_f32 v49, v49, v52\n\t"
    "v_exp_f32 v48, v48\n\t"
    "v_sub_f32 v50, v50, v52\n\t"
    "v_exp_f32 v49, v49\n\t"
    "v_sub_f32 v51, v51, v52\n\t"
    "v_exp_f32 v50, v50\n\t"
    "v_exp_f32 v51, v51\n\t"
    "s_waitcnt lgkmcnt(0)\n\t"
    "v_pk_mul_f32 v[60:61], v[60:61], v[48:49]\n\t"
    "v_pk_mul_f32 v[56:57], v[56:57], v[48:49]\n\t"
    "v_pk_fma_f32 v[60:61], v[62:63], v[50:51], v[60:61]\n\t"
    "v_pk_fma_f32 v[56:57], v[58:59], v[50:51], v[56:57]\n\t"
    "v_add_f32 v60, v60, v61\n\t"
    "v_add_f32 v56, v56, v57\n\t"
    "v_rcp_f32 v60, v60\n\t"
    "s_nop 0\n\t"
    "v_mul_f32 v52, v56, v60\n\t"
    PACK_STORE
    :: [mla] "v"(mla), [oa] "v"(oa), [so] "v"(so), [ws] "s"(ws), [c8z] "s"(C8Z) : "memory");
}

__device__ __forceinline__ void dma16(u32 voff, const char* sbase, u32 lds_dst) {
  u32 keep;
  asm volatile(SNOP "s_mov_b32 %0, m0\n\ts_mov_b32 m0, %3\n\ts_nop 0\n\tglobal_load_lds_dwordx4 %1, %2" NTKV "\n\ts_mov_b32 m0, %0"
               : "=&s"(keep) : "v"(voff), "s"(sbase), "s"(lds_dst) : "memory");
}
__device__ __forceinline__ void kv_load(const char* base, int w, int lane, u32 ldsb) {
  asm volatile("" : "+s"(base));
  const int r8 = (lane & 31) >> 2;
#pragma unroll
  for (int i = 0; i < 4; ++i) {
    const u32 ch = 8 * (i & 1) + 4 * (lane >> 5) + ((lane & 3) ^ ((2 * (i >> 1) + (r8 >> 2)) & 3));
    dma16(r8 * 256 + ch * 16, base + (size_t)(16 * w + 8 * (i >> 1)) * 256, ldsb + w * 4096 + i * 1024);
  }
}
__device__ __forceinline__ bool has_sent(v4u x) {
  const u32 m = __builtin_elementwise_max(__builtin_elementwise_max(x[0], x[1]), __builtin_elementwise_max(x[2], x[3]));
  return __builtin_amdgcn_ballot_w64(m == SENT) != 0ull;
}
__device__ __forceinline__ v4u poll128(const char* base, u32 voff) {
  v4u x;
  for (u32 spins = 0;; ++spins) {
    asm volatile("s_nop 4\n\tglobal_load_dwordx4 %0, %1, %2 sc1\n\ts_waitcnt vmcnt(0)" : "=&v"(x) : "v"(voff), "s"(base) : "memory");
    if (!has_sent(x) || spins > SPIN_CAP) break;
  }
  return x;
}
__device__ __forceinline__ v4u pack8(float v) {
  const u32 x = safe(pk(v, dpp<XOR1>(v)));
  return v4u{x, dppu<0x102>(x), dppu<0x104>(x), dppu<0x106>(x)};
}
__device__ __forceinline__ void sum_partials(v4u x, float (&v)[8]) {
#pragma unroll
  for (int m = 0; m < 4; ++m) { v[2 * m] = bflo(x[m]); v[2 * m + 1] = bfhi(x[m]); }
#pragma unroll
  for (int j = 0; j < 8; ++j) v[j] += dpp<XOR1>(v[j]);
#pragma unroll
  for (int j = 0; j < 8; ++j) v[j] += dpp<XOR2>(v[j]);
#pragma unroll
  for (int j = 0; j < 8; ++j) v[j] += dpp<HMIR>(v[j]);
}
__device__ __forceinline__ void x_stats(v4u xa, char* lds, int w, int lane) {
  const bool p = (lane >> 3) & 1; const int kq = lane >> 4;
  const v4f gm = mfma(xa, xa, v4f{0.f, 0.f, 0.f, 0.f});
  const v4f sm = mfma(xa, v4u{ONES, ONES, ONES, ONES}, v4f{0.f, 0.f, 0.f, 0.f});
  float s2 = (gm[0] + gm[1]) + (gm[2] + gm[3]);
  s2 = (p == (bool)(kq >> 1)) ? s2 : 0.f;
  s2 += dpp<XOR1>(s2); s2 += dpp<XOR2>(s2); s2 += dpp<HMIR>(s2); s2 += dpp<ROR8>(s2);
  s2 = x32_add(x16_add(s2));
  float s1 = (sm[0] + sm[1]) + (sm[2] + sm[3]);
  s1 = x32_add(x16_add(s1));
  if (lane == 0) *(v2f*)(lds + L_ST + w * 8) = v2f{s1, s2};
}
}

#define ST(i) do {} while (0)
#define STFLUSH(sv, base) do {} while (0)
#define SGPR_ATTR

__device__ __forceinline__ u32 lb_st() { return L_ST; }
#define OPAQUE_LANE() int lane = lane0; asm volatile("" : "+v"(lane)); const int c8 = lane & 7, kq = lane >> 4; const bool p = (lane >> 3) & 1; (void)c8; (void)p; (void)kq
#define OPAQUE_S(x) asm volatile("" : "+s"(x))

constexpr int FW = 4;

struct Addr {
  u32 vB, vA, vL, lw, so;
  u32 mr, orr;
  u32 r0, r1, r2, r3, r4, r5, r6, r7, r8;
};
#define PIN(x) asm volatile("" : "+v"(x))

template <int TYPE>
__device__ __forceinline__ void layer(const int rep, char* ws, char* lds, int b, int g, int s, int w, const int lane0, v4u xraw, Addr& A, u32 qaT,
                                      const char* nkc, const char* nvc, const char* nwq, const char* cwo, const char* nwo, const bool lead,
                                      const u32 tag, int& stampv, u32 (&cnt)[3]) {
  constexpr int RQ = TYPE ? R_WQB : R_WQA, RO = TYPE ? R_WOB : R_WOA;
  const int L = rep * 2 + TYPE;
  OPAQUE_S(b); OPAQUE_S(g); OPAQUE_S(s); OPAQUE_S(w);
  const u32 grp = (u32)(b * 8 + g), cu = grp * 8 + s;
#define LD_WQ(R, ptr) do { OPAQUE_LANE(); OPAQUE_S(ptr); bulk8<R, 8192>(ptr + (size_t)(64 * w) * 8192 + (512 * g + 64 * s) * 2, (u32)(lane >> 3) * 65536 + c8 * 16); } while (0)
#define LD_WO(R, ptr) do { OPAQUE_LANE(); OPAQUE_S(ptr); bulk8<R, 1024>(ptr + (size_t)(512 * g + 64 * w) * 1024 + 128 * s, (u32)(lane >> 3) * 8192 + c8 * 16); } while (0)
  const char* wsL = ws + L * BOXL;
  const char* box = ws + WS_BOX + cu * BOX + L * BOXL;
  {
    if (rep == 0) {
      if (SCHED == 0) { if (TYPE == 0) { if (PRETOUCH && w == 7) VMWAIT(13); else VMWAIT(8); } else VMWAIT(0); }
      else { if (TYPE == 0) { if (!lead) VMWAIT(4); } else { VMWAIT(4); } }
      if (w >= 4) ST(7);
      transpose8<RQ>();
    }
    if (L > 0) {
      const char* wsP = wsL - BOXL;
      if (DLY_B && rep) __builtin_amdgcn_s_sleep(DLY_B);
      ST(0);
      u32 sp = 0;
      seg_p1<RQ, true, 0>(A.vB, wsP, A.lw, sp);
      cnt[0] += sp;
    } else {
      const v4u xa = (lane0 & 7) == 0 ? xraw : v4u{0u, 0u, 0u, 0u};
      asm volatile("v_mov_b32 v48, %0\n\tv_mov_b32 v49, %1\n\tv_mov_b32 v50, %2\n\tv_mov_b32 v51, %3" :: "v"(xa[0]), "v"(xa[1]), "v"(xa[2]), "v"(xa[3]));
      u32 sp = 0;
      seg_p1<RQ, false, 0>(0, ws, A.lw, sp);
    }
    ST(2);
    if (rep == 0 && TYPE == 1) {
      OPAQUE_LANE();
      if (SCHED == 0) kv_load(nkc, w, lane, L_K + 32768);
      kv_load(nvc, w, lane, L_V + 32768);
    }
    v4u xa;
    asm volatile("v_mov_b32 %0, v48\n\tv_mov_b32 %1, v49\n\tv_mov_b32 %2, v50\n\tv_mov_b32 %3, v51" : "=v"(xa[0]), "=v"(xa[1]), "=v"(xa[2]), "=v"(xa[3]));
    if (rep == 0) {
      asm volatile("v_mov_b32 v48, %0\n\tv_mov_b32 v49, %0\n\tv_mov_b32 v50, %0\n\tv_mov_b32 v51, %0" :: "s"(ONES));
      u32 sp = 0;
      seg_p1<RQ, false, L_REDC - L_RED>(0, ws, A.lw, sp);
    }
    BAR();
    ST(3);
    if (w == FW) {
      seg_final2<false>(A.r2, 0, A.so, box + BOX_Q);
      ST(4);
      OPAQUE_LANE();
      if (rep == 0) {
        const __amdgpu_buffer_rsrc_t rs = __builtin_amdgcn_make_buffer_rsrc(ws, 0, WS_END, 0x00020000);
        const v4f r0 = *(const v4f*)(lds + L_REDC + lane * 32), r1 = *(const v4f*)(lds + L_REDC + lane * 32 + 16);
        const float cs = 0.125f * (((r0[0] + r0[1]) + (r0[2] + r0[3])) + ((r1[0] + r1[1]) + (r1[2] + r1[3])));
        const v4u pc = pack8(cs);
        if (c8 == 0) __builtin_amdgcn_raw_buffer_store_b128(pc, rs, WS_BOX + cu * BOX + BOX_CS + TYPE * 128 + lane * 2, 0, 16);
      }
      x_stats(xa, lds, w, lane);
      if (L == 0) {
        const u64 flag = (u64)tag | ((u64)(~tag) << 32);
        for (u32 spins = 0;; ++spins) {
          const u64 f = __hip_atomic_load((GAS u64*)(ws + WS_RDY + b * 512 + lane * 8), __ATOMIC_RELAXED, __HIP_MEMORY_SCOPE_AGENT);
          if (__all(f == flag) || spins > SPIN_CAP) break;
        }
        if (g == 0 && s == 0 && lane == 0) __hip_atomic_store((GAS u32*)(ws + WS_EPOCH + b * 256), tag, __ATOMIC_RELAXED, __HIP_MEMORY_SCOPE_AGENT);
      }
      if (DLY_C) __builtin_amdgcn_s_sleep(DLY_C);
      u32 sp = 0;
      seg_qpoll<0>(A.r0, wsL, A.r1, sp);
      cnt[1] += sp;
      if (rep == 0) { sp = 0; seg_qpoll<L_CSI + TYPE * 1088 - L_Q>(A.r0, ws + (BOX_CS + TYPE * 128 - BOX_Q), A.r1, sp); }
      ST(5);
    } else {
      OPAQUE_LANE();
      x_stats(xa, lds, w, lane);
    }
    if (g == 0 && w == s) {
      OPAQUE_LANE();
      float v[8];
      sum_partials(xa, v);
      if (c8 == 0) {
        float* xs = (float*)(lds + L_XS) + 16 * kq + (p ? 8 : 0);
        *(v4f*)xs = v4f{v[0], v[1], v[2], v[3]};
        *(v4f*)(xs + 4) = v4f{v[4], v[5], v[6], v[7]};
      }
    }
    if (rep == 0) {
      if (SCHED == 1 && TYPE == 0 && lead) VMWAIT(16); else VMWAIT(0);
      if (w >= 4) ST(8);
      BAR();
      if (SCHED == 0) LD_WO(RO, cwo);
      else {
        if (!lead) LD_WO(RO, cwo);
        else if (TYPE == 0) LD_WO(R_WOB, nwo);
      }
    }
  }
  if (w < 4) {
    seg_kpre<TYPE * 32768>(A.r0, A.r1);
    ST(6);
    BAR();
    ST(7);
    seg_attn<TYPE * 32768>(qaT, A.r6, A.r2, A.r3, A.r4, A.r5, A.r7, A.r8);
    ST(8);
  } else {
    BAR();
  }
  {
    BAR();
    seg_merge(A.mr, A.orr, A.so, box + w * 128);
    if (w == 7) {
      OPAQUE_LANE();
      const __amdgpu_buffer_rsrc_t rs = __builtin_amdgcn_make_buffer_rsrc(ws, 0, WS_END, 0x00020000);
      const int hh = (lane >> 2) & 3, aa = lane & 3;
      const float m1 = ((const float*)(lds + L_ML))[hh * 4 + aa], l1 = ((const float*)(lds + L_ML))[16 + hh * 4 + aa];
      float M1 = __builtin_fmaxf(m1, dpp<XOR1>(m1)); M1 = __builtin_fmaxf(M1, dpp<XOR2>(M1));
      float L1 = __builtin_amdgcn_exp2f(m1 - M1) * l1; L1 += dpp<XOR1>(L1); L1 += dpp<XOR2>(L1);
      const u32 e = safe(__float_as_uint(M1 + __builtin_amdgcn_logf(L1)));
      const v4u ev = {dppu<0x150>(e), dppu<0x154>(e), dppu<0x158>(e), dppu<0x15C>(e)};
      if (lane < 8) __builtin_amdgcn_raw_buffer_store_b128(ev, rs, WS_LSR + ((L * 32 + grp) * 8 + lane) * 128 + s * 16, 0, 16);
    }
    ST(9);
  }
  {
    const char* wsS = ws + L * 32768;
    if (rep == 0) {
      if (SCHED == 1 && TYPE == 0 && lead) VMWAIT(8); else VMWAIT(0);
      transpose8<RO>();
    } else {
      if (DLY_A) __builtin_amdgcn_s_sleep(DLY_A);
    }
    ST(1);
    u32 sp = 0;
    seg_p3<RO>(A.vA, A.vL, wsL, wsS, A.lw, sp);
    cnt[2] += sp;
    ST(10);
    if (rep == 0 && TYPE == 0) {
      if (SCHED == 0) LD_WQ(R_WQB, nwq);
      else {
        if (!lead) LD_WQ(R_WQB, nwq);
        OPAQUE_LANE();
        kv_load(nkc, w, lane, L_K + 32768);
      }
    }
    BAR();
    if (w == FW) seg_final2<true>(A.r2, A.r3, A.so, box + BOX_X);
    ST(11);
  }
  STFLUSH(stampv, L * 12);
}

__global__ void __launch_bounds__(512, 2) __attribute__((amdgpu_num_vgpr(24) SGPR_ATTR))
fused_kernel(const char* __restrict__ xin, const char* kA, const char* vA, const char* wqA, const char* woA,
             const char* kB, const char* vB, const char* wqB, const char* woB, unsigned short* out, char* ws) {
  extern __shared__ __attribute__((aligned(16))) char lds[];
  const int id = blockIdx.x, g = id & 7, b = id >> 6, s = (id >> 3) & 7;
  const int tid = threadIdx.x, lane = tid & 63, w = __builtin_amdgcn_readfirstlane(tid >> 6);
  const int c8 = lane & 7, kq = lane >> 4;
  const bool p = (lane >> 3) & 1;
  int stampv = 0;
  const __amdgpu_buffer_rsrc_t rs = __builtin_amdgcn_make_buffer_rsrc(ws, 0, WS_END, 0x00020000);
  const u32 cu = (u32)((b * 8 + g) * 8 + s);
  asm volatile("" ::: "v127", "v255");
  u32 ep; v4u xraw;
  asm volatile("s_nop 4\n\tglobal_load_dword v56, %0, %1 sc1\n\tglobal_load_dwordx4 v[52:55], %2, %3"
               :: "v"(WS_EPOCH + b * 256), "s"(ws), "v"((u32)((b * 512 + 64 * w + 16 * kq + (p ? 8 : 0)) * 2)), "s"(xin) : "memory");
  if (w == 5) {
    const v4u sv = {SENT, SENT, SENT, SENT};
#pragma unroll
    for (int i = 0; i < 5; ++i) __builtin_amdgcn_raw_buffer_store_b128(sv, rs, WS_BOX + cu * BOX + i * 1024 + lane * 16, 0, 16);
    const u32 o6 = lane < 16 ? WS_BOX + cu * BOX + 5120 + lane * 16
                             : WS_LSR + (u32)(((((lane - 16) >> 3) * 32 + b * 8 + g) * 8 + (lane & 7)) * 128 + s * 16);
    if (lane < 48) __builtin_amdgcn_raw_buffer_store_b128(sv, rs, o6, 0, 16);
  }
  if (w == 6) ((float*)(lds + L_XS))[lane] = 0.f;
  const size_t kvoff = ((size_t)(b * 8 + g) * SK + (SK - WIN) + s * 128) * 256;
  const bool lead = (w >> 1) == b;
  const size_t qoff = (size_t)(64 * w) * 8192 + (512 * g + 64 * s) * 2, ooff = (size_t)(512 * g + 64 * w) * 1024 + 128 * s;
  const u32 vq = (u32)(lane >> 3) * 65536 + c8 * 16, vo = (u32)(lane >> 3) * 8192 + c8 * 16;
  if (SCHED == 0 || lead) {
    bulk8<R_WQA, 8192>(wqA + qoff, vq);
    VMWAIT(8);
  } else {
    VMWAIT(0);
  }
  asm volatile("v_mov_b32 %0, v52\n\tv_mov_b32 %1, v53\n\tv_mov_b32 %2, v54\n\tv_mov_b32 %3, v55\n\tv_mov_b32 %4, v56"
               : "=v"(xraw[0]), "=v"(xraw[1]), "=v"(xraw[2]), "=v"(xraw[3]), "=v"(ep));
  ST(6);
  const u32 tag = (u32)__builtin_amdgcn_readfirstlane((int)ep) + 1u;
  if (w == 5 && lane == 0) {
    const u64 flag = (u64)tag | ((u64)(~tag) << 32);
    __hip_atomic_store((GAS u64*)(ws + WS_RDY + b * 512 + (s * 8 + g) * 8), flag, __ATOMIC_RELAXED, __HIP_MEMORY_SCOPE_AGENT);
  }
  if (SCHED == 0) {
    asm volatile("s_barrier" ::: "memory");
    kv_load(kA + kvoff, w, lane, L_K);
    kv_load(vA + kvoff, w, lane, L_V);
  } else {
    kv_load(kA + kvoff, w, lane, L_K);
    if (lead) VMWAIT(4);
    asm volatile("s_barrier" ::: "memory");
    if (!lead) bulk8<R_WQA, 8192>(wqA + qoff, vq);
    kv_load(vA + kvoff, w, lane, L_V);
    if (lead) { bulk8<R_WOA, 1024>(woA + ooff, vo); bulk8<R_WQB, 8192>(wqB + qoff, vq); }
  }
  if (PRETOUCH && w == 7) {
    const size_t kvo = ((size_t)(b * 8 + g) * SK + (SK - WIN) + s * 128) * 256;
    asm volatile("s_nop 4\n\tglobal_load_dword v100, %0, %2\n\tglobal_load_dword v101, %1, %3\n\tglobal_load_dword v102, %1, %4\n\t"
                 "global_load_dword v103, %5, %6\n\tglobal_load_dword v104, %5, %7"
                 :: "v"((u32)(lane * 65536 + (512 * g + 64 * s) * 2)), "v"((u32)((512 * g + lane * 8) * 1024 + 128 * s)), "s"(wqB), "s"(woA), "s"(woB),
                    "v"((u32)(lane * 512)), "s"(kB + kvo), "s"(vB + kvo) : "memory");
  }
  Addr A; u32 qa0, qa1;
  {
    const u32 grp = (u32)(b * 8 + g);
    const u32 oct = kq * 32 + (p ? 16 : 0);
    A.vB = WS_BOX + (u32)((b * 8 + c8) * 8 + w) * BOX + BOX_X + oct;
    A.vA = WS_BOX + (grp * 8 + c8) * BOX + w * 128 + oct;
    A.vL = WS_LSR + (grp * 8 + s) * 128 + c8 * 16 + (w >> 1) * 4;
    A.lw = L_RED + (8 * c8 + (lane >> 3)) * 32 + w * 4;
    A.so = lane * 2;
    A.mr = L_ML + (w >> 1) * 16;
    A.orr = L_OM + (w & 1) * 1024 + lane * 16 + (w >> 1) * 4;
    const int r16 = lane & 15, q4 = r16 >> 2, p2 = lane & 3;
    qa0 = L_Q + (lane & 3) * 272 + kq * 16;
    qa1 = qa0;
    if (lane & 4) { qa0 += L_CSI - L_Q; qa1 += L_CSI + 1088 - L_Q; }
    if (w < 4) {
      const u32 kb = L_K + 2048 * (4 * w + (r16 >> 2)) + 64 * (r16 & 3);
      A.r0 = kb + 16 * (kq ^ ((2 * (r16 >> 2)) & 3));
      A.r1 = kb + 256 + 16 * (kq ^ ((2 * (r16 >> 2) + 1) & 3));
      const u32 vb = L_V + 2048 * (4 * w + kq) + 64 * q4 + 8 * (p2 & 1);
      A.r2 = vb + 16 * (((p2 >> 1)) ^ ((2 * kq) & 3));
      A.r3 = vb + 256 + 16 * (((p2 >> 1)) ^ ((2 * kq + 1) & 3));
      A.r4 = vb + 16 * ((2 + (p2 >> 1)) ^ ((2 * kq) & 3));
      A.r5 = vb + 256 + 16 * ((2 + (p2 >> 1)) ^ ((2 * kq + 1) & 3));
      A.r7 = L_OM + w * 2048 + lane * 16;
      A.r8 = L_ML + (lane * 4 + w) * 4;
    } else {
      A.r0 = WS_BOX + (grp * 8 + (lane >> 3)) * BOX + BOX_Q + c8 * 16;
      A.r1 = L_Q + kq * 272 + (lane & 15) * 16;
      A.r2 = L_RED + lane * 32;
      A.r3 = L_XS + lane * 4;
      A.r4 = A.r5 = A.r7 = A.r8 = 0;
    }
    A.r6 = L_ST + c8 * 8;
    PIN(A.r6);
    PIN(A.vB); PIN(A.vA); PIN(A.vL); PIN(A.lw); PIN(A.so); PIN(A.mr); PIN(A.orr); PIN(A.r0); PIN(A.r1); PIN(A.r2); PIN(A.r3); PIN(A.r4); PIN(A.r5); PIN(A.r7); PIN(A.r8);
    PIN(qa0); PIN(qa1);
  }
  u32 cnt[3] = {0, 0, 0};
  for (int rep = 0; rep < 2; ++rep) {
    layer<0>(rep, ws, lds, b, g, s, w, lane, xraw, A, qa0, kB + kvoff, nullptr, wqB, woA, woB, lead, tag, stampv, cnt);
    layer<1>(rep, ws, lds, b, g, s, w, lane, xraw, A, qa1, kB + kvoff, vB + kvoff, nullptr, woB, nullptr, lead, tag, stampv, cnt);
  }
  if (g == 0 && w == s) {
    const u32 voff = WS_BOX + (u32)((b * 8 + c8) * 8 + w) * BOX + 3u * BOXL + BOX_X + kq * 32 + (p ? 16 : 0);
    if (DLY_B) __builtin_amdgcn_s_sleep(DLY_B);
    const v4u x = poll128(ws, voff);
    float v[8];
    sum_partials(x, v);
    if (c8 == 0) *(v4u*)(out + b * 512 + 64 * w + 16 * kq + (p ? 8 : 0)) = v4u{pk(v[0], v[1]), pk(v[2], v[3]), pk(v[4], v[5]), pk(v[6], v[7])};
  }
}

extern "C" void kernel_launch(void* const* d_in, const int* in_sizes, int n_in, void* d_out, int out_size, void* d_ws, size_t ws_size,
                              hipStream_t stream) {
  (void)hipFuncSetAttribute((const void*)fused_kernel, hipFuncAttributeMaxDynamicSharedMemorySize, L_END);
  fused_kernel<<<256, 512, L_END, stream>>>((const char*)d_in[0], (const char*)d_in[1], (const char*)d_in[2], (const char*)d_in[3], (const char*)d_in[4],
                                            (const char*)d_in[6], (const char*)d_in[7], (const char*)d_in[8], (const char*)d_in[9],
                                            (unsigned short*)d_out, (char*)d_ws);
}
```

```cpp
#include <hip/hip_runtime.h>
#include <stdint.h>

typedef unsigned int u32;
typedef unsigned long long u64;
typedef u32 v4u __attribute__((ext_vector_type(4)));
typedef u32 v2u __attribute__((ext_vector_type(2)));
typedef float v4f __attribute__((ext_vector_type(4)));
typedef float v2f __attribute__((ext_vector_type(2)));
typedef __bf16 v8b __attribute__((ext_vector_type(8)));
typedef __bf16 v2b __attribute__((ext_vector_type(2)));
#define GAS __attribute__((address_space(1)))

#define DLY_C 2
#define DLY_A 8
#define DLY_B 10
#define SCHED 2
#define PRETOUCH 0
#define NTKV " nt"
#define NTW " nt"

namespace {
constexpr int SK = 4096, WIN = 1024;
constexpr u32 WS_EPOCH = 0;
constexpr u32 WS_RDY = 4096;
constexpr u32 WS_LSR = 8192;
constexpr u32 WS_BOX = WS_LSR + 131072;
constexpr u32 BOXL = 1280, BOX_Q = 1024, BOX_X = 1152, BOX_CS = 5120, BOX = 5376;
constexpr u32 WS_END = WS_BOX + 256 * BOX;
constexpr u32 WS_DBG = 4u << 20;
constexpr u32 L_KV = 0;
constexpr u32 L_K = 0, L_V = 65536;
constexpr u32 L_OM = 131072;
constexpr u32 L_ML = L_OM + 8192;
constexpr u32 L_Q = L_ML + 128;
constexpr u32 L_CSI = L_Q + 1088;
constexpr u32 L_RED = L_CSI + 2176;
constexpr u32 L_REDC = L_RED + 2048;
constexpr u32 L_ST = L_REDC + 2048;
constexpr u32 L_RED3 = L_ST + 64;
constexpr u32 L_XS = L_RED3 + 2048;
constexpr u32 L_END = L_XS + 256;
static_assert(L_END <= 160 * 1024, "lds");
static_assert(L_Q % 16 == 0 && L_CSI % 16 == 0 && L_RED % 16 == 0 && L_ST % 16 == 0 && L_RED3 % 16 == 0 && L_XS % 16 == 0, "align");
constexpr u32 SENT = 0xFFFFFFFFu;
constexpr u32 SPIN_CAP = 1u << 15;
constexpr float C2 = 0.08838834764831845f * 1.4426950408889634f;
constexpr u32 ONES = 0x3F803F80u;
constexpr u64 PMASK = 0xFF00FF00FF00FF00ull;
constexpr u64 C8Z = 0x0101010101010101ull;

__device__ __forceinline__ float bflo(u32 w) { return __uint_as_float(w << 16); }
__device__ __forceinline__ float bfhi(u32 w) { return __uint_as_float(w & 0xffff0000u); }
__device__ __forceinline__ u32 pk(float lo, float hi) { v2f f = {lo, hi}; return __builtin_bit_cast(u32, __builtin_convertvector(f, v2b)); }
__device__ __forceinline__ u32 safe(u32 v) { return v < 0xFFFFFFFEu ? v : 0xFFFFFFFEu; }
template <int CTRL> __device__ __forceinline__ float dpp(float x) {
  return __builtin_bit_cast(float, __builtin_amdgcn_mov_dpp(__builtin_bit_cast(int, x), CTRL, 0xf, 0xf, true));
}
template <int CTRL> __device__ __forceinline__ u32 dppu(u32 x) { return (u32)__builtin_amdgcn_mov_dpp((int)x, CTRL, 0xf, 0xf, true); }
constexpr int XOR1 = 0xB1, XOR2 = 0x4E, HMIR = 0x141, ROR8 = 0x128;
__device__ __forceinline__ float x16_add(float x) { auto s = __builtin_amdgcn_permlane16_swap(__float_as_uint(x), __float_as_uint(x), false, false); return __uint_as_float(s[0]) + __uint_as_float(s[1]); }
__device__ __forceinline__ float x32_add(float x) { auto s = __builtin_amdgcn_permlane32_swap(__float_as_uint(x), __float_as_uint(x), false, false); return __uint_as_float(s[0]) + __uint_as_float(s[1]); }
__device__ __forceinline__ v4f mfma(v4u a, v4u b, v4f c) {
  return __builtin_amdgcn_mfma_f32_16x16x32_bf16(__builtin_bit_cast(v8b, a), __builtin_bit_cast(v8b, b), c, 0, 0, 0);
}
#define BAR() asm volatile("s_waitcnt lgkmcnt(0)\n\ts_barrier" ::: "memory")
#define VMWAIT(n) asm volatile("s_waitcnt vmcnt(" #n ")" ::: "memory")

constexpr int R_WQA = 128, R_WOA = 160, R_WQB = 192, R_WOB = 224;
template <int BASE, u32 STRIDE>
__device__ __forceinline__ void bulk8(const char* base, u32 voff) {
  asm volatile("" : "+s"(base));
  const __amdgpu_buffer_rsrc_t r = __builtin_amdgcn_make_buffer_rsrc((void*)base, 0, 0x7fffffff, 0x00020000);
  asm volatile("s_nop 4\n\t"
               "buffer_load_dwordx4 v[%c10:%c11], %0, %1, 0 offen" NTW "\n\tbuffer_load_dwordx4 v[%c12:%c13], %0, %1, %3 offen" NTW "\n\t"
               "buffer_load_dwordx4 v[%c14:%c15], %0, %1, %4 offen" NTW "\n\tbuffer_load_dwordx4 v[%c16:%c17], %0, %1, %5 offen" NTW "\n\t"
               "buffer_load_dwordx4 v[%c18:%c19], %0, %1, %6 offen" NTW "\n\tbuffer_load_dwordx4 v[%c20:%c21], %0, %1, %7 offen" NTW "\n\t"
               "buffer_load_dwordx4 v[%c22:%c23], %0, %1, %8 offen" NTW "\n\tbuffer_load_dwordx4 v[%c24:%c25], %0, %1, %9 offen" NTW
               :: "v"(voff), "s"(r), "s"(0), "s"(STRIDE), "s"(2 * STRIDE), "s"(3 * STRIDE), "s"(4 * STRIDE), "s"(5 * STRIDE), "s"(6 * STRIDE), "s"(7 * STRIDE),
                  "i"(BASE), "i"(BASE + 3), "i"(BASE + 4), "i"(BASE + 7), "i"(BASE + 8), "i"(BASE + 11), "i"(BASE + 12), "i"(BASE + 15),
                  "i"(BASE + 16), "i"(BASE + 19), "i"(BASE + 20), "i"(BASE + 23), "i"(BASE + 24), "i"(BASE + 27), "i"(BASE + 28), "i"(BASE + 31)
               : "memory");
}
template <int B, int M, int R>
__device__ __forceinline__ void tp_pair(u32 slo, u32 shi) {
  if constexpr (M == R) {
    asm volatile("v_perm_b32 v48, v%c3, v%c2, %0\n\tv_perm_b32 v%c3, v%c3, v%c2, %1\n\tv_mov_b32 v%c2, v48"
                 :: "s"(slo), "s"(shi), "i"(B + 8 * M + R), "i"(B + 8 * M + 4 + R));
  } else {
    asm volatile("v_perm_b32 v48, v%c3, v%c2, %0\n\tv_perm_b32 v49, v%c3, v%c2, %1\n\t"
                 "v_perm_b32 v%c2, v%c5, v%c4, %0\n\tv_perm_b32 v%c3, v%c5, v%c4, %1\n\t"
                 "v_mov_b32 v%c4, v48\n\tv_mov_b32 v%c5, v49"
                 :: "s"(slo), "s"(shi), "i"(B + 8 * M + R), "i"(B + 8 * M + 4 + R), "i"(B + 8 * R + M), "i"(B + 8 * R + 4 + M));
  }
}
template <int B>
__device__ __forceinline__ void transpose8() {
  const u32 slo = 0x05040100u, shi = 0x07060302u;
  tp_pair<B, 0, 0>(slo, shi); tp_pair<B, 1, 1>(slo, shi); tp_pair<B, 2, 2>(slo, shi); tp_pair<B, 3, 3>(slo, shi);
  tp_pair<B, 0, 1>(slo, shi); tp_pair<B, 0, 2>(slo, shi); tp_pair<B, 0, 3>(slo, shi);
  tp_pair<B, 1, 2>(slo, shi); tp_pair<B, 1, 3>(slo, shi); tp_pair<B, 2, 3>(slo, shi);
  asm volatile("s_nop 1");
}
#define SNOP
#define MF(d, b) "v_mfma_f32_16x16x32_bf16 v[" #d ":" #d "+3], v[48:51], v[%c[wb]+" #b ":%c[wb]+" #b "+3], 0\n\t"
#define FOLD_TAIL \
  "v_permlane32_swap_b32 v52, v68\n\t" \
  "v_permlane32_swap_b32 v56, v72\n\t" \
  "v_permlane32_swap_b32 v60, v76\n\t" \
  "v_permlane32_swap_b32 v64, v80\n\t" \
  "v_cndmask_b32_e64 v52, v52, v68, %[pm]\n\t" \
  "v_cndmask_b32_e64 v60, v60, v76, %[pm]\n\t" \
  "v_cndmask_b32_e64 v56, v56, v72, %[pm]\n\t" \
  "v_cndmask_b32_e64 v64, v64, v80, %[pm]\n\t" \
  "v_permlane16_swap_b32 v52, v60\n\t" \
  "s_nop 0\n\t" \
  "v_permlane16_swap_b32 v56, v64\n\t" \
  "v_add_f32 v52, v52, v60\n\t" \
  "v_add_f32 v56, v56, v64\n\t" \
  "s_nop 0\n\t" \
  "v_add_f32_dpp v52, v52, v52 row_ror:8 row_mask:0xf bank_mask:0xf\n\t" \
  "v_add_f32_dpp v56, v56, v56 row_ror:8 row_mask:0xf bank_mask:0xf\n\t" \
  "v_cndmask_b32_e64 v52, v52, v56, %[pm]\n\t"

template <int WB, bool POLL, int LOFF>
__device__ __forceinline__ void seg_p1(u32 voff, const char* ws, u32 ldsw, u32& spins) {
  if constexpr (POLL) {
    asm volatile(
      SNOP "Lp%=:\n\t"
      "global_load_dwordx4 v[48:51], %[voff], %[ws] sc1\n\t"
      "s_add_u32 %[sp], %[sp], 1\n\t"
      "s_waitcnt vmcnt(0)\n\t"
      MF(52, 0) "v_max3_u32 v84, v48, v49, v50\n\t"
      MF(56, 4) "v_max_u32 v84, v84, v51\n\t"
      MF(60, 8) "v_cmp_eq_u32 vcc, -1, v84\n\t"
      MF(64, 12)
      "s_cbranch_vccz Lk%=\n\t"
      "s_cmp_lt_u32 %[sp], %[cap]\n\t"
      "s_cbranch_scc1 Lp%=\n\t"
      "Lk%=:\n\t"
      : [sp] "+s"(spins) : [voff] "v"(voff), [ws] "s"(ws), [wb] "i"(WB), [cap] "i"(SPIN_CAP) : "memory", "vcc", "scc");
  } else {
    asm volatile("s_nop 1\n\t" MF(52, 0) "s_nop 0\n\t" MF(56, 4) "s_nop 0\n\t" MF(60, 8) "s_nop 0\n\t" MF(64, 12) "s_nop 0\n\t" :: [wb] "i"(WB));
  }
  asm volatile(
    MF(68, 16) "v_pk_add_f32 v[52:53], v[52:53], v[54:55]\n\t"
    MF(72, 20) "v_pk_add_f32 v[56:57], v[56:57], v[58:59]\n\t"
    MF(76, 24) "v_pk_add_f32 v[60:61], v[60:61], v[62:63]\n\t"
    MF(80, 28) "v_pk_add_f32 v[64:65], v[64:65], v[66:67]\n\t"
    "v_add_f32 v52, v52, v53\n\t"
    "v_pk_add_f32 v[68:69], v[68:69], v[70:71]\n\t"
    "v_add_f32 v56, v56, v57\n\t"
    "v_pk_add_f32 v[72:73], v[72:73], v[74:75]\n\t"
    "v_add_f32 v60, v60, v61\n\t"
    "v_pk_add_f32 v[76:77], v[76:77], v[78:79]\n\t"
    "v_add_f32 v64, v64, v65\n\t"
    "v_pk_add_f32 v[80:81], v[80:81], v[82:83]\n\t"
    "v_add_f32 v68, v68, v69\n\t"
    "v_add_f32 v72, v72, v73\n\t"
    "v_add_f32 v76, v76, v77\n\t"
    "v_add_f32 v80, v80, v81\n\t"
    "s_nop 0\n\t"
    FOLD_TAIL
    "ds_write_b32 %[lw], v52 offset:%c[lo]\n\t"
    :: [wb] "i"(WB), [pm] "s"(PMASK), [lw] "v"(ldsw), [lo] "i"(LOFF) : "memory");
}
template <int WB>
__device__ __forceinline__ void seg_p3(u32 voff, u32 voff2, const char* ws, const char* ws2, u32 ldsw, u32& spins) {
  asm volatile(
    SNOP "Lp%=:\n\t"
    "global_load_dwordx4 v[48:51], %[voff], %[ws] sc1\n\t"
    "global_load_dword v84, %[voff2], %[ws2] sc1\n\t"
    "s_add_u32 %[sp], %[sp], 1\n\t"
    "s_waitcnt vmcnt(0)\n\t"
    MF(52, 0) "v_max3_u32 v85, v48, v49, v50\n\t"
    MF(56, 4) "v_max3_u32 v85, v85, v51, v84\n\t"
    MF(60, 8) "v_cmp_eq_u32 vcc, -1, v85\n\t"
    MF(64, 12)
    "s_cbranch_vccz Lk%=\n\t"
    "s_cmp_lt_u32 %[sp], %[cap]\n\t"
    "s_cbranch_scc1 Lp%=\n\t"
    "Lk%=:\n\t"
    "v_max_f32_dpp v85, v84, v84 quad_perm:[1,0,3,2] row_mask:0xf bank_mask:0xf\n\t"
    MF(68, 16)
    "s_nop 0\n\t"
    "v_max_f32_dpp v85, v85, v85 quad_perm:[2,3,0,1] row_mask:0xf bank_mask:0xf\n\t"
    MF(72, 20)
    "s_nop 0\n\t"
    "v_max_f32_dpp v85, v85, v85 row_half_mirror row_mask:0xf bank_mask:0xf\n\t"
    MF(76, 24)
    "v_sub_f32 v86, v84, v85\n\t"
    MF(80, 28)
    "v_exp_f32 v86, v86\n\t"
    "s_nop 1\n\t"
    "v_mov_b32_dpp v90, v86 row_newbcast:0 row_mask:0x5 bank_mask:0xf\n\t"
    "v_mov_b32_dpp v91, v86 row_newbcast:1 row_mask:0x5 bank_mask:0xf\n\t"
    "v_mov_b32_dpp v92, v86 row_newbcast:2 row_mask:0x5 bank_mask:0xf\n\t"
    "v_mov_b32_dpp v93, v86 row_newbcast:3 row_mask:0x5 bank_mask:0xf\n\t"
    "v_mov_b32_dpp v90, v86 row_newbcast:4 row_mask:0xa bank_mask:0xf\n\t"
    "v_mov_b32_dpp v91, v86 row_newbcast:5 row_mask:0xa bank_mask:0xf\n\t"
    "v_mov_b32_dpp v92, v86 row_newbcast:6 row_mask:0xa bank_mask:0xf\n\t"
    "v_mov_b32_dpp v93, v86 row_newbcast:7 row_mask:0xa bank_mask:0xf\n\t"
    "v_add_f32_dpp v87, v86, v86 quad_perm:[1,0,3,2] row_mask:0xf bank_mask:0xf\n\t"
    "v_pk_mul_f32 v[52:53], v[52:53], v[90:91]\n\t"
    "v_pk_mul_f32 v[56:57], v[56:57], v[90:91]\n\t"
    "v_add_f32_dpp v87, v87, v87 quad_perm:[2,3,0,1] row_mask:0xf bank_mask:0xf\n\t"
    "v_pk_mul_f32 v[60:61], v[60:61], v[90:91]\n\t"
    "v_pk_mul_f32 v[64:65], v[64:65], v[90:91]\n\t"
    "v_add_f32_dpp v87, v87, v87 row_half_mirror row_mask:0xf bank_mask:0xf\n\t"
    "v_pk_mul_f32 v[68:69], v[68:69], v[90:91]\n\t"
    "v_pk_mul_f32 v[72:73], v[72:73], v[90:91]\n\t"
    "v_rcp_f32 v87, v87\n\t"
    "v_pk_mul_f32 v[76:77], v[76:77], v[90:91]\n\t"
    "v_pk_mul_f32 v[80:81], v[80:81], v[90:91]\n\t"
    "v_pk_fma_f32 v[52:53], v[54:55], v[92:93], v[52:53]\n\t"
    "v_pk_fma_f32 v[68:69], v[70:71], v[92:93], v[68:69]\n\t"
    "v_pk_fma_f32 v[56:57], v[58:59], v[92:93], v[56:57]\n\t"
    "v_pk_fma_f32 v[72:73], v[74:75], v[92:93], v[72:73]\n\t"
    "v_pk_fma_f32 v[60:61], v[62:63], v[92:93], v[60:61]\n\t"
    "v_pk_fma_f32 v[76:77], v[78:79], v[92:93], v[76:77]\n\t"
    "v_pk_fma_f32 v[64:65], v[66:67], v[92:93], v[64:65]\n\t"
    "v_pk_fma_f32 v[80:81], v[82:83], v[92:93], v[80:81]\n\t"
    "v_add_f32 v52, v52, v53\n\t"
    "v_add_f32 v68, v68, v69\n\t"
    "v_add_f32 v56, v56, v57\n\t"
    "v_add_f32 v72, v72, v73\n\t"
    "v_add_f32 v60, v60, v61\n\t"
    "v_add_f32 v76, v76, v77\n\t"
    "v_add_f32 v64, v64, v65\n\t"
    "v_add_f32 v80, v80, v81\n\t"
    "s_nop 1\n\t"
    FOLD_TAIL
    "v_mul_f32 v52, v52, v87\n\t"
    "ds_write_b32 %[lw], v52 offset:%c[lo]\n\t"
    : [sp] "+s"(spins)
    : [voff] "v"(voff), [voff2] "v"(voff2), [ws] "s"(ws), [ws2] "s"(ws2), [wb] "i"(WB), [cap] "i"(SPIN_CAP), [pm] "s"(PMASK), [lw] "v"(ldsw), [lo] "i"(L_RED3 - L_RED) : "memory", "vcc", "scc");
}
#define PACK_STORE \
  "s_nop 1\n\t" \
  "v_mov_b32_dpp v53, v52 quad_perm:[1,0,3,2] row_mask:0xf bank_mask:0xf\n\t" \
  "v_cvt_pk_bf16_f32 v54, v52, v53\n\t" \
  "v_min_u32 v54, -2, v54\n\t" \
  "s_nop 1\n\t" \
  "v_mov_b32_dpp v55, v54 row_shl:2 row_mask:0xf bank_mask:0xf\n\t" \
  "v_mov_b32_dpp v56, v54 row_shl:4 row_mask:0xf bank_mask:0xf\n\t" \
  "v_mov_b32_dpp v57, v54 row_shl:6 row_mask:0xf bank_mask:0xf\n\t" \
  "s_mov_b64 exec, %[c8z]\n\t" \
  "global_store_dwordx4 %[so], v[54:57], %[ws] sc1\n\t" \
  "s_mov_b64 exec, -1\n\t" \
  "s_nop 1\n\t"
template <bool XS>
__device__ __forceinline__ void seg_final2(u32 ldsr, u32 xsr, u32 so, const char* ws) {
  constexpr u32 LO = XS ? L_RED3 - L_RED : 0;
  if constexpr (XS) {
    asm volatile(
      "ds_read_b128 v[52:55], %[lr] offset:%c[lo]\n\t"
      "ds_read_b128 v[56:59], %[lr] offset:%c[lo]+16\n\t"
      "ds_read_b32 v60, %[xr]\n\t"
      "s_waitcnt lgkmcnt(1)\n\t"
      "v_pk_add_f32 v[52:53], v[52:53], v[56:57]\n\t"
      "v_pk_add_f32 v[54:55], v[54:55], v[58:59]\n\t"
      "v_pk_add_f32 v[52:53], v[52:53], v[54:55]\n\t"
      "s_waitcnt lgkmcnt(0)\n\t"
      "v_add_f32 v52, v52, v53\n\t"
      "v_add_f32 v52, v52, v60\n\t"
      PACK_STORE
      :: [lr] "v"(ldsr), [xr] "v"(xsr), [so] "v"(so), [ws] "s"(ws), [c8z] "s"(C8Z), [lo] "i"(LO) : "memory");
  } else {
    asm volatile(
      "ds_read_b128 v[52:55], %[lr]\n\t"
      "ds_read_b128 v[56:59], %[lr] offset:16\n\t"
      "s_waitcnt lgkmcnt(0)\n\t"
      "v_pk_add_f32 v[52:53], v[52:53], v[56:57]\n\t"
      "v_pk_add_f32 v[54:55], v[54:55], v[58:59]\n\t"
      "v_pk_add_f32 v[52:53], v[52:53], v[54:55]\n\t"
      "v_add_f32 v52, v52, v53\n\t"
      PACK_STORE
      :: [lr] "v"(ldsr), [so] "v"(so), [ws] "s"(ws), [c8z] "s"(C8Z) : "memory");
  }
}
template <int LOFF>
__device__ __forceinline__ void seg_qpoll(u32 voff, const char* ws, u32 ldsw, u32& spins) {
  asm volatile(
    SNOP "Lp%=:\n\t"
    "global_load_dwordx4 v[52:55], %[voff], %[ws] sc1\n\t"
    "s_add_u32 %[sp], %[sp], 1\n\t"
    "s_waitcnt vmcnt(0)\n\t"
    "v_max3_u32 v56, v52, v53, v54\n\t"
    "v_max_u32 v56, v56, v55\n\t"
    "v_cmp_eq_u32 vcc, -1, v56\n\t"
    "ds_write_b128 %[lw], v[52:55] offset:%c[lo]\n\t"
    "s_cbranch_vccz Lk%=\n\t"
    "s_cmp_lt_u32 %[sp], %[cap]\n\t"
    "s_cbranch_scc1 Lp%=\n\t"
    "Lk%=:\n\t"
    : [sp] "+s"(spins) : [voff] "v"(voff), [ws] "s"(ws), [lw] "v"(ldsw), [cap] "i"(SPIN_CAP), [lo] "i"(LOFF) : "memory", "vcc", "scc");
}
template <int TO>
__device__ __forceinline__ void seg_kpre(u32 ka0, u32 ka1) {
  asm volatile(
    "ds_read_b128 v[48:51], %0 offset:%c2\n\tds_read_b128 v[64:67], %1 offset:%c2\n\t"
    "ds_read_b128 v[52:55], %0 offset:%c2+512\n\tds_read_b128 v[68:71], %1 offset:%c2+512\n\t"
    "ds_read_b128 v[56:59], %0 offset:%c2+1024\n\tds_read_b128 v[72:75], %1 offset:%c2+1024\n\t"
    "ds_read_b128 v[60:63], %0 offset:%c2+1536\n\tds_read_b128 v[76:79], %1 offset:%c2+1536\n\t"
    "v_mov_b32 v96, 0\n\tv_mov_b32 v97, 0\n\tv_mov_b32 v98, 0\n\tv_mov_b32 v99, 0\n\t"
    "v_mov_b32 v100, 0\n\tv_mov_b32 v101, 0\n\tv_mov_b32 v102, 0\n\tv_mov_b32 v103, 0\n\t"
    "v_mov_b32 v104, 0\n\tv_mov_b32 v105, 0\n\tv_mov_b32 v106, 0\n\tv_mov_b32 v107, 0\n\t"
    "v_mov_b32 v108, 0\n\tv_mov_b32 v109, 0\n\tv_mov_b32 v110, 0\n\tv_mov_b32 v111, 0\n\t"
    :: "v"(ka0), "v"(ka1), "i"(TO) : "memory");
}
#define QK(s, k, q, c) "v_mfma_f32_16x16x32_bf16 v[" #s ":" #s "+3], v[" #k ":" #k "+3], v[" #q ":" #q "+3], " c "\n\t"
#define TR(d, a, off) "ds_read_b64_tr_b16 v[" #d ":" #d "+1], %[" #a "] offset:%c[to]+" #off "\n\t"
#define PV(r) "v_mfma_f32_16x16x32_bf16 v[" #r ":" #r "+3], v[124:127], v[" #r ":" #r "+3], 0\n\t"
#define SM(r) "v_fma_f32 v" #r ", v" #r ", v99, v121\n\tv_exp_f32 v" #r ", v" #r "\n\t"
#define PVA(acc, a, v, c) "v_mfma_f32_16x16x32_bf16 v[" #acc ":" #acc "+3], v[" #a ":" #a "+3], v[" #v ":" #v "+3], " c "\n\t"
#define PCP(d, s, ctl, bank) "v_mov_b32_dpp v" #d ", v" #s " " ctl " row_mask:0xf bank_mask:" bank "\n\t"
template <int TO>
__device__ __forceinline__ void seg_attn(u32 qa, u32 sta, u32 va0, u32 va1, u32 va2, u32 va3, u32 oa, u32 mla) {
  asm volatile(
    "ds_read_b128 v[80:83], %[qa]\n\t"
    "ds_read_b128 v[84:87], %[qa] offset:64\n\t"
    "ds_read_b128 v[88:91], %[qa] offset:128\n\t"
    "ds_read_b128 v[92:95], %[qa] offset:192\n\t"
    "ds_read_b64 v[120:121], %[sta]\n\t"
    "s_waitcnt lgkmcnt(4)\n\t"
    QK(112, 48, 80, "0") QK(116, 64, 80, "0")
    "s_waitcnt lgkmcnt(3)\n\t"
    QK(112, 52, 84, "v[112:115]") QK(116, 68, 84, "v[116:119]")
    "s_waitcnt lgkmcnt(2)\n\t"
    QK(112, 56, 88, "v[112:115]") QK(116, 72, 88, "v[116:119]")
    "s_waitcnt lgkmcnt(0)\n\t"
    QK(112, 60, 92, "v[112:115]")
    "v_add_f32_dpp v120, v120, v120 quad_perm:[1,0,3,2] row_mask:0xf bank_mask:0xf\n\t"
    "v_add_f32_dpp v121, v121, v121 quad_perm:[1,0,3,2] row_mask:0xf bank_mask:0xf\n\t"
    QK(116, 76, 92, "v[116:119]")
    TR(48, va0, 0)
    "v_add_f32_dpp v120, v120, v120 quad_perm:[2,3,0,1] row_mask:0xf bank_mask:0xf\n\t"
    "v_add_f32_dpp v121, v121, v121 quad_perm:[2,3,0,1] row_mask:0xf bank_mask:0xf\n\t"
    TR(50, va1, 0) TR(52, va2, 0)
    "v_add_f32_dpp v120, v120, v120 row_half_mirror row_mask:0xf bank_mask:0xf\n\t"
    "v_add_f32_dpp v121, v121, v121 row_half_mirror row_mask:0xf bank_mask:0xf\n\t"
    TR(54, va3, 0)
    "v_mul_f32 v122, 0xbb000000, v120\n\t"
    "v_mul_f32 v123, 0x3b000000, v121\n\t"
    "v_fma_f32 v123, -v122, v122, v123\n\t"
    "v_fmac_f32_dpp v112, v112, v122 row_shl:4 row_mask:0xf bank_mask:0xf bound_ctrl:1\n\t"
    "v_max_f32 v123, 0, v123\n\t"
    "v_fmac_f32_dpp v113, v113, v122 row_shl:4 row_mask:0xf bank_mask:0xf bound_ctrl:1\n\t"
    "v_add_f32 v123, 0x3727c5ac, v123\n\t"
    "v_fmac_f32_dpp v114, v114, v122 row_shl:4 row_mask:0xf bank_mask:0xf bound_ctrl:1\n\t"
    "v_rsq_f32 v123, v123\n\t"
    "v_fmac_f32_dpp v115, v115, v122 row_shl:4 row_mask:0xf bank_mask:0xf bound_ctrl:1\n\t"
    "v_fmac_f32_dpp v116, v116, v122 row_shl:4 row_mask:0xf bank_mask:0xf bound_ctrl:1\n\t"
    "v_max3_f32 v120, v112, v113, v114\n\t"
    "v_fmac_f32_dpp v117, v117, v122 row_shl:4 row_mask:0xf bank_mask:0xf bound_ctrl:1\n\t"
    "v_fmac_f32_dpp v118, v118, v122 row_shl:4 row_mask:0xf bank_mask:0xf bound_ctrl:1\n\t"
    "v_fmac_f32_dpp v119, v119, v122 row_shl:4 row_mask:0xf bank_mask:0xf bound_ctrl:1\n\t"
    "v_max3_f32 v120, v120, v115, v116\n\t"
    "v_mul_f32 v123, %[c2], v123\n\t"
    "v_max3_f32 v120, v120, v117, v118\n\t"
    "v_max_f32 v120, v120, v119\n\t"
    "v_mov_b32 v121, v120\n\t"
    TR(56, va0, 512) TR(58, va1, 512)
    "v_permlane16_swap_b32 v120, v121\n\t"
    "v_max_f32 v120, v120, v121\n\t"
    "v_mov_b32 v121, v120\n\t"
    TR(60, va2, 512) TR(62, va3, 512)
    "v_permlane32_swap_b32 v120, v121\n\t"
    "v_max_f32 v120, v120, v121\n\t"
    "v_mul_f32_e64 v121, -v120, v123\n\t"
    "v_mul_f32 v122, v120, v123\n\t"
    "v_fma_f32 v112, v112, v123, v121\n\t"
    "v_fma_f32 v113, v113, v123, v121\n\t"
    "v_exp_f32 v112, v112\n\t"
    TR(64, va0, 1024)
    "v_fma_f32 v114, v114, v123, v121\n\t"
    "v_exp_f32 v113, v113\n\t"
    TR(66, va1, 1024)
    "v_fma_f32 v115, v115, v123, v121\n\t"
    "v_exp_f32 v114, v114\n\t"
    TR(68, va2, 1024)
    "v_fma_f32 v116, v116, v123, v121\n\t"
    "v_exp_f32 v115, v115\n\t"
    TR(70, va3, 1024)
    "v_fma_f32 v117, v117, v123, v121\n\t"
    "v_exp_f32 v116, v116\n\t"
    "v_cvt_pk_bf16_f32 v124, v112, v113\n\t"
    "v_fma_f32 v118, v118, v123, v121\n\t"
    "v_exp_f32 v117, v117\n\t"
    TR(72, va0, 1536)
    "v_fma_f32 v119, v119, v123, v121\n\t"
    "v_exp_f32 v118, v118\n\t"
    "v_cvt_pk_bf16_f32 v125, v114, v115\n\t"
    "v_exp_f32 v119, v119\n\t"
    TR(74, va1, 1536) TR(76, va2, 1536) TR(78, va3, 1536)
    "v_cvt_pk_bf16_f32 v126, v116, v117\n\t"
    "v_cvt_pk_bf16_f32 v127, v118, v119\n\t"
    PCP(96, 124, "quad_perm:[0,1,2,3]", "0x1") PCP(100, 124, "row_shr:4", "0x2") PCP(104, 124, "quad_perm:[0,1,2,3]", "0x4") PCP(108, 124, "row_shr:4", "0x8")
    PCP(97, 125, "quad_perm:[0,1,2,3]", "0x1") PCP(101, 125, "row_shr:4", "0x2") PCP(105, 125, "quad_perm:[0,1,2,3]", "0x4") PCP(109, 125, "row_shr:4", "0x8")
    PCP(98, 126, "quad_perm:[0,1,2,3]", "0x1") PCP(102, 126, "row_shr:4", "0x2") PCP(106, 126, "quad_perm:[0,1,2,3]", "0x4") PCP(110, 126, "row_shr:4", "0x8")
    PCP(99, 127, "quad_perm:[0,1,2,3]", "0x1") PCP(103, 127, "row_shr:4", "0x2") PCP(107, 127, "quad_perm:[0,1,2,3]", "0x4") PCP(111, 127, "row_shr:4", "0x8")
    "v_mov_b32 v84, %[ones]\n\tv_mov_b32 v85, %[ones]\n\tv_mov_b32 v86, %[ones]\n\tv_mov_b32 v87, %[ones]\n\t"
    "s_waitcnt lgkmcnt(0)\n\t"
    PVA(112, 96, 48, "0") PVA(116, 96, 64, "0")
    PVA(112, 100, 52, "v[112:115]") PVA(116, 100, 68, "v[116:119]")
    PVA(112, 104, 56, "v[112:115]") PVA(116, 104, 72, "v[116:119]")
    PVA(112, 108, 60, "v[112:115]") PVA(116, 108, 76, "v[116:119]")
    "v_mfma_f32_16x16x32_bf16 v[80:83], v[84:87], v[124:127], 0\n\t"
    "s_nop 6\n\t"
    "ds_write_b128 %[oa], v[112:115]\n\t"
    "ds_write_b128 %[oa], v[116:119] offset:1024\n\t"
    "s_mov_b64 exec, 0xf\n\t"
    "ds_write2_b32 %[mla], v122, v80 offset0:0 offset1:16\n\t"
    "s_mov_b64 exec, -1\n\t"
    :: [qa] "v"(qa), [sta] "v"(sta), [va0] "v"(va0), [va1] "v"(va1), [va2] "v"(va2), [va3] "v"(va3), [oa] "v"(oa), [mla] "v"(mla),
       [c2] "s"(C2), [ones] "s"(ONES), [to] "i"(TO) : "memory");
}
__device__ __forceinline__ void seg_merge(u32 mla, u32 oa, u32 so, const char* ws) {
  asm volatile(
    "ds_read_b128 v[48:51], %[mla]\n\t"
    "ds_read_b128 v[60:63], %[mla] offset:64\n\t"
    "ds_read2st64_b32 v[56:57], %[oa] offset0:0 offset1:8\n\t"
    "ds_read2st64_b32 v[58:59], %[oa] offset0:16 offset1:24\n\t"
    "s_waitcnt lgkmcnt(3)\n\t"
    "v_max3_f32 v52, v48, v49, v50\n\t"
    "v_max_f32 v52, v52, v51\n\t"
    "v_sub_f32 v48, v48, v52\n\t"
    "v_sub_f32 v49, v49, v52\n\t"
    "v_exp_f32 v48, v48\n\t"
    "v_sub_f32 v50, v50, v52\n\t"
    "v_exp_f32 v49, v49\n\t"
    "v_sub_f32 v51, v51, v52\n\t"
    "v_exp_f32 v50, v50\n\t"
    "v_exp_f32 v51, v51\n\t"
    "s_waitcnt lgkmcnt(0)\n\t"
    "v_pk_mul_f32 v[60:61], v[60:61], v[48:49]\n\t"
    "v_pk_mul_f32 v[56:57], v[56:57], v[48:49]\n\t"
    "v_pk_fma_f32 v[60:61], v[62:63], v[50:51], v[60:61]\n\t"
    "v_pk_fma_f32 v[56:57], v[58:59], v[50:51], v[56:57]\n\t"
    "v_add_f32 v60, v60, v61\n\t"
    "v_add_f32 v56, v56, v57\n\t"
    "v_rcp_f32 v60, v60\n\t"
    "s_nop 0\n\t"
    "v_mul_f32 v52, v56, v60\n\t"
    PACK_STORE
    :: [mla] "v"(mla), [oa] "v"(oa), [so] "v"(so), [ws] "s"(ws), [c8z] "s"(C8Z) : "memory");
}

__device__ __forceinline__ void dma16(u32 voff, const char* sbase, u32 lds_dst) {
  u32 keep;
  asm volatile(SNOP "s_mov_b32 %0, m0\n\ts_mov_b32 m0, %3\n\ts_nop 0\n\tglobal_load_lds_dwordx4 %1, %2" NTKV "\n\ts_mov_b32 m0, %0"
               : "=&s"(keep) : "v"(voff), "s"(sbase), "s"(lds_dst) : "memory");
}
__device__ __forceinline__ void kv_load(const char* base, int w, int lane, u32 ldsb) {
  asm volatile("" : "+s"(base));
  const int r8 = (lane & 31) >> 2;
#pragma unroll
  for (int i = 0; i < 4; ++i) {
    const u32 ch = 8 * (i & 1) + 4 * (lane >> 5) + ((lane & 3) ^ ((2 * (i >> 1) + (r8 >> 2)) & 3));
    dma16(r8 * 256 + ch * 16, base + (size_t)(16 * w + 8 * (i >> 1)) * 256, ldsb + w * 4096 + i * 1024);
  }
}
__device__ __forceinline__ bool has_sent(v4u x) {
  const u32 m = __builtin_elementwise_max(__builtin_elementwise_max(x[0], x[1]), __builtin_elementwise_max(x[2], x[3]));
  return __builtin_amdgcn_ballot_w64(m == SENT) != 0ull;
}
__device__ __forceinline__ v4u poll128(const char* base, u32 voff) {
  v4u x;
  for (u32 spins = 0;; ++spins) {
    asm volatile("s_nop 4\n\tglobal_load_dwordx4 %0, %1, %2 sc1\n\ts_waitcnt vmcnt(0)" : "=&v"(x) : "v"(voff), "s"(base) : "memory");
    if (!has_sent(x) || spins > SPIN_CAP) break;
  }
  return x;
}
__device__ __forceinline__ v4u pack8(float v) {
  const u32 x = safe(pk(v, dpp<XOR1>(v)));
  return v4u{x, dppu<0x102>(x), dppu<0x104>(x), dppu<0x106>(x)};
}
__device__ __forceinline__ void sum_partials(v4u x, float (&v)[8]) {
#pragma unroll
  for (int m = 0; m < 4; ++m) { v[2 * m] = bflo(x[m]); v[2 * m + 1] = bfhi(x[m]); }
#pragma unroll
  for (int j = 0; j < 8; ++j) v[j] += dpp<XOR1>(v[j]);
#pragma unroll
  for (int j = 0; j < 8; ++j) v[j] += dpp<XOR2>(v[j]);
#pragma unroll
  for (int j = 0; j < 8; ++j) v[j] += dpp<HMIR>(v[j]);
}
__device__ __forceinline__ void x_stats(v4u xa, char* lds, int w, int lane) {
  const bool p = (lane >> 3) & 1; const int kq = lane >> 4;
  const v4f gm = mfma(xa, xa, v4f{0.f, 0.f, 0.f, 0.f});
  const v4f sm = mfma(xa, v4u{ONES, ONES, ONES, ONES}, v4f{0.f, 0.f, 0.f, 0.f});
  float s2 = (gm[0] + gm[1]) + (gm[2] + gm[3]);
  s2 = (p == (bool)(kq >> 1)) ? s2 : 0.f;
  s2 += dpp<XOR1>(s2); s2 += dpp<XOR2>(s2); s2 += dpp<HMIR>(s2); s2 += dpp<ROR8>(s2);
  s2 = x32_add(x16_add(s2));
  float s1 = (sm[0] + sm[1]) + (sm[2] + sm[3]);
  s1 = x32_add(x16_add(s1));
  if (lane == 0) *(v2f*)(lds + L_ST + w * 8) = v2f{s1, s2};
}
}

#define ST(i) do {} while (0)
#define STFLUSH(sv, base) do {} while (0)
#define SGPR_ATTR

__device__ __forceinline__ u32 lb_st() { return L_ST; }
#define OPAQUE_LANE() int lane = lane0; asm volatile("" : "+v"(lane)); const int c8 = lane & 7, kq = lane >> 4; const bool p = (lane >> 3) & 1; (void)c8; (void)p; (void)kq
#define OPAQUE_S(x) asm volatile("" : "+s"(x))

constexpr int FW = 4;

struct Addr {
  u32 vB, vA, vL, lw, so;
  u32 mr, orr;
  u32 r0, r1, r2, r3, r4, r5, r6, r7, r8;
};
#define PIN(x) asm volatile("" : "+v"(x))

template <int TYPE>
__device__ __forceinline__ void layer(const int rep, char* ws, char* lds, int b, int g, int s, int w, const int lane0, v4u xraw, Addr& A, u32 qaT,
                                      const char* nkc, const char* nvc, const char* nwq, const char* cwo, const char* nwo, const bool lead,
                                      const u32 tag, int& stampv, u32 (&cnt)[3]) {
  constexpr int RQ = TYPE ? R_WQB : R_WQA, RO = TYPE ? R_WOB : R_WOA;
  const int L = rep * 2 + TYPE;
  OPAQUE_S(b); OPAQUE_S(g); OPAQUE_S(s); OPAQUE_S(w);
  const u32 grp = (u32)(b * 8 + g), cu = grp * 8 + s;
#define LD_WQ(R, ptr) do { OPAQUE_LANE(); OPAQUE_S(ptr); bulk8<R, 8192>(ptr + (size_t)(64 * w) * 8192 + (512 * g + 64 * s) * 2, (u32)(lane >> 3) * 65536 + c8 * 16); } while (0)
#define LD_WO(R, ptr) do { OPAQUE_LANE(); OPAQUE_S(ptr); bulk8<R, 1024>(ptr + (size_t)(512 * g + 64 * w) * 1024 + 128 * s, (u32)(lane >> 3) * 8192 + c8 * 16); } while (0)
  const char* wsL = ws + L * BOXL;
  const char* box = ws + WS_BOX + cu * BOX + L * BOXL;
  {
    if (rep == 0) {
      if (SCHED == 2) VMWAIT(0);
      else if (SCHED == 0) { if (TYPE == 0) { if (PRETOUCH && w == 7) VMWAIT(13); else VMWAIT(8); } else VMWAIT(0); }
      else { if (TYPE == 0) { if (!lead) VMWAIT(4); } else { VMWAIT(4); } }
      if (w >= 4) ST(7);
      transpose8<RQ>();
    }
    if (L > 0) {
      const char* wsP = wsL - BOXL;
      if (DLY_B && rep) __builtin_amdgcn_s_sleep(DLY_B);
      ST(0);
      u32 sp = 0;
      seg_p1<RQ, true, 0>(A.vB, wsP, A.lw, sp);
      cnt[0] += sp;
    } else {
      const v4u xa = (lane0 & 7) == 0 ? xraw : v4u{0u, 0u, 0u, 0u};
      asm volatile("v_mov_b32 v48, %0\n\tv_mov_b32 v49, %1\n\tv_mov_b32 v50, %2\n\tv_mov_b32 v51, %3" :: "v"(xa[0]), "v"(xa[1]), "v"(xa[2]), "v"(xa[3]));
      u32 sp = 0;
      seg_p1<RQ, false, 0>(0, ws, A.lw, sp);
    }
    ST(2);
    if (SCHED != 2 && rep == 0 && TYPE == 1) {
      OPAQUE_LANE();
      if (SCHED == 0) kv_load(nkc, w, lane, L_K + 32768);
      kv_load(nvc, w, lane, L_V + 32768);
    }
    v4u xa;
    asm volatile("v_mov_b32 %0, v48\n\tv_mov_b32 %1, v49\n\tv_mov_b32 %2, v50\n\tv_mov_b32 %3, v51" : "=v"(xa[0]), "=v"(xa[1]), "=v"(xa[2]), "=v"(xa[3]));
    if (rep == 0) {
      asm volatile("v_mov_b32 v48, %0\n\tv_mov_b32 v49, %0\n\tv_mov_b32 v50, %0\n\tv_mov_b32 v51, %0" :: "s"(ONES));
      u32 sp = 0;
      seg_p1<RQ, false, L_REDC - L_RED>(0, ws, A.lw, sp);
    }
    BAR();
    ST(3);
    if (w == FW) {
      seg_final2<false>(A.r2, 0, A.so, box + BOX_Q);
      ST(4);
      OPAQUE_LANE();
      if (rep == 0) {
        const __amdgpu_buffer_rsrc_t rs = __builtin_amdgcn_make_buffer_rsrc(ws, 0, WS_END, 0x00020000);
        const v4f r0 = *(const v4f*)(lds + L_REDC + lane * 32), r1 = *(const v4f*)(lds + L_REDC + lane * 32 + 16);
        const float cs = 0.125f * (((r0[0] + r0[1]) + (r0[2] + r0[3])) + ((r1[0] + r1[1]) + (r1[2] + r1[3])));
        const v4u pc = pack8(cs);
        if (c8 == 0) __builtin_amdgcn_raw_buffer_store_b128(pc, rs, WS_BOX + cu * BOX + BOX_CS + TYPE * 128 + lane * 2, 0, 16);
      }
      x_stats(xa, lds, w, lane);
      if (L == 0) {
        const u64 flag = (u64)tag | ((u64)(~tag) << 32);
        for (u32 spins = 0;; ++spins) {
          const u64 f = __hip_atomic_load((GAS u64*)(ws + WS_RDY + b * 512 + lane * 8), __ATOMIC_RELAXED, __HIP_MEMORY_SCOPE_AGENT);
          if (__all(f == flag) || spins > SPIN_CAP) break;
        }
        if (g == 0 && s == 0 && lane == 0) __hip_atomic_store((GAS u32*)(ws + WS_EPOCH + b * 256), tag, __ATOMIC_RELAXED, __HIP_MEMORY_SCOPE_AGENT);
      }
      if (DLY_C) __builtin_amdgcn_s_sleep(DLY_C);
      u32 sp = 0;
      seg_qpoll<0>(A.r0, wsL, A.r1, sp);
      cnt[1] += sp;
      if (rep == 0) { sp = 0; seg_qpoll<L_CSI + TYPE * 1088 - L_Q>(A.r0, ws + (BOX_CS + TYPE * 128 - BOX_Q), A.r1, sp); }
      ST(5);
    } else {
      OPAQUE_LANE();
      x_stats(xa, lds, w, lane);
    }
    if (g == 0 && w == s) {
      OPAQUE_LANE();
      float v[8];
      sum_partials(xa, v);
      if (c8 == 0) {
        float* xs = (float*)(lds + L_XS) + 16 * kq + (p ? 8 : 0);
        *(v4f*)xs = v4f{v[0], v[1], v[2], v[3]};
        *(v4f*)(xs + 4) = v4f{v[4], v[5], v[6], v[7]};
      }
    }
    if (rep == 0) {
      if (SCHED == 1 && TYPE == 0 && lead) VMWAIT(16); else VMWAIT(0);
      if (w >= 4) ST(8);
      BAR();
      if (SCHED == 2) {}
      else if (SCHED == 0) LD_WO(RO, cwo);
      else {
        if (!lead) LD_WO(RO, cwo);
        else if (TYPE == 0) LD_WO(R_WOB, nwo);
      }
    }
  }
  if (w < 4) {
    seg_kpre<TYPE * 32768>(A.r0, A.r1);
    ST(6);
    BAR();
    ST(7);
    seg_attn<TYPE * 32768>(qaT, A.r6, A.r2, A.r3, A.r4, A.r5, A.r7, A.r8);
    ST(8);
  } else {
    BAR();
  }
  {
    BAR();
    seg_merge(A.mr, A.orr, A.so, box + w * 128);
    if (w == 7) {
      OPAQUE_LANE();
      const __amdgpu_buffer_rsrc_t rs = __builtin_amdgcn_make_buffer_rsrc(ws, 0, WS_END, 0x00020000);
      const int hh = (lane >> 2) & 3, aa = lane & 3;
      const float m1 = ((const float*)(lds + L_ML))[hh * 4 + aa], l1 = ((const float*)(lds + L_ML))[16 + hh * 4 + aa];
      float M1 = __builtin_fmaxf(m1, dpp<XOR1>(m1)); M1 = __builtin_fmaxf(M1, dpp<XOR2>(M1));
      float L1 = __builtin_amdgcn_exp2f(m1 - M1) * l1; L1 += dpp<XOR1>(L1); L1 += dpp<XOR2>(L1);
      const u32 e = safe(__float_as_uint(M1 + __builtin_amdgcn_logf(L1)));
      const v4u ev = {dppu<0x150>(e), dppu<0x154>(e), dppu<0x158>(e), dppu<0x15C>(e)};
      if (lane < 8) __builtin_amdgcn_raw_buffer_store_b128(ev, rs, WS_LSR + ((L * 32 + grp) * 8 + lane) * 128 + s * 16, 0, 16);
    }
    ST(9);
  }
  {
    const char* wsS = ws + L * 32768;
    if (rep == 0) {
      if (SCHED == 1 && TYPE == 0 && lead) VMWAIT(8); else VMWAIT(0);
      transpose8<RO>();
    } else {
      if (DLY_A) __builtin_amdgcn_s_sleep(DLY_A);
    }
    ST(1);
    u32 sp = 0;
    seg_p3<RO>(A.vA, A.vL, wsL, wsS, A.lw, sp);
    cnt[2] += sp;
    ST(10);
    if (SCHED != 2 && rep == 0 && TYPE == 0) {
      if (SCHED == 0) LD_WQ(R_WQB, nwq);
      else {
        if (!lead) LD_WQ(R_WQB, nwq);
        OPAQUE_LANE();
        kv_load(nkc, w, lane, L_K + 32768);
      }
    }
    BAR();
    if (w == FW) seg_final2<true>(A.r2, A.r3, A.so, box + BOX_X);
    ST(11);
  }
  STFLUSH(stampv, L * 12);
}

__global__ void __launch_bounds__(512, 2) __attribute__((amdgpu_num_vgpr(24) SGPR_ATTR))
fused_kernel(const char* __restrict__ xin, const char* kA, const char* vA, const char* wqA, const char* woA,
             const char* kB, const char* vB, const char* wqB, const char* woB, unsigned short* out, char* ws) {
  extern __shared__ __attribute__((aligned(16))) char lds[];
  const int id = blockIdx.x, g = id & 7, b = id >> 6, s = (id >> 3) & 7;
  const int tid = threadIdx.x, lane = tid & 63, w = __builtin_amdgcn_readfirstlane(tid >> 6);
  const int c8 = lane & 7, kq = lane >> 4;
  const bool p = (lane >> 3) & 1;
  int stampv = 0;
  const __amdgpu_buffer_rsrc_t rs = __builtin_amdgcn_make_buffer_rsrc(ws, 0, WS_END, 0x00020000);
  const u32 cu = (u32)((b * 8 + g) * 8 + s);
  asm volatile("" ::: "v127", "v255");
  u32 ep; v4u xraw;
  asm volatile("s_nop 4\n\tglobal_load_dword v56, %0, %1 sc1\n\tglobal_load_dwordx4 v[52:55], %2, %3"
               :: "v"(WS_EPOCH + b * 256), "s"(ws), "v"((u32)((b * 512 + 64 * w + 16 * kq + (p ? 8 : 0)) * 2)), "s"(xin) : "memory");
  if (w == 5) {
    const v4u sv = {SENT, SENT, SENT, SENT};
#pragma unroll
    for (int i = 0; i < 5; ++i) __builtin_amdgcn_raw_buffer_store_b128(sv, rs, WS_BOX + cu * BOX + i * 1024 + lane * 16, 0, 16);
    const u32 o6 = lane < 16 ? WS_BOX + cu * BOX + 5120 + lane * 16
                             : WS_LSR + (u32)(((((lane - 16) >> 3) * 32 + b * 8 + g) * 8 + (lane & 7)) * 128 + s * 16);
    if (lane < 48) __builtin_amdgcn_raw_buffer_store_b128(sv, rs, o6, 0, 16);
  }
  if (w == 6) ((float*)(lds + L_XS))[lane] = 0.f;
  const size_t kvoff = ((size_t)(b * 8 + g) * SK + (SK - WIN) + s * 128) * 256;
  const bool lead = (w >> 1) == b;
  const size_t qoff = (size_t)(64 * w) * 8192 + (512 * g + 64 * s) * 2, ooff = (size_t)(512 * g + 64 * w) * 1024 + 128 * s;
  const u32 vq = (u32)(lane >> 3) * 65536 + c8 * 16, vo = (u32)(lane >> 3) * 8192 + c8 * 16;
  if (SCHED == 0 || SCHED == 2 || lead) {
    bulk8<R_WQA, 8192>(wqA + qoff, vq);
    VMWAIT(8);
  } else {
    VMWAIT(0);
  }
  asm volatile("v_mov_b32 %0, v52\n\tv_mov_b32 %1, v53\n\tv_mov_b32 %2, v54\n\tv_mov_b32 %3, v55\n\tv_mov_b32 %4, v56"
               : "=v"(xraw[0]), "=v"(xraw[1]), "=v"(xraw[2]), "=v"(xraw[3]), "=v"(ep));
  ST(6);
  const u32 tag = (u32)__builtin_amdgcn_readfirstlane((int)ep) + 1u;
  if (w == 5 && lane == 0) {
    const u64 flag = (u64)tag | ((u64)(~tag) << 32);
    __hip_atomic_store((GAS u64*)(ws + WS_RDY + b * 512 + (s * 8 + g) * 8), flag, __ATOMIC_RELAXED, __HIP_MEMORY_SCOPE_AGENT);
  }
  if (SCHED == 0) {
    asm volatile("s_barrier" ::: "memory");
    kv_load(kA + kvoff, w, lane, L_K);
    kv_load(vA + kvoff, w, lane, L_V);
  } else if (SCHED == 2) {
    kv_load(kA + kvoff, w, lane, L_K);
    kv_load(vA + kvoff, w, lane, L_V);
    bulk8<R_WOA, 1024>(woA + ooff, vo);
    bulk8<R_WQB, 8192>(wqB + qoff, vq);
    kv_load(kB + kvoff, w, lane, L_K + 32768);
    kv_load(vB + kvoff, w, lane, L_V + 32768);
    bulk8<R_WOB, 1024>(woB + ooff, vo);
  } else {
    kv_load(kA + kvoff, w, lane, L_K);
    if (lead) VMWAIT(4);
    asm volatile("s_barrier" ::: "memory");
    if (!lead) bulk8<R_WQA, 8192>(wqA + qoff, vq);
    kv_load(vA + kvoff, w, lane, L_V);
    if (lead) { bulk8<R_WOA, 1024>(woA + ooff, vo); bulk8<R_WQB, 8192>(wqB + qoff, vq); }
  }
  if (PRETOUCH && w == 7) {
    const size_t kvo = ((size_t)(b * 8 + g) * SK + (SK - WIN) + s * 128) * 256;
    asm volatile("s_nop 4\n\tglobal_load_dword v100, %0, %2\n\tglobal_load_dword v101, %1, %3\n\tglobal_load_dword v102, %1, %4\n\t"
                 "global_load_dword v103, %5, %6\n\tglobal_load_dword v104, %5, %7"
                 :: "v"((u32)(lane * 65536 + (512 * g + 64 * s) * 2)), "v"((u32)((512 * g + lane * 8) * 1024 + 128 * s)), "s"(wqB), "s"(woA), "s"(woB),
                    "v"((u32)(lane * 512)), "s"(kB + kvo), "s"(vB + kvo) : "memory");
  }
  Addr A; u32 qa0, qa1;
  {
    const u32 grp = (u32)(b * 8 + g);
    const u32 oct = kq * 32 + (p ? 16 : 0);
    A.vB = WS_BOX + (u32)((b * 8 + c8) * 8 + w) * BOX + BOX_X + oct;
    A.vA = WS_BOX + (grp * 8 + c8) * BOX + w * 128 + oct;
    A.vL = WS_LSR + (grp * 8 + s) * 128 + c8 * 16 + (w >> 1) * 4;
    A.lw = L_RED + (8 * c8 + (lane >> 3)) * 32 + w * 4;
    A.so = lane * 2;
    A.mr = L_ML + (w >> 1) * 16;
    A.orr = L_OM + (w & 1) * 1024 + lane * 16 + (w >> 1) * 4;
    const int r16 = lane & 15, q4 = r16 >> 2, p2 = lane & 3;
    qa0 = L_Q + (lane & 3) * 272 + kq * 16;
    qa1 = qa0;
    if (lane & 4) { qa0 += L_CSI - L_Q; qa1 += L_CSI + 1088 - L_Q; }
    if (w < 4) {
      const u32 kb = L_K + 2048 * (4 * w + (r16 >> 2)) + 64 * (r16 & 3);
      A.r0 = kb + 16 * (kq ^ ((2 * (r16 >> 2)) & 3));
      A.r1 = kb + 256 + 16 * (kq ^ ((2 * (r16 >> 2) + 1) & 3));
      const u32 vb = L_V + 2048 * (4 * w + kq) + 64 * q4 + 8 * (p2 & 1);
      A.r2 = vb + 16 * (((p2 >> 1)) ^ ((2 * kq) & 3));
      A.r3 = vb + 256 + 16 * (((p2 >> 1)) ^ ((2 * kq + 1) & 3));
      A.r4 = vb + 16 * ((2 + (p2 >> 1)) ^ ((2 * kq) & 3));
      A.r5 = vb + 256 + 16 * ((2 + (p2 >> 1)) ^ ((2 * kq + 1) & 3));
      A.r7 = L_OM + w * 2048 + lane * 16;
      A.r8 = L_ML + (lane * 4 + w) * 4;
    } else {
      A.r0 = WS_BOX + (grp * 8 + (lane >> 3)) * BOX + BOX_Q + c8 * 16;
      A.r1 = L_Q + kq * 272 + (lane & 15) * 16;
      A.r2 = L_RED + lane * 32;
      A.r3 = L_XS + lane * 4;
      A.r4 = A.r5 = A.r7 = A.r8 = 0;
    }
    A.r6 = L_ST + c8 * 8;
    PIN(A.r6);
    PIN(A.vB); PIN(A.vA); PIN(A.vL); PIN(A.lw); PIN(A.so); PIN(A.mr); PIN(A.orr); PIN(A.r0); PIN(A.r1); PIN(A.r2); PIN(A.r3); PIN(A.r4); PIN(A.r5); PIN(A.r7); PIN(A.r8);
    PIN(qa0); PIN(qa1);
  }
  u32 cnt[3] = {0, 0, 0};
  for (int rep = 0; rep < 2; ++rep) {
    layer<0>(rep, ws, lds, b, g, s, w, lane, xraw, A, qa0, kB + kvoff, nullptr, wqB, woA, woB, lead, tag, stampv, cnt);
    layer<1>(rep, ws, lds, b, g, s, w, lane, xraw, A, qa1, kB + kvoff, vB + kvoff, nullptr, woB, nullptr, lead, tag, stampv, cnt);
  }
  if (g == 0 && w == s) {
    const u32 voff = WS_BOX + (u32)((b * 8 + c8) * 8 + w) * BOX + 3u * BOXL + BOX_X + kq * 32 + (p ? 16 : 0);
    if (DLY_B) __builtin_amdgcn_s_sleep(DLY_B);
    const v4u x = poll128(ws, voff);
    float v[8];
    sum_partials(x, v);
    if (c8 == 0) *(v4u*)(out + b * 512 + 64 * w + 16 * kq + (p ? 8 : 0)) = v4u{pk(v[0], v[1]), pk(v[2], v[3]), pk(v[4], v[5]), pk(v[6], v[7])};
  }
}

extern "C" void kernel_launch(void* const* d_in, const int* in_sizes, int n_in, void* d_out, int out_size, void* d_ws, size_t ws_size,
                              hipStream_t stream) {
  (void)hipFuncSetAttribute((const void*)fused_kernel, hipFuncAttributeMaxDynamicSharedMemorySize, L_END);
  fused_kernel<<<256, 512, L_END, stream>>>((const char*)d_in[0], (const char*)d_in[1], (const char*)d_in[2], (const char*)d_in[3], (const char*)d_in[4],
                                            (const char*)d_in[6], (const char*)d_in[7], (const char*)d_in[8], (const char*)d_in[9],
                                            (unsigned short*)d_out, (char*)d_ws);
}
```

```cpp
#include <hip/hip_runtime.h>
#include <stdint.h>

typedef unsigned int u32;
typedef unsigned long long u64;
typedef u32 v4u __attribute__((ext_vector_type(4)));
typedef u32 v2u __attribute__((ext_vector_type(2)));
typedef float v4f __attribute__((ext_vector_type(4)));
typedef float v2f __attribute__((ext_vector_type(2)));
typedef __bf16 v8b __attribute__((ext_vector_type(8)));
typedef __bf16 v2b __attribute__((ext_vector_type(2)));
#define GAS __attribute__((address_space(1)))

#define DLY_C 2
#define DLY_A 8
#define DLY_B 10
#define SCHED 2
#define EXTRA 0
#define PRETOUCH 0
#define NTKV ""
#define NTW ""

namespace {
constexpr int SK = 4096, WIN = 1024;
constexpr u32 WS_EPOCH = 0;
constexpr u32 WS_RDY = 4096;
constexpr u32 WS_LSR = 8192;
constexpr u32 WS_BOX = WS_LSR + 131072;
constexpr u32 BOXL = 1280, BOX_Q = 1024, BOX_X = 1152, BOX_CS = 5120, BOX = 5376;
constexpr u32 WS_END = WS_BOX + 256 * BOX;
constexpr u32 WS_DBG = 4u << 20;
constexpr u32 L_KV = 0;
constexpr u32 L_K = 0, L_V = 65536;
constexpr u32 L_OM = 131072;
constexpr u32 L_ML = L_OM + 8192;
constexpr u32 L_Q = L_ML + 128;
constexpr u32 L_CSI = L_Q + 1088;
constexpr u32 L_RED = L_CSI + 2176;
constexpr u32 L_REDC = L_RED + 2048;
constexpr u32 L_ST = L_REDC + 2048;
constexpr u32 L_RED3 = L_ST + 64;
constexpr u32 L_XS = L_RED3 + 2048;
constexpr u32 L_END = L_XS + 256;
static_assert(L_END <= 160 * 1024, "lds");
static_assert(L_Q % 16 == 0 && L_CSI % 16 == 0 && L_RED % 16 == 0 && L_ST % 16 == 0 && L_RED3 % 16 == 0 && L_XS % 16 == 0, "align");
constexpr u32 SENT = 0xFFFFFFFFu;
constexpr u32 SPIN_CAP = 1u << 15;
constexpr float C2 = 0.08838834764831845f * 1.4426950408889634f;
constexpr u32 ONES = 0x3F803F80u;
constexpr u64 PMASK = 0xFF00FF00FF00FF00ull;
constexpr u64 C8Z = 0x0101010101010101ull;

__device__ __forceinline__ float bflo(u32 w) { return __uint_as_float(w << 16); }
__device__ __forceinline__ float bfhi(u32 w) { return __uint_as_float(w & 0xffff0000u); }
__device__ __forceinline__ u32 pk(float lo, float hi) { v2f f = {lo, hi}; return __builtin_bit_cast(u32, __builtin_convertvector(f, v2b)); }
__device__ __forceinline__ u32 safe(u32 v) { return v < 0xFFFFFFFEu ? v : 0xFFFFFFFEu; }
template <int CTRL> __device__ __forceinline__ float dpp(float x) {
  return __builtin_bit_cast(float, __builtin_amdgcn_mov_dpp(__builtin_bit_cast(int, x), CTRL, 0xf, 0xf, true));
}
template <int CTRL> __device__ __forceinline__ u32 dppu(u32 x) { return (u32)__builtin_amdgcn_mov_dpp((int)x, CTRL, 0xf, 0xf, true); }
constexpr int XOR1 = 0xB1, XOR2 = 0x4E, HMIR = 0x141, ROR8 = 0x128;
__device__ __forceinline__ float x16_add(float x) { auto s = __builtin_amdgcn_permlane16_swap(__float_as_uint(x), __float_as_uint(x), false, false); return __uint_as_float(s[0]) + __uint_as_float(s[1]); }
__device__ __forceinline__ float x32_add(float x) { auto s = __builtin_amdgcn_permlane32_swap(__float_as_uint(x), __float_as_uint(x), false, false); return __uint_as_float(s[0]) + __uint_as_float(s[1]); }
__device__ __forceinline__ v4f mfma(v4u a, v4u b, v4f c) {
  return __builtin_amdgcn_mfma_f32_16x16x32_bf16(__builtin_bit_cast(v8b, a), __builtin_bit_cast(v8b, b), c, 0, 0, 0);
}
#define BAR() asm volatile("s_waitcnt lgkmcnt(0)\n\ts_barrier" ::: "memory")
#define VMWAIT(n) asm volatile("s_waitcnt vmcnt(" #n ")" ::: "memory")

constexpr int R_WQA = 128, R_WOA = 160, R_WQB = 192, R_WOB = 224;
template <int BASE, u32 STRIDE>
__device__ __forceinline__ void bulk8(const char* base, u32 voff) {
  asm volatile("" : "+s"(base));
  const __amdgpu_buffer_rsrc_t r = __builtin_amdgcn_make_buffer_rsrc((void*)base, 0, 0x7fffffff, 0x00020000);
  asm volatile("s_nop 4\n\t"
               "buffer_load_dwordx4 v[%c10:%c11], %0, %1, 0 offen" NTW "\n\tbuffer_load_dwordx4 v[%c12:%c13], %0, %1, %3 offen" NTW "\n\t"
               "buffer_load_dwordx4 v[%c14:%c15], %0, %1, %4 offen" NTW "\n\tbuffer_load_dwordx4 v[%c16:%c17], %0, %1, %5 offen" NTW "\n\t"
               "buffer_load_dwordx4 v[%c18:%c19], %0, %1, %6 offen" NTW "\n\tbuffer_load_dwordx4 v[%c20:%c21], %0, %1, %7 offen" NTW "\n\t"
               "buffer_load_dwordx4 v[%c22:%c23], %0, %1, %8 offen" NTW "\n\tbuffer_load_dwordx4 v[%c24:%c25], %0, %1, %9 offen" NTW
               :: "v"(voff), "s"(r), "s"(0), "s"(STRIDE), "s"(2 * STRIDE), "s"(3 * STRIDE), "s"(4 * STRIDE), "s"(5 * STRIDE), "s"(6 * STRIDE), "s"(7 * STRIDE),
                  "i"(BASE), "i"(BASE + 3), "i"(BASE + 4), "i"(BASE + 7), "i"(BASE + 8), "i"(BASE + 11), "i"(BASE + 12), "i"(BASE + 15),
                  "i"(BASE + 16), "i"(BASE + 19), "i"(BASE + 20), "i"(BASE + 23), "i"(BASE + 24), "i"(BASE + 27), "i"(BASE + 28), "i"(BASE + 31)
               : "memory");
}
template <int B, int M, int R>
__device__ __forceinline__ void tp_pair(u32 slo, u32 shi) {
  if constexpr (M == R) {
    asm volatile("v_perm_b32 v48, v%c3, v%c2, %0\n\tv_perm_b32 v%c3, v%c3, v%c2, %1\n\tv_mov_b32 v%c2, v48"
                 :: "s"(slo), "s"(shi), "i"(B + 8 * M + R), "i"(B + 8 * M + 4 + R));
  } else {
    asm volatile("v_perm_b32 v48, v%c3, v%c2, %0\n\tv_perm_b32 v49, v%c3, v%c2, %1\n\t"
                 "v_perm_b32 v%c2, v%c5, v%c4, %0\n\tv_perm_b32 v%c3, v%c5, v%c4, %1\n\t"
                 "v_mov_b32 v%c4, v48\n\tv_mov_b32 v%c5, v49"
                 :: "s"(slo), "s"(shi), "i"(B + 8 * M + R), "i"(B + 8 * M + 4 + R), "i"(B + 8 * R + M), "i"(B + 8 * R + 4 + M));
  }
}
template <int B>
__device__ __forceinline__ void transpose8() {
  const u32 slo = 0x05040100u, shi = 0x07060302u;
  tp_pair<B, 0, 0>(slo, shi); tp_pair<B, 1, 1>(slo, shi); tp_pair<B, 2, 2>(slo, shi); tp_pair<B, 3, 3>(slo, shi);
  tp_pair<B, 0, 1>(slo, shi); tp_pair<B, 0, 2>(slo, shi); tp_pair<B, 0, 3>(slo, shi);
  tp_pair<B, 1, 2>(slo, shi); tp_pair<B, 1, 3>(slo, shi); tp_pair<B, 2, 3>(slo, shi);
  asm volatile("s_nop 1");
}
#define SNOP
#define MF(d, b) "v_mfma_f32_16x16x32_bf16 v[" #d ":" #d "+3], v[48:51], v[%c[wb]+" #b ":%c[wb]+" #b "+3], 0\n\t"
#define FOLD_TAIL \
  "v_permlane32_swap_b32 v52, v68\n\t" \
  "v_permlane32_swap_b32 v56, v72\n\t" \
  "v_permlane32_swap_b32 v60, v76\n\t" \
  "v_permlane32_swap_b32 v64, v80\n\t" \
  "v_cndmask_b32_e64 v52, v52, v68, %[pm]\n\t" \
  "v_cndmask_b32_e64 v60, v60, v76, %[pm]\n\t" \
  "v_cndmask_b32_e64 v56, v56, v72, %[pm]\n\t" \
  "v_cndmask_b32_e64 v64, v64, v80, %[pm]\n\t" \
  "v_permlane16_swap_b32 v52, v60\n\t" \
  "s_nop 0\n\t" \
  "v_permlane16_swap_b32 v56, v64\n\t" \
  "v_add_f32 v52, v52, v60\n\t" \
  "v_add_f32 v56, v56, v64\n\t" \
  "s_nop 0\n\t" \
  "v_add_f32_dpp v52, v52, v52 row_ror:8 row_mask:0xf bank_mask:0xf\n\t" \
  "v_add_f32_dpp v56, v56, v56 row_ror:8 row_mask:0xf bank_mask:0xf\n\t" \
  "v_cndmask_b32_e64 v52, v52, v56, %[pm]\n\t"

template <int WB, bool POLL, int LOFF>
__device__ __forceinline__ void seg_p1(u32 voff, const char* ws, u32 ldsw, u32& spins) {
  if constexpr (POLL) {
    asm volatile(
      SNOP "Lp%=:\n\t"
      "global_load_dwordx4 v[48:51], %[voff], %[ws] sc1\n\t"
      "s_add_u32 %[sp], %[sp], 1\n\t"
      "s_waitcnt vmcnt(0)\n\t"
      MF(52, 0) "v_max3_u32 v84, v48, v49, v50\n\t"
      MF(56, 4) "v_max_u32 v84, v84, v51\n\t"
      MF(60, 8) "v_cmp_eq_u32 vcc, -1, v84\n\t"
      MF(64, 12)
      "s_cbranch_vccz Lk%=\n\t"
      "s_cmp_lt_u32 %[sp], %[cap]\n\t"
      "s_cbranch_scc1 Lp%=\n\t"
      "Lk%=:\n\t"
      : [sp] "+s"(spins) : [voff] "v"(voff), [ws] "s"(ws), [wb] "i"(WB), [cap] "i"(SPIN_CAP) : "memory", "vcc", "scc");
  } else {
    asm volatile("s_nop 1\n\t" MF(52, 0) "s_nop 0\n\t" MF(56, 4) "s_nop 0\n\t" MF(60, 8) "s_nop 0\n\t" MF(64, 12) "s_nop 0\n\t" :: [wb] "i"(WB));
  }
  asm volatile(
    MF(68, 16) "v_pk_add_f32 v[52:53], v[52:53], v[54:55]\n\t"
    MF(72, 20) "v_pk_add_f32 v[56:57], v[56:57], v[58:59]\n\t"
    MF(76, 24) "v_pk_add_f32 v[60:61], v[60:61], v[62:63]\n\t"
    MF(80, 28) "v_pk_add_f32 v[64:65], v[64:65], v[66:67]\n\t"
    "v_add_f32 v52, v52, v53\n\t"
    "v_pk_add_f32 v[68:69], v[68:69], v[70:71]\n\t"
    "v_add_f32 v56, v56, v57\n\t"
    "v_pk_add_f32 v[72:73], v[72:73], v[74:75]\n\t"
    "v_add_f32 v60, v60, v61\n\t"
    "v_pk_add_f32 v[76:77], v[76:77], v[78:79]\n\t"
    "v_add_f32 v64, v64, v65\n\t"
    "v_pk_add_f32 v[80:81], v[80:81], v[82:83]\n\t"
    "v_add_f32 v68, v68, v69\n\t"
    "v_add_f32 v72, v72, v73\n\t"
    "v_add_f32 v76, v76, v77\n\t"
    "v_add_f32 v80, v80, v81\n\t"
    "s_nop 0\n\t"
    FOLD_TAIL
    "ds_write_b32 %[lw], v52 offset:%c[lo]\n\t"
    :: [wb] "i"(WB), [pm] "s"(PMASK), [lw] "v"(ldsw), [lo] "i"(LOFF) : "memory");
}
template <int WB>
__device__ __forceinline__ void seg_p3(u32 voff, u32 voff2, const char* ws, const char* ws2, u32 ldsw, u32& spins) {
  asm volatile(
    SNOP "Lp%=:\n\t"
    "global_load_dwordx4 v[48:51], %[voff], %[ws] sc1\n\t"
    "global_load_dword v84, %[voff2], %[ws2] sc1\n\t"
    "s_add_u32 %[sp], %[sp], 1\n\t"
    "s_waitcnt vmcnt(0)\n\t"
    MF(52, 0) "v_max3_u32 v85, v48, v49, v50\n\t"
    MF(56, 4) "v_max3_u32 v85, v85, v51, v84\n\t"
    MF(60, 8) "v_cmp_eq_u32 vcc, -1, v85\n\t"
    MF(64, 12)
    "s_cbranch_vccz Lk%=\n\t"
    "s_cmp_lt_u32 %[sp], %[cap]\n\t"
    "s_cbranch_scc1 Lp%=\n\t"
    "Lk%=:\n\t"
    "v_max_f32_dpp v85, v84, v84 quad_perm:[1,0,3,2] row_mask:0xf bank_mask:0xf\n\t"
    MF(68, 16)
    "s_nop 0\n\t"
    "v_max_f32_dpp v85, v85, v85 quad_perm:[2,3,0,1] row_mask:0xf bank_mask:0xf\n\t"
    MF(72, 20)
    "s_nop 0\n\t"
    "v_max_f32_dpp v85, v85, v85 row_half_mirror row_mask:0xf bank_mask:0xf\n\t"
    MF(76, 24)
    "v_sub_f32 v86, v84, v85\n\t"
    MF(80, 28)
    "v_exp_f32 v86, v86\n\t"
    "s_nop 1\n\t"
    "v_mov_b32_dpp v90, v86 row_newbcast:0 row_mask:0x5 bank_mask:0xf\n\t"
    "v_mov_b32_dpp v91, v86 row_newbcast:1 row_mask:0x5 bank_mask:0xf\n\t"
    "v_mov_b32_dpp v92, v86 row_newbcast:2 row_mask:0x5 bank_mask:0xf\n\t"
    "v_mov_b32_dpp v93, v86 row_newbcast:3 row_mask:0x5 bank_mask:0xf\n\t"
    "v_mov_b32_dpp v90, v86 row_newbcast:4 row_mask:0xa bank_mask:0xf\n\t"
    "v_mov_b32_dpp v91, v86 row_newbcast:5 row_mask:0xa bank_mask:0xf\n\t"
    "v_mov_b32_dpp v92, v86 row_newbcast:6 row_mask:0xa bank_mask:0xf\n\t"
    "v_mov_b32_dpp v93, v86 row_newbcast:7 row_mask:0xa bank_mask:0xf\n\t"
    "v_add_f32_dpp v87, v86, v86 quad_perm:[1,0,3,2] row_mask:0xf bank_mask:0xf\n\t"
    "v_pk_mul_f32 v[52:53], v[52:53], v[90:91]\n\t"
    "v_pk_mul_f32 v[56:57], v[56:57], v[90:91]\n\t"
    "v_add_f32_dpp v87, v87, v87 quad_perm:[2,3,0,1] row_mask:0xf bank_mask:0xf\n\t"
    "v_pk_mul_f32 v[60:61], v[60:61], v[90:91]\n\t"
    "v_pk_mul_f32 v[64:65], v[64:65], v[90:91]\n\t"
    "v_add_f32_dpp v87, v87, v87 row_half_mirror row_mask:0xf bank_mask:0xf\n\t"
    "v_pk_mul_f32 v[68:69], v[68:69], v[90:91]\n\t"
    "v_pk_mul_f32 v[72:73], v[72:73], v[90:91]\n\t"
    "v_rcp_f32 v87, v87\n\t"
    "v_pk_mul_f32 v[76:77], v[76:77], v[90:91]\n\t"
    "v_pk_mul_f32 v[80:81], v[80:81], v[90:91]\n\t"
    "v_pk_fma_f32 v[52:53], v[54:55], v[92:93], v[52:53]\n\t"
    "v_pk_fma_f32 v[68:69], v[70:71], v[92:93], v[68:69]\n\t"
    "v_pk_fma_f32 v[56:57], v[58:59], v[92:93], v[56:57]\n\t"
    "v_pk_fma_f32 v[72:73], v[74:75], v[92:93], v[72:73]\n\t"
    "v_pk_fma_f32 v[60:61], v[62:63], v[92:93], v[60:61]\n\t"
    "v_pk_fma_f32 v[76:77], v[78:79], v[92:93], v[76:77]\n\t"
    "v_pk_fma_f32 v[64:65], v[66:67], v[92:93], v[64:65]\n\t"
    "v_pk_fma_f32 v[80:81], v[82:83], v[92:93], v[80:81]\n\t"
    "v_add_f32 v52, v52, v53\n\t"
    "v_add_f32 v68, v68, v69\n\t"
    "v_add_f32 v56, v56, v57\n\t"
    "v_add_f32 v72, v72, v73\n\t"
    "v_add_f32 v60, v60, v61\n\t"
    "v_add_f32 v76, v76, v77\n\t"
    "v_add_f32 v64, v64, v65\n\t"
    "v_add_f32 v80, v80, v81\n\t"
    "s_nop 1\n\t"
    FOLD_TAIL
    "v_mul_f32 v52, v52, v87\n\t"
    "ds_write_b32 %[lw], v52 offset:%c[lo]\n\t"
    : [sp] "+s"(spins)
    : [voff] "v"(voff), [voff2] "v"(voff2), [ws] "s"(ws), [ws2] "s"(ws2), [wb] "i"(WB), [cap] "i"(SPIN_CAP), [pm] "s"(PMASK), [lw] "v"(ldsw), [lo] "i"(L_RED3 - L_RED) : "memory", "vcc", "scc");
}
#define PACK_STORE \
  "s_nop 1\n\t" \
  "v_mov_b32_dpp v53, v52 quad_perm:[1,0,3,2] row_mask:0xf bank_mask:0xf\n\t" \
  "v_cvt_pk_bf16_f32 v54, v52, v53\n\t" \
  "v_min_u32 v54, -2, v54\n\t" \
  "s_nop 1\n\t" \
  "v_mov_b32_dpp v55, v54 row_shl:2 row_mask:0xf bank_mask:0xf\n\t" \
  "v_mov_b32_dpp v56, v54 row_shl:4 row_mask:0xf bank_mask:0xf\n\t" \
  "v_mov_b32_dpp v57, v54 row_shl:6 row_mask:0xf bank_mask:0xf\n\t" \
  "s_mov_b64 exec, %[c8z]\n\t" \
  "global_store_dwordx4 %[so], v[54:57], %[ws] sc1\n\t" \
  "s_mov_b64 exec, -1\n\t" \
  "s_nop 1\n\t"
template <bool XS>
__device__ __forceinline__ void seg_final2(u32 ldsr, u32 xsr, u32 so, const char* ws) {
  constexpr u32 LO = XS ? L_RED3 - L_RED : 0;
  if constexpr (XS) {
    asm volatile(
      "ds_read_b128 v[52:55], %[lr] offset:%c[lo]\n\t"
      "ds_read_b128 v[56:59], %[lr] offset:%c[lo]+16\n\t"
      "ds_read_b32 v60, %[xr]\n\t"
      "s_waitcnt lgkmcnt(1)\n\t"
      "v_pk_add_f32 v[52:53], v[52:53], v[56:57]\n\t"
      "v_pk_add_f32 v[54:55], v[54:55], v[58:59]\n\t"
      "v_pk_add_f32 v[52:53], v[52:53], v[54:55]\n\t"
      "s_waitcnt lgkmcnt(0)\n\t"
      "v_add_f32 v52, v52, v53\n\t"
      "v_add_f32 v52, v52, v60\n\t"
      PACK_STORE
      :: [lr] "v"(ldsr), [xr] "v"(xsr), [so] "v"(so), [ws] "s"(ws), [c8z] "s"(C8Z), [lo] "i"(LO) : "memory");
  } else {
    asm volatile(
      "ds_read_b128 v[52:55], %[lr]\n\t"
      "ds_read_b128 v[56:59], %[lr] offset:16\n\t"
      "s_waitcnt lgkmcnt(0)\n\t"
      "v_pk_add_f32 v[52:53], v[52:53], v[56:57]\n\t"
      "v_pk_add_f32 v[54:55], v[54:55], v[58:59]\n\t"
      "v_pk_add_f32 v[52:53], v[52:53], v[54:55]\n\t"
      "v_add_f32 v52, v52, v53\n\t"
      PACK_STORE
      :: [lr] "v"(ldsr), [so] "v"(so), [ws] "s"(ws), [c8z] "s"(C8Z) : "memory");
  }
}
template <int LOFF>
__device__ __forceinline__ void seg_qpoll(u32 voff, const char* ws, u32 ldsw, u32& spins) {
  asm volatile(
    SNOP "Lp%=:\n\t"
    "global_load_dwordx4 v[52:55], %[voff], %[ws] sc1\n\t"
    "s_add_u32 %[sp], %[sp], 1\n\t"
    "s_waitcnt vmcnt(0)\n\t"
    "v_max3_u32 v56, v52, v53, v54\n\t"
    "v_max_u32 v56, v56, v55\n\t"
    "v_cmp_eq_u32 vcc, -1, v56\n\t"
    "ds_write_b128 %[lw], v[52:55] offset:%c[lo]\n\t"
    "s_cbranch_vccz Lk%=\n\t"
    "s_cmp_lt_u32 %[sp], %[cap]\n\t"
    "s_cbranch_scc1 Lp%=\n\t"
    "Lk%=:\n\t"
    : [sp] "+s"(spins) : [voff] "v"(voff), [ws] "s"(ws), [lw] "v"(ldsw), [cap] "i"(SPIN_CAP), [lo] "i"(LOFF) : "memory", "vcc", "scc");
}
template <int TO>
__device__ __forceinline__ void seg_kpre(u32 ka0, u32 ka1) {
  asm volatile(
    "ds_read_b128 v[48:51], %0 offset:%c2\n\tds_read_b128 v[64:67], %1 offset:%c2\n\t"
    "ds_read_b128 v[52:55], %0 offset:%c2+512\n\tds_read_b128 v[68:71], %1 offset:%c2+512\n\t"
    "ds_read_b128 v[56:59], %0 offset:%c2+1024\n\tds_read_b128 v[72:75], %1 offset:%c2+1024\n\t"
    "ds_read_b128 v[60:63], %0 offset:%c2+1536\n\tds_read_b128 v[76:79], %1 offset:%c2+1536\n\t"
    "v_mov_b32 v96, 0\n\tv_mov_b32 v97, 0\n\tv_mov_b32 v98, 0\n\tv_mov_b32 v99, 0\n\t"
    "v_mov_b32 v100, 0\n\tv_mov_b32 v101, 0\n\tv_mov_b32 v102, 0\n\tv_mov_b32 v103, 0\n\t"
    "v_mov_b32 v104, 0\n\tv_mov_b32 v105, 0\n\tv_mov_b32 v106, 0\n\tv_mov_b32 v107, 0\n\t"
    "v_mov_b32 v108, 0\n\tv_mov_b32 v109, 0\n\tv_mov_b32 v110, 0\n\tv_mov_b32 v111, 0\n\t"
    :: "v"(ka0), "v"(ka1), "i"(TO) : "memory");
}
#define QK(s, k, q, c) "v_mfma_f32_16x16x32_bf16 v[" #s ":" #s "+3], v[" #k ":" #k "+3], v[" #q ":" #q "+3], " c "\n\t"
#define TR(d, a, off) "ds_read_b64_tr_b16 v[" #d ":" #d "+1], %[" #a "] offset:%c[to]+" #off "\n\t"
#define PV(r) "v_mfma_f32_16x16x32_bf16 v[" #r ":" #r "+3], v[124:127], v[" #r ":" #r "+3], 0\n\t"
#define SM(r) "v_fma_f32 v" #r ", v" #r ", v99, v121\n\tv_exp_f32 v" #r ", v" #r "\n\t"
#define PVA(acc, a, v, c) "v_mfma_f32_16x16x32_bf16 v[" #acc ":" #acc "+3], v[" #a ":" #a "+3], v[" #v ":" #v "+3], " c "\n\t"
#define PCP(d, s, ctl, bank) "v_mov_b32_dpp v" #d ", v" #s " " ctl " row_mask:0xf bank_mask:" bank "\n\t"
template <int TO>
__device__ __forceinline__ void seg_attn(u32 qa, u32 sta, u32 va0, u32 va1, u32 va2, u32 va3, u32 oa, u32 mla) {
  asm volatile(
    "ds_read_b128 v[80:83], %[qa]\n\t"
    "ds_read_b128 v[84:87], %[qa] offset:64\n\t"
    "ds_read_b128 v[88:91], %[qa] offset:128\n\t"
    "ds_read_b128 v[92:95], %[qa] offset:192\n\t"
    "ds_read_b64 v[120:121], %[sta]\n\t"
    "s_waitcnt lgkmcnt(4)\n\t"
    QK(112, 48, 80, "0") QK(116, 64, 80, "0")
    "s_waitcnt lgkmcnt(3)\n\t"
    QK(112, 52, 84, "v[112:115]") QK(116, 68, 84, "v[116:119]")
    "s_waitcnt lgkmcnt(2)\n\t"
    QK(112, 56, 88, "v[112:115]") QK(116, 72, 88, "v[116:119]")
    "s_waitcnt lgkmcnt(0)\n\t"
    QK(112, 60, 92, "v[112:115]")
    "v_add_f32_dpp v120, v120, v120 quad_perm:[1,0,3,2] row_mask:0xf bank_mask:0xf\n\t"
    "v_add_f32_dpp v121, v121, v121 quad_perm:[1,0,3,2] row_mask:0xf bank_mask:0xf\n\t"
    QK(116, 76, 92, "v[116:119]")
    TR(48, va0, 0)
    "v_add_f32_dpp v120, v120, v120 quad_perm:[2,3,0,1] row_mask:0xf bank_mask:0xf\n\t"
    "v_add_f32_dpp v121, v121, v121 quad_perm:[2,3,0,1] row_mask:0xf bank_mask:0xf\n\t"
    TR(50, va1, 0) TR(52, va2, 0)
    "v_add_f32_dpp v120, v120, v120 row_half_mirror row_mask:0xf bank_mask:0xf\n\t"
    "v_add_f32_dpp v121, v121, v121 row_half_mirror row_mask:0xf bank_mask:0xf\n\t"
    TR(54, va3, 0)
    "v_mul_f32 v122, 0xbb000000, v120\n\t"
    "v_mul_f32 v123, 0x3b000000, v121\n\t"
    "v_fma_f32 v123, -v122, v122, v123\n\t"
    "v_fmac_f32_dpp v112, v112, v122 row_shl:4 row_mask:0xf bank_mask:0xf bound_ctrl:1\n\t"
    "v_max_f32 v123, 0, v123\n\t"
    "v_fmac_f32_dpp v113, v113, v122 row_shl:4 row_mask:0xf bank_mask:0xf bound_ctrl:1\n\t"
    "v_add_f32 v123, 0x3727c5ac, v123\n\t"
    "v_fmac_f32_dpp v114, v114, v122 row_shl:4 row_mask:0xf bank_mask:0xf bound_ctrl:1\n\t"
    "v_rsq_f32 v123, v123\n\t"
    "v_fmac_f32_dpp v115, v115, v122 row_shl:4 row_mask:0xf bank_mask:0xf bound_ctrl:1\n\t"
    "v_fmac_f32_dpp v116, v116, v122 row_shl:4 row_mask:0xf bank_mask:0xf bound_ctrl:1\n\t"
    "v_max3_f32 v120, v112, v113, v114\n\t"
    "v_fmac_f32_dpp v117, v117, v122 row_shl:4 row_mask:0xf bank_mask:0xf bound_ctrl:1\n\t"
    "v_fmac_f32_dpp v118, v118, v122 row_shl:4 row_mask:0xf bank_mask:0xf bound_ctrl:1\n\t"
    "v_fmac_f32_dpp v119, v119, v122 row_shl:4 row_mask:0xf bank_mask:0xf bound_ctrl:1\n\t"
    "v_max3_f32 v120, v120, v115, v116\n\t"
    "v_mul_f32 v123, %[c2], v123\n\t"
    "v_max3_f32 v120, v120, v117, v118\n\t"
    "v_max_f32 v120, v120, v119\n\t"
    "v_mov_b32 v121, v120\n\t"
    TR(56, va0, 512) TR(58, va1, 512)
    "v_permlane16_swap_b32 v120, v121\n\t"
    "v_max_f32 v120, v120, v121\n\t"
    "v_mov_b32 v121, v120\n\t"
    TR(60, va2, 512) TR(62, va3, 512)
    "v_permlane32_swap_b32 v120, v121\n\t"
    "v_max_f32 v120, v120, v121\n\t"
    "v_mul_f32_e64 v121, -v120, v123\n\t"
    "v_mul_f32 v122, v120, v123\n\t"
    "v_fma_f32 v112, v112, v123, v121\n\t"
    "v_fma_f32 v113, v113, v123, v121\n\t"
    "v_exp_f32 v112, v112\n\t"
    TR(64, va0, 1024)
    "v_fma_f32 v114, v114, v123, v121\n\t"
    "v_exp_f32 v113, v113\n\t"
    TR(66, va1, 1024)
    "v_fma_f32 v115, v115, v123, v121\n\t"
    "v_exp_f32 v114, v114\n\t"
    TR(68, va2, 1024)
    "v_fma_f32 v116, v116, v123, v121\n\t"
    "v_exp_f32 v115, v115\n\t"
    TR(70, va3, 1024)
    "v_fma_f32 v117, v117, v123, v121\n\t"
    "v_exp_f32 v116, v116\n\t"
    "v_cvt_pk_bf16_f32 v124, v112, v113\n\t"
    "v_fma_f32 v118, v118, v123, v121\n\t"
    "v_exp_f32 v117, v117\n\t"
    TR(72, va0, 1536)
    "v_fma_f32 v119, v119, v123, v121\n\t"
    "v_exp_f32 v118, v118\n\t"
    "v_cvt_pk_bf16_f32 v125, v114, v115\n\t"
    "v_exp_f32 v119, v119\n\t"
    TR(74, va1, 1536) TR(76, va2, 1536) TR(78, va3, 1536)
    "v_cvt_pk_bf16_f32 v126, v116, v117\n\t"
    "v_cvt_pk_bf16_f32 v127, v118, v119\n\t"
    PCP(96, 124, "quad_perm:[0,1,2,3]", "0x1") PCP(100, 124, "row_shr:4", "0x2") PCP(104, 124, "quad_perm:[0,1,2,3]", "0x4") PCP(108, 124, "row_shr:4", "0x8")
    PCP(97, 125, "quad_perm:[0,1,2,3]", "0x1") PCP(101, 125, "row_shr:4", "0x2") PCP(105, 125, "quad_perm:[0,1,2,3]", "0x4") PCP(109, 125, "row_shr:4", "0x8")
    PCP(98, 126, "quad_perm:[0,1,2,3]", "0x1") PCP(102, 126, "row_shr:4", "0x2") PCP(106, 126, "quad_perm:[0,1,2,3]", "0x4") PCP(110, 126, "row_shr:4", "0x8")
    PCP(99, 127, "quad_perm:[0,1,2,3]", "0x1") PCP(103, 127, "row_shr:4", "0x2") PCP(107, 127, "quad_perm:[0,1,2,3]", "0x4") PCP(111, 127, "row_shr:4", "0x8")
    "v_mov_b32 v84, %[ones]\n\tv_mov_b32 v85, %[ones]\n\tv_mov_b32 v86, %[ones]\n\tv_mov_b32 v87, %[ones]\n\t"
    "s_waitcnt lgkmcnt(0)\n\t"
    PVA(112, 96, 48, "0") PVA(116, 96, 64, "0")
    PVA(112, 100, 52, "v[112:115]") PVA(116, 100, 68, "v[116:119]")
    PVA(112, 104, 56, "v[112:115]") PVA(116, 104, 72, "v[116:119]")
    PVA(112, 108, 60, "v[112:115]") PVA(116, 108, 76, "v[116:119]")
    "v_mfma_f32_16x16x32_bf16 v[80:83], v[84:87], v[124:127], 0\n\t"
    "s_nop 6\n\t"
    "ds_write_b128 %[oa], v[112:115]\n\t"
    "ds_write_b128 %[oa], v[116:119] offset:1024\n\t"
    "s_mov_b64 exec, 0xf\n\t"
    "ds_write2_b32 %[mla], v122, v80 offset0:0 offset1:16\n\t"
    "s_mov_b64 exec, -1\n\t"
    :: [qa] "v"(qa), [sta] "v"(sta), [va0] "v"(va0), [va1] "v"(va1), [va2] "v"(va2), [va3] "v"(va3), [oa] "v"(oa), [mla] "v"(mla),
       [c2] "s"(C2), [ones] "s"(ONES), [to] "i"(TO) : "memory");
}
__device__ __forceinline__ void seg_merge(u32 mla, u32 oa, u32 so, const char* ws) {
  asm volatile(
    "ds_read_b128 v[48:51], %[mla]\n\t"
    "ds_read_b128 v[60:63], %[mla] offset:64\n\t"
    "ds_read2st64_b32 v[56:57], %[oa] offset0:0 offset1:8\n\t"
    "ds_read2st64_b32 v[58:59], %[oa] offset0:16 offset1:24\n\t"
    "s_waitcnt lgkmcnt(3)\n\t"
    "v_max3_f32 v52, v48, v49, v50\n\t"
    "v_max_f32 v52, v52, v51\n\t"
    "v_sub_f32 v48, v48, v52\n\t"
    "v_sub_f32 v49, v49, v52\n\t"
    "v_exp_f32 v48, v48\n\t"
    "v_sub_f32 v50, v50, v52\n\t"
    "v_exp_f32 v49, v49\n\t"
    "v_sub_f32 v51, v51, v52\n\t"
    "v_exp_f32 v50, v50\n\t"
    "v_exp_f32 v51, v51\n\t"
    "s_waitcnt lgkmcnt(0)\n\t"
    "v_pk_mul_f32 v[60:61], v[60:61], v[48:49]\n\t"
    "v_pk_mul_f32 v[56:57], v[56:57], v[48:49]\n\t"
    "v_pk_fma_f32 v[60:61], v[62:63], v[50:51], v[60:61]\n\t"
    "v_pk_fma_f32 v[56:57], v[58:59], v[50:51], v[56:57]\n\t"
    "v_add_f32 v60, v60, v61\n\t"
    "v_add_f32 v56, v56, v57\n\t"
    "v_rcp_f32 v60, v60\n\t"
    "s_nop 0\n\t"
    "v_mul_f32 v52, v56, v60\n\t"
    PACK_STORE
    :: [mla] "v"(mla), [oa] "v"(oa), [so] "v"(so), [ws] "s"(ws), [c8z] "s"(C8Z) : "memory");
}

__device__ __forceinline__ void dma16(u32 voff, const char* sbase, u32 lds_dst) {
  u32 keep;
  asm volatile(SNOP "s_mov_b32 %0, m0\n\ts_mov_b32 m0, %3\n\ts_nop 0\n\tglobal_load_lds_dwordx4 %1, %2" NTKV "\n\ts_mov_b32 m0, %0"
               : "=&s"(keep) : "v"(voff), "s"(sbase), "s"(lds_dst) : "memory");
}
__device__ __forceinline__ void kv_load(const char* base, int w, int lane, u32 ldsb) {
  asm volatile("" : "+s"(base));
  const int r8 = (lane & 31) >> 2;
#pragma unroll
  for (int i = 0; i < 4; ++i) {
    const u32 ch = 8 * (i & 1) + 4 * (lane >> 5) + ((lane & 3) ^ ((2 * (i >> 1) + (r8 >> 2)) & 3));
    dma16(r8 * 256 + ch * 16, base + (size_t)(16 * w + 8 * (i >> 1)) * 256, ldsb + w * 4096 + i * 1024);
  }
}
__device__ __forceinline__ bool has_sent(v4u x) {
  const u32 m = __builtin_elementwise_max(__builtin_elementwise_max(x[0], x[1]), __builtin_elementwise_max(x[2], x[3]));
  return __builtin_amdgcn_ballot_w64(m == SENT) != 0ull;
}
__device__ __forceinline__ v4u poll128(const char* base, u32 voff) {
  v4u x;
  for (u32 spins = 0;; ++spins) {
    asm volatile("s_nop 4\n\tglobal_load_dwordx4 %0, %1, %2 sc1\n\ts_waitcnt vmcnt(0)" : "=&v"(x) : "v"(voff), "s"(base) : "memory");
    if (!has_sent(x) || spins > SPIN_CAP) break;
  }
  return x;
}
__device__ __forceinline__ v4u pack8(float v) {
  const u32 x = safe(pk(v, dpp<XOR1>(v)));
  return v4u{x, dppu<0x102>(x), dppu<0x104>(x), dppu<0x106>(x)};
}
__device__ __forceinline__ void sum_partials(v4u x, float (&v)[8]) {
#pragma unroll
  for (int m = 0; m < 4; ++m) { v[2 * m] = bflo(x[m]); v[2 * m + 1] = bfhi(x[m]); }
#pragma unroll
  for (int j = 0; j < 8; ++j) v[j] += dpp<XOR1>(v[j]);
#pragma unroll
  for (int j = 0; j < 8; ++j) v[j] += dpp<XOR2>(v[j]);
#pragma unroll
  for (int j = 0; j < 8; ++j) v[j] += dpp<HMIR>(v[j]);
}
__device__ __forceinline__ void x_stats(v4u xa, char* lds, int w, int lane) {
  const bool p = (lane >> 3) & 1; const int kq = lane >> 4;
  const v4f gm = mfma(xa, xa, v4f{0.f, 0.f, 0.f, 0.f});
  const v4f sm = mfma(xa, v4u{ONES, ONES, ONES, ONES}, v4f{0.f, 0.f, 0.f, 0.f});
  float s2 = (gm[0] + gm[1]) + (gm[2] + gm[3]);
  s2 = (p == (bool)(kq >> 1)) ? s2 : 0.f;
  s2 += dpp<XOR1>(s2); s2 += dpp<XOR2>(s2); s2 += dpp<HMIR>(s2); s2 += dpp<ROR8>(s2);
  s2 = x32_add(x16_add(s2));
  float s1 = (sm[0] + sm[1]) + (sm[2] + sm[3]);
  s1 = x32_add(x16_add(s1));
  if (lane == 0) *(v2f*)(lds + L_ST + w * 8) = v2f{s1, s2};
}
}

#define ST(i) do {} while (0)
#define STFLUSH(sv, base) do {} while (0)
#define SGPR_ATTR

__device__ __forceinline__ u32 lb_st() { return L_ST; }
#define OPAQUE_LANE() int lane = lane0; asm volatile("" : "+v"(lane)); const int c8 = lane & 7, kq = lane >> 4; const bool p = (lane >> 3) & 1; (void)c8; (void)p; (void)kq
#define OPAQUE_S(x) asm volatile("" : "+s"(x))

constexpr int FW = 4;

struct Addr {
  u32 vB, vA, vL, lw, so;
  u32 mr, orr;
  u32 r0, r1, r2, r3, r4, r5, r6, r7, r8;
};
#define PIN(x) asm volatile("" : "+v"(x))

template <int TYPE>
__device__ __forceinline__ void layer(const int rep, char* ws, char* lds, int b, int g, int s, int w, const int lane0, v4u xraw, Addr& A, u32 qaT,
                                      const char* nkc, const char* nvc, const char* nwq, const char* cwo, const char* nwo, const bool lead,
                                      const u32 tag, int& stampv, u32 (&cnt)[3]) {
  constexpr int RQ = TYPE ? R_WQB : R_WQA, RO = TYPE ? R_WOB : R_WOA;
  const int L = rep * 2 + TYPE;
  OPAQUE_S(b); OPAQUE_S(g); OPAQUE_S(s); OPAQUE_S(w);
  const u32 grp = (u32)(b * 8 + g), cu = grp * 8 + s;
#define LD_WQ(R, ptr) do { OPAQUE_LANE(); OPAQUE_S(ptr); bulk8<R, 8192>(ptr + (size_t)(64 * w) * 8192 + (512 * g + 64 * s) * 2, (u32)(lane >> 3) * 65536 + c8 * 16); } while (0)
#define LD_WO(R, ptr) do { OPAQUE_LANE(); OPAQUE_S(ptr); bulk8<R, 1024>(ptr + (size_t)(512 * g + 64 * w) * 1024 + 128 * s, (u32)(lane >> 3) * 8192 + c8 * 16); } while (0)
  const char* wsL = ws + L * BOXL;
  const char* box = ws + WS_BOX + cu * BOX + L * BOXL;
  {
    if (rep == 0) {
      if (SCHED == 2) VMWAIT(0);
      else if (SCHED == 0) { if (TYPE == 0) { if (PRETOUCH && w == 7) VMWAIT(13); else VMWAIT(8); } else VMWAIT(0); }
      else { if (TYPE == 0) { if (!lead) VMWAIT(4); } else { VMWAIT(4); } }
      if (w >= 4) ST(7);
      transpose8<RQ>();
    }
    if (L > 0) {
      const char* wsP = wsL - BOXL;
      if (DLY_B && rep) __builtin_amdgcn_s_sleep(DLY_B);
      ST(0);
      u32 sp = 0;
      seg_p1<RQ, true, 0>(A.vB, wsP, A.lw, sp);
      cnt[0] += sp;
    } else {
      const v4u xa = (lane0 & 7) == 0 ? xraw : v4u{0u, 0u, 0u, 0u};
      asm volatile("v_mov_b32 v48, %0\n\tv_mov_b32 v49, %1\n\tv_mov_b32 v50, %2\n\tv_mov_b32 v51, %3" :: "v"(xa[0]), "v"(xa[1]), "v"(xa[2]), "v"(xa[3]));
      u32 sp = 0;
      seg_p1<RQ, false, 0>(0, ws, A.lw, sp);
    }
    ST(2);
    if (SCHED != 2 && rep == 0 && TYPE == 1) {
      OPAQUE_LANE();
      if (SCHED == 0) kv_load(nkc, w, lane, L_K + 32768);
      kv_load(nvc, w, lane, L_V + 32768);
    }
    v4u xa;
    asm volatile("v_mov_b32 %0, v48\n\tv_mov_b32 %1, v49\n\tv_mov_b32 %2, v50\n\tv_mov_b32 %3, v51" : "=v"(xa[0]), "=v"(xa[1]), "=v"(xa[2]), "=v"(xa[3]));
    if (rep == 0) {
      asm volatile("v_mov_b32 v48, %0\n\tv_mov_b32 v49, %0\n\tv_mov_b32 v50, %0\n\tv_mov_b32 v51, %0" :: "s"(ONES));
      u32 sp = 0;
      seg_p1<RQ, false, L_REDC - L_RED>(0, ws, A.lw, sp);
    }
    BAR();
    ST(3);
    if (w == FW) {
      seg_final2<false>(A.r2, 0, A.so, box + BOX_Q);
      ST(4);
      OPAQUE_LANE();
      if (rep == 0) {
        const __amdgpu_buffer_rsrc_t rs = __builtin_amdgcn_make_buffer_rsrc(ws, 0, WS_END, 0x00020000);
        const v4f r0 = *(const v4f*)(lds + L_REDC + lane * 32), r1 = *(const v4f*)(lds + L_REDC + lane * 32 + 16);
        const float cs = 0.125f * (((r0[0] + r0[1]) + (r0[2] + r0[3])) + ((r1[0] + r1[1]) + (r1[2] + r1[3])));
        const v4u pc = pack8(cs);
        if (c8 == 0) __builtin_amdgcn_raw_buffer_store_b128(pc, rs, WS_BOX + cu * BOX + BOX_CS + TYPE * 128 + lane * 2, 0, 16);
      }
      x_stats(xa, lds, w, lane);
      if (L == 0) {
        const u64 flag = (u64)tag | ((u64)(~tag) << 32);
        for (u32 spins = 0;; ++spins) {
          const u64 f = __hip_atomic_load((GAS u64*)(ws + WS_RDY + b * 512 + lane * 8), __ATOMIC_RELAXED, __HIP_MEMORY_SCOPE_AGENT);
          if (__all(f == flag) || spins > SPIN_CAP) break;
        }
        if (g == 0 && s == 0 && lane == 0) __hip_atomic_store((GAS u32*)(ws + WS_EPOCH + b * 256), tag, __ATOMIC_RELAXED, __HIP_MEMORY_SCOPE_AGENT);
      }
      if (DLY_C) __builtin_amdgcn_s_sleep(DLY_C);
      u32 sp = 0;
      seg_qpoll<0>(A.r0, wsL, A.r1, sp);
      cnt[1] += sp;
      if (rep == 0) { sp = 0; seg_qpoll<L_CSI + TYPE * 1088 - L_Q>(A.r0, ws + (BOX_CS + TYPE * 128 - BOX_Q), A.r1, sp); }
      ST(5);
    } else {
      OPAQUE_LANE();
      x_stats(xa, lds, w, lane);
    }
    if (g == 0 && w == s) {
      OPAQUE_LANE();
      float v[8];
      sum_partials(xa, v);
      if (c8 == 0) {
        float* xs = (float*)(lds + L_XS) + 16 * kq + (p ? 8 : 0);
        *(v4f*)xs = v4f{v[0], v[1], v[2], v[3]};
        *(v4f*)(xs + 4) = v4f{v[4], v[5], v[6], v[7]};
      }
    }
    if (rep == 0) {
      if (SCHED == 1 && TYPE == 0 && lead) VMWAIT(16); else VMWAIT(0);
      if (w >= 4) ST(8);
      BAR();
      if (SCHED == 2) {}
      else if (SCHED == 0) LD_WO(RO, cwo);
      else {
        if (!lead) LD_WO(RO, cwo);
        else if (TYPE == 0) LD_WO(R_WOB, nwo);
      }
    }
  }
  if (w < 4) {
    seg_kpre<TYPE * 32768>(A.r0, A.r1);
    ST(6);
    BAR();
    ST(7);
    seg_attn<TYPE * 32768>(qaT, A.r6, A.r2, A.r3, A.r4, A.r5, A.r7, A.r8);
    ST(8);
  } else {
    BAR();
  }
  {
    BAR();
    seg_merge(A.mr, A.orr, A.so, box + w * 128);
    if (w == 7) {
      OPAQUE_LANE();
      const __amdgpu_buffer_rsrc_t rs = __builtin_amdgcn_make_buffer_rsrc(ws, 0, WS_END, 0x00020000);
      const int hh = (lane >> 2) & 3, aa = lane & 3;
      const float m1 = ((const float*)(lds + L_ML))[hh * 4 + aa], l1 = ((const float*)(lds + L_ML))[16 + hh * 4 + aa];
      float M1 = __builtin_fmaxf(m1, dpp<XOR1>(m1)); M1 = __builtin_fmaxf(M1, dpp<XOR2>(M1));
      float L1 = __builtin_amdgcn_exp2f(m1 - M1) * l1; L1 += dpp<XOR1>(L1); L1 += dpp<XOR2>(L1);
      const u32 e = safe(__float_as_uint(M1 + __builtin_amdgcn_logf(L1)));
      const v4u ev = {dppu<0x150>(e), dppu<0x154>(e), dppu<0x158>(e), dppu<0x15C>(e)};
      if (lane < 8) __builtin_amdgcn_raw_buffer_store_b128(ev, rs, WS_LSR + ((L * 32 + grp) * 8 + lane) * 128 + s * 16, 0, 16);
    }
    ST(9);
  }
  {
    const char* wsS = ws + L * 32768;
    if (rep == 0) {
      if (SCHED == 1 && TYPE == 0 && lead) VMWAIT(8); else VMWAIT(0);
      transpose8<RO>();
    } else {
      if (DLY_A) __builtin_amdgcn_s_sleep(DLY_A);
    }
    ST(1);
    u32 sp = 0;
    seg_p3<RO>(A.vA, A.vL, wsL, wsS, A.lw, sp);
    cnt[2] += sp;
    ST(10);
    if (SCHED != 2 && rep == 0 && TYPE == 0) {
      if (SCHED == 0) LD_WQ(R_WQB, nwq);
      else {
        if (!lead) LD_WQ(R_WQB, nwq);
        OPAQUE_LANE();
        kv_load(nkc, w, lane, L_K + 32768);
      }
    }
    BAR();
    if (w == FW) seg_final2<true>(A.r2, A.r3, A.so, box + BOX_X);
    ST(11);
  }
  STFLUSH(stampv, L * 12);
}

__global__ void __launch_bounds__(512, 2) __attribute__((amdgpu_num_vgpr(24) SGPR_ATTR))
fused_kernel(const char* __restrict__ xin, const char* kA, const char* vA, const char* wqA, const char* woA,
             const char* kB, const char* vB, const char* wqB, const char* woB, unsigned short* out, char* ws) {
  extern __shared__ __attribute__((aligned(16))) char lds[];
  const int id = blockIdx.x, g = id & 7, b = id >> 6, s = (id >> 3) & 7;
  const int tid = threadIdx.x, lane = tid & 63, w = __builtin_amdgcn_readfirstlane(tid >> 6);
  const int c8 = lane & 7, kq = lane >> 4;
  const bool p = (lane >> 3) & 1;
  int stampv = 0;
  const __amdgpu_buffer_rsrc_t rs = __builtin_amdgcn_make_buffer_rsrc(ws, 0, WS_END, 0x00020000);
  const u32 cu = (u32)((b * 8 + g) * 8 + s);
  asm volatile("" ::: "v127", "v255");
  u32 ep; v4u xraw;
  asm volatile("s_nop 4\n\tglobal_load_dword v56, %0, %1 sc1\n\tglobal_load_dwordx4 v[52:55], %2, %3"
               :: "v"(WS_EPOCH + b * 256), "s"(ws), "v"((u32)((b * 512 + 64 * w + 16 * kq + (p ? 8 : 0)) * 2)), "s"(xin) : "memory");
  if (w == 5) {
    const v4u sv = {SENT, SENT, SENT, SENT};
#pragma unroll
    for (int i = 0; i < 5; ++i) __builtin_amdgcn_raw_buffer_store_b128(sv, rs, WS_BOX + cu * BOX + i * 1024 + lane * 16, 0, 16);
    const u32 o6 = lane < 16 ? WS_BOX + cu * BOX + 5120 + lane * 16
                             : WS_LSR + (u32)(((((lane - 16) >> 3) * 32 + b * 8 + g) * 8 + (lane & 7)) * 128 + s * 16);
    if (lane < 48) __builtin_amdgcn_raw_buffer_store_b128(sv, rs, o6, 0, 16);
  }
  if (w == 6) ((float*)(lds + L_XS))[lane] = 0.f;
  const size_t kvoff = ((size_t)(b * 8 + g) * SK + (SK - WIN) + s * 128) * 256;
  const bool lead = (w >> 1) == b;
  const size_t qoff = (size_t)(64 * w) * 8192 + (512 * g + 64 * s) * 2, ooff = (size_t)(512 * g + 64 * w) * 1024 + 128 * s;
  const u32 vq = (u32)(lane >> 3) * 65536 + c8 * 16, vo = (u32)(lane >> 3) * 8192 + c8 * 16;
  if (SCHED == 0 || SCHED == 2 || lead) {
    bulk8<R_WQA, 8192>(wqA + qoff, vq);
    VMWAIT(8);
  } else {
    VMWAIT(0);
  }
  asm volatile("v_mov_b32 %0, v52\n\tv_mov_b32 %1, v53\n\tv_mov_b32 %2, v54\n\tv_mov_b32 %3, v55\n\tv_mov_b32 %4, v56"
               : "=v"(xraw[0]), "=v"(xraw[1]), "=v"(xraw[2]), "=v"(xraw[3]), "=v"(ep));
  ST(6);
  const u32 tag = (u32)__builtin_amdgcn_readfirstlane((int)ep) + 1u;
  if (w == 5 && lane == 0) {
    const u64 flag = (u64)tag | ((u64)(~tag) << 32);
    __hip_atomic_store((GAS u64*)(ws + WS_RDY + b * 512 + (s * 8 + g) * 8), flag, __ATOMIC_RELAXED, __HIP_MEMORY_SCOPE_AGENT);
  }
  if (SCHED == 0) {
    asm volatile("s_barrier" ::: "memory");
    kv_load(kA + kvoff, w, lane, L_K);
    kv_load(vA + kvoff, w, lane, L_V);
  } else if (SCHED == 2) {
    kv_load(kA + kvoff, w, lane, L_K);
    kv_load(vA + kvoff, w, lane, L_V);
    bulk8<R_WOA, 1024>(woA + ooff, vo);
    bulk8<R_WQB, 8192>(wqB + qoff, vq);
    kv_load(kB + kvoff, w, lane, L_K + 32768);
    kv_load(vB + kvoff, w, lane, L_V + 32768);
    bulk8<R_WOB, 1024>(woB + ooff, vo);
    if (EXTRA == 1) {
      const char* e = kA + ((size_t)(b * 8 + g) * SK + s * 64 * 4 + w * 32) * 256;
      bulk8<64, 1024>(e, (u32)lane * 16);
      const char* e2 = vA + ((size_t)(b * 8 + g) * SK + s * 64 * 4 + w * 32) * 256;
      bulk8<64, 1024>(e2, (u32)lane * 16);
    } else if (EXTRA == 2) {
      const char* e = kB + ((size_t)(g * 8 + s) * 8 + w) * 16384;
      bulk8<64, 1024>(e, (u32)lane * 16);
      bulk8<64, 1024>(e + 8192, (u32)lane * 16);
    }
  } else {
    kv_load(kA + kvoff, w, lane, L_K);
    if (lead) VMWAIT(4);
    asm volatile("s_barrier" ::: "memory");
    if (!lead) bulk8<R_WQA, 8192>(wqA + qoff, vq);
    kv_load(vA + kvoff, w, lane, L_V);
    if (lead) { bulk8<R_WOA, 1024>(woA + ooff, vo); bulk8<R_WQB, 8192>(wqB + qoff, vq); }
  }
  if (PRETOUCH && w == 7) {
    const size_t kvo = ((size_t)(b * 8 + g) * SK + (SK - WIN) + s * 128) * 256;
    asm volatile("s_nop 4\n\tglobal_load_dword v100, %0, %2\n\tglobal_load_dword v101, %1, %3\n\tglobal_load_dword v102, %1, %4\n\t"
                 "global_load_dword v103, %5, %6\n\tglobal_load_dword v104, %5, %7"
                 :: "v"((u32)(lane * 65536 + (512 * g + 64 * s) * 2)), "v"((u32)((512 * g + lane * 8) * 1024 + 128 * s)), "s"(wqB), "s"(woA), "s"(woB),
                    "v"((u32)(lane * 512)), "s"(kB + kvo), "s"(vB + kvo) : "memory");
  }
  Addr A; u32 qa0, qa1;
  {
    const u32 grp = (u32)(b * 8 + g);
    const u32 oct = kq * 32 + (p ? 16 : 0);
    A.vB = WS_BOX + (u32)((b * 8 + c8) * 8 + w) * BOX + BOX_X + oct;
    A.vA = WS_BOX + (grp * 8 + c8) * BOX + w * 128 + oct;
    A.vL = WS_LSR + (grp * 8 + s) * 128 + c8 * 16 + (w >> 1) * 4;
    A.lw = L_RED + (8 * c8 + (lane >> 3)) * 32 + w * 4;
    A.so = lane * 2;
    A.mr = L_ML + (w >> 1) * 16;
    A.orr = L_OM + (w & 1) * 1024 + lane * 16 + (w >> 1) * 4;
    const int r16 = lane & 15, q4 = r16 >> 2, p2 = lane & 3;
    qa0 = L_Q + (lane & 3) * 272 + kq * 16;
    qa1 = qa0;
    if (lane & 4) { qa0 += L_CSI - L_Q; qa1 += L_CSI + 1088 - L_Q; }
    if (w < 4) {
      const u32 kb = L_K + 2048 * (4 * w + (r16 >> 2)) + 64 * (r16 & 3);
      A.r0 = kb + 16 * (kq ^ ((2 * (r16 >> 2)) & 3));
      A.r1 = kb + 256 + 16 * (kq ^ ((2 * (r16 >> 2) + 1) & 3));
      const u32 vb = L_V + 2048 * (4 * w + kq) + 64 * q4 + 8 * (p2 & 1);
      A.r2 = vb + 16 * (((p2 >> 1)) ^ ((2 * kq) & 3));
      A.r3 = vb + 256 + 16 * (((p2 >> 1)) ^ ((2 * kq + 1) & 3));
      A.r4 = vb + 16 * ((2 + (p2 >> 1)) ^ ((2 * kq) & 3));
      A.r5 = vb + 256 + 16 * ((2 + (p2 >> 1)) ^ ((2 * kq + 1) & 3));
      A.r7 = L_OM + w * 2048 + lane * 16;
      A.r8 = L_ML + (lane * 4 + w) * 4;
    } else {
      A.r0 = WS_BOX + (grp * 8 + (lane >> 3)) * BOX + BOX_Q + c8 * 16;
      A.r1 = L_Q + kq * 272 + (lane & 15) * 16;
      A.r2 = L_RED + lane * 32;
      A.r3 = L_XS + lane * 4;
      A.r4 = A.r5 = A.r7 = A.r8 = 0;
    }
    A.r6 = L_ST + c8 * 8;
    PIN(A.r6);
    PIN(A.vB); PIN(A.vA); PIN(A.vL); PIN(A.lw); PIN(A.so); PIN(A.mr); PIN(A.orr); PIN(A.r0); PIN(A.r1); PIN(A.r2); PIN(A.r3); PIN(A.r4); PIN(A.r5); PIN(A.r7); PIN(A.r8);
    PIN(qa0); PIN(qa1);
  }
  u32 cnt[3] = {0, 0, 0};
  for (int rep = 0; rep < 2; ++rep) {
    layer<0>(rep, ws, lds, b, g, s, w, lane, xraw, A, qa0, kB + kvoff, nullptr, wqB, woA, woB, lead, tag, stampv, cnt);
    layer<1>(rep, ws, lds, b, g, s, w, lane, xraw, A, qa1, kB + kvoff, vB + kvoff, nullptr, woB, nullptr, lead, tag, stampv, cnt);
  }
  if (g == 0 && w == s) {
    const u32 voff = WS_BOX + (u32)((b * 8 + c8) * 8 + w) * BOX + 3u * BOXL + BOX_X + kq * 32 + (p ? 16 : 0);
    if (DLY_B) __builtin_amdgcn_s_sleep(DLY_B);
    const v4u x = poll128(ws, voff);
    float v[8];
    sum_partials(x, v);
    if (c8 == 0) *(v4u*)(out + b * 512 + 64 * w + 16 * kq + (p ? 8 : 0)) = v4u{pk(v[0], v[1]), pk(v[2], v[3]), pk(v[4], v[5]), pk(v[6], v[7])};
  }
}

extern "C" void kernel_launch(void* const* d_in, const int* in_sizes, int n_in, void* d_out, int out_size, void* d_ws, size_t ws_size,
                              hipStream_t stream) {
  (void)hipFuncSetAttribute((const void*)fused_kernel, hipFuncAttributeMaxDynamicSharedMemorySize, L_END);
  fused_kernel<<<256, 512, L_END, stream>>>((const char*)d_in[0], (const char*)d_in[1], (const char*)d_in[2], (const char*)d_in[3], (const char*)d_in[4],
                                            (const char*)d_in[6], (const char*)d_in[7], (const char*)d_in[8], (const char*)d_in[9],
                                            (unsigned short*)d_out, (char*)d_ws);
}
```

```cpp
#include <hip/hip_runtime.h>
#include <stdint.h>

typedef unsigned int u32;
typedef unsigned long long u64;
typedef u32 v4u __attribute__((ext_vector_type(4)));
typedef u32 v2u __attribute__((ext_vector_type(2)));
typedef float v4f __attribute__((ext_vector_type(4)));
typedef float v2f __attribute__((ext_vector_type(2)));
typedef __bf16 v8b __attribute__((ext_vector_type(8)));
typedef __bf16 v2b __attribute__((ext_vector_type(2)));
#define GAS __attribute__((address_space(1)))

#define DLY_C 2
#define DLY_A 8
#define DLY_B 10
#define SCHED 2
#define EXTRA 0
#define TOUCHBOX 0
#define KVDELAY 0
#define PRETOUCH 0
#define NTKV ""
#define NTW ""

namespace {
constexpr int SK = 4096, WIN = 1024;
constexpr u32 WS_EPOCH = 0;
constexpr u32 WS_RDY = 4096;
constexpr u32 WS_LSR = 8192;
constexpr u32 WS_BOX = WS_LSR + 131072;
constexpr u32 BOXL = 1280, BOX_Q = 1024, BOX_X = 1152, BOX_CS = 5120, BOX = 5376;
constexpr u32 WS_END = WS_BOX + 256 * BOX;
constexpr u32 WS_DBG = 4u << 20;
constexpr u32 L_KV = 0;
constexpr u32 L_K = 0, L_V = 65536;
constexpr u32 L_OM = 131072;
constexpr u32 L_ML = L_OM + 8192;
constexpr u32 L_Q = L_ML + 128;
constexpr u32 L_CSI = L_Q + 1088;
constexpr u32 L_RED = L_CSI + 2176;
constexpr u32 L_REDC = L_RED + 2048;
constexpr u32 L_ST = L_REDC + 2048;
constexpr u32 L_RED3 = L_ST + 64;
constexpr u32 L_XS = L_RED3 + 2048;
constexpr u32 L_END = L_XS + 256;
static_assert(L_END <= 160 * 1024, "lds");
static_assert(L_Q % 16 == 0 && L_CSI % 16 == 0 && L_RED % 16 == 0 && L_ST % 16 == 0 && L_RED3 % 16 == 0 && L_XS % 16 == 0, "align");
constexpr u32 SENT = 0xFFFFFFFFu;
constexpr u32 SPIN_CAP = 1u << 15;
constexpr float C2 = 0.08838834764831845f * 1.4426950408889634f;
constexpr u32 ONES = 0x3F803F80u;
constexpr u64 PMASK = 0xFF00FF00FF00FF00ull;
constexpr u64 C8Z = 0x0101010101010101ull;

__device__ __forceinline__ float bflo(u32 w) { return __uint_as_float(w << 16); }
__device__ __forceinline__ float bfhi(u32 w) { return __uint_as_float(w & 0xffff0000u); }
__device__ __forceinline__ u32 pk(float lo, float hi) { v2f f = {lo, hi}; return __builtin_bit_cast(u32, __builtin_convertvector(f, v2b)); }
__device__ __forceinline__ u32 safe(u32 v) { return v < 0xFFFFFFFEu ? v : 0xFFFFFFFEu; }
template <int CTRL> __device__ __forceinline__ float dpp(float x) {
  return __builtin_bit_cast(float, __builtin_amdgcn_mov_dpp(__builtin_bit_cast(int, x), CTRL, 0xf, 0xf, true));
}
template <int CTRL> __device__ __forceinline__ u32 dppu(u32 x) { return (u32)__builtin_amdgcn_mov_dpp((int)x, CTRL, 0xf, 0xf, true); }
constexpr int XOR1 = 0xB1, XOR2 = 0x4E, HMIR = 0x141, ROR8 = 0x128;
__device__ __forceinline__ float x16_add(float x) { auto s = __builtin_amdgcn_permlane16_swap(__float_as_uint(x), __float_as_uint(x), false, false); return __uint_as_float(s[0]) + __uint_as_float(s[1]); }
__device__ __forceinline__ float x32_add(float x) { auto s = __builtin_amdgcn_permlane32_swap(__float_as_uint(x), __float_as_uint(x), false, false); return __uint_as_float(s[0]) + __uint_as_float(s[1]); }
__device__ __forceinline__ v4f mfma(v4u a, v4u b, v4f c) {
  return __builtin_amdgcn_mfma_f32_16x16x32_bf16(__builtin_bit_cast(v8b, a), __builtin_bit_cast(v8b, b), c, 0, 0, 0);
}
#define BAR() asm volatile("s_waitcnt lgkmcnt(0)\n\ts_barrier" ::: "memory")
#define VMWAIT(n) asm volatile("s_waitcnt vmcnt(" #n ")" ::: "memory")

constexpr int R_WQA = 128, R_WOA = 160, R_WQB = 192, R_WOB = 224;
template <int BASE, u32 STRIDE>
__device__ __forceinline__ void bulk8(const char* base, u32 voff) {
  asm volatile("" : "+s"(base));
  const __amdgpu_buffer_rsrc_t r = __builtin_amdgcn_make_buffer_rsrc((void*)base, 0, 0x7fffffff, 0x00020000);
  asm volatile("s_nop 4\n\t"
               "buffer_load_dwordx4 v[%c10:%c11], %0, %1, 0 offen" NTW "\n\tbuffer_load_dwordx4 v[%c12:%c13], %0, %1, %3 offen" NTW "\n\t"
               "buffer_load_dwordx4 v[%c14:%c15], %0, %1, %4 offen" NTW "\n\tbuffer_load_dwordx4 v[%c16:%c17], %0, %1, %5 offen" NTW "\n\t"
               "buffer_load_dwordx4 v[%c18:%c19], %0, %1, %6 offen" NTW "\n\tbuffer_load_dwordx4 v[%c20:%c21], %0, %1, %7 offen" NTW "\n\t"
               "buffer_load_dwordx4 v[%c22:%c23], %0, %1, %8 offen" NTW "\n\tbuffer_load_dwordx4 v[%c24:%c25], %0, %1, %9 offen" NTW
               :: "v"(voff), "s"(r), "s"(0), "s"(STRIDE), "s"(2 * STRIDE), "s"(3 * STRIDE), "s"(4 * STRIDE), "s"(5 * STRIDE), "s"(6 * STRIDE), "s"(7 * STRIDE),
                  "i"(BASE), "i"(BASE + 3), "i"(BASE + 4), "i"(BASE + 7), "i"(BASE + 8), "i"(BASE + 11), "i"(BASE + 12), "i"(BASE + 15),
                  "i"(BASE + 16), "i"(BASE + 19), "i"(BASE + 20), "i"(BASE + 23), "i"(BASE + 24), "i"(BASE + 27), "i"(BASE + 28), "i"(BASE + 31)
               : "memory");
}
template <int B, int M, int R>
__device__ __forceinline__ void tp_pair(u32 slo, u32 shi) {
  if constexpr (M == R) {
    asm volatile("v_perm_b32 v48, v%c3, v%c2, %0\n\tv_perm_b32 v%c3, v%c3, v%c2, %1\n\tv_mov_b32 v%c2, v48"
                 :: "s"(slo), "s"(shi), "i"(B + 8 * M + R), "i"(B + 8 * M + 4 + R));
  } else {
    asm volatile("v_perm_b32 v48, v%c3, v%c2, %0\n\tv_perm_b32 v49, v%c3, v%c2, %1\n\t"
                 "v_perm_b32 v%c2, v%c5, v%c4, %0\n\tv_perm_b32 v%c3, v%c5, v%c4, %1\n\t"
                 "v_mov_b32 v%c4, v48\n\tv_mov_b32 v%c5, v49"
                 :: "s"(slo), "s"(shi), "i"(B + 8 * M + R), "i"(B + 8 * M + 4 + R), "i"(B + 8 * R + M), "i"(B + 8 * R + 4 + M));
  }
}
template <int B>
__device__ __forceinline__ void transpose8() {
  const u32 slo = 0x05040100u, shi = 0x07060302u;
  tp_pair<B, 0, 0>(slo, shi); tp_pair<B, 1, 1>(slo, shi); tp_pair<B, 2, 2>(slo, shi); tp_pair<B, 3, 3>(slo, shi);
  tp_pair<B, 0, 1>(slo, shi); tp_pair<B, 0, 2>(slo, shi); tp_pair<B, 0, 3>(slo, shi);
  tp_pair<B, 1, 2>(slo, shi); tp_pair<B, 1, 3>(slo, shi); tp_pair<B, 2, 3>(slo, shi);
  asm volatile("s_nop 1");
}
#define SNOP
#define MF(d, b) "v_mfma_f32_16x16x32_bf16 v[" #d ":" #d "+3], v[48:51], v[%c[wb]+" #b ":%c[wb]+" #b "+3], 0\n\t"
#define FOLD_TAIL \
  "v_permlane32_swap_b32 v52, v68\n\t" \
  "v_permlane32_swap_b32 v56, v72\n\t" \
  "v_permlane32_swap_b32 v60, v76\n\t" \
  "v_permlane32_swap_b32 v64, v80\n\t" \
  "v_cndmask_b32_e64 v52, v52, v68, %[pm]\n\t" \
  "v_cndmask_b32_e64 v60, v60, v76, %[pm]\n\t" \
  "v_cndmask_b32_e64 v56, v56, v72, %[pm]\n\t" \
  "v_cndmask_b32_e64 v64, v64, v80, %[pm]\n\t" \
  "v_permlane16_swap_b32 v52, v60\n\t" \
  "s_nop 0\n\t" \
  "v_permlane16_swap_b32 v56, v64\n\t" \
  "v_add_f32 v52, v52, v60\n\t" \
  "v_add_f32 v56, v56, v64\n\t" \
  "s_nop 0\n\t" \
  "v_add_f32_dpp v52, v52, v52 row_ror:8 row_mask:0xf bank_mask:0xf\n\t" \
  "v_add_f32_dpp v56, v56, v56 row_ror:8 row_mask:0xf bank_mask:0xf\n\t" \
  "v_cndmask_b32_e64 v52, v52, v56, %[pm]\n\t"

template <int WB, bool POLL, int LOFF>
__device__ __forceinline__ void seg_p1(u32 voff, const char* ws, u32 ldsw, u32& spins) {
  if constexpr (POLL) {
    asm volatile(
      SNOP "Lp%=:\n\t"
      "global_load_dwordx4 v[48:51], %[voff], %[ws] sc1\n\t"
      "s_add_u32 %[sp], %[sp], 1\n\t"
      "s_waitcnt vmcnt(0)\n\t"
      MF(52, 0) "v_max3_u32 v84, v48, v49, v50\n\t"
      MF(56, 4) "v_max_u32 v84, v84, v51\n\t"
      MF(60, 8) "v_cmp_eq_u32 vcc, -1, v84\n\t"
      MF(64, 12)
      "s_cbranch_vccz Lk%=\n\t"
      "s_cmp_lt_u32 %[sp], %[cap]\n\t"
      "s_cbranch_scc1 Lp%=\n\t"
      "Lk%=:\n\t"
      : [sp] "+s"(spins) : [voff] "v"(voff), [ws] "s"(ws), [wb] "i"(WB), [cap] "i"(SPIN_CAP) : "memory", "vcc", "scc");
  } else {
    asm volatile("s_nop 1\n\t" MF(52, 0) "s_nop 0\n\t" MF(56, 4) "s_nop 0\n\t" MF(60, 8) "s_nop 0\n\t" MF(64, 12) "s_nop 0\n\t" :: [wb] "i"(WB));
  }
  asm volatile(
    MF(68, 16) "v_pk_add_f32 v[52:53], v[52:53], v[54:55]\n\t"
    MF(72, 20) "v_pk_add_f32 v[56:57], v[56:57], v[58:59]\n\t"
    MF(76, 24) "v_pk_add_f32 v[60:61], v[60:61], v[62:63]\n\t"
    MF(80, 28) "v_pk_add_f32 v[64:65], v[64:65], v[66:67]\n\t"
    "v_add_f32 v52, v52, v53\n\t"
    "v_pk_add_f32 v[68:69], v[68:69], v[70:71]\n\t"
    "v_add_f32 v56, v56, v57\n\t"
    "v_pk_add_f32 v[72:73], v[72:73], v[74:75]\n\t"
    "v_add_f32 v60, v60, v61\n\t"
    "v_pk_add_f32 v[76:77], v[76:77], v[78:79]\n\t"
    "v_add_f32 v64, v64, v65\n\t"
    "v_pk_add_f32 v[80:81], v[80:81], v[82:83]\n\t"
    "v_add_f32 v68, v68, v69\n\t"
    "v_add_f32 v72, v72, v73\n\t"
    "v_add_f32 v76, v76, v77\n\t"
    "v_add_f32 v80, v80, v81\n\t"
    "s_nop 0\n\t"
    FOLD_TAIL
    "ds_write_b32 %[lw], v52 offset:%c[lo]\n\t"
    :: [wb] "i"(WB), [pm] "s"(PMASK), [lw] "v"(ldsw), [lo] "i"(LOFF) : "memory");
}
__device__ __forceinline__ void seg_p3_issue(u32 voff, u32 voff2, const char* ws, const char* ws2) {
  asm volatile("s_nop 4\n\tglobal_load_dwordx4 v[48:51], %0, %2 sc1\n\tglobal_load_dword v84, %1, %3 sc1" :: "v"(voff), "v"(voff2), "s"(ws), "s"(ws2) : "memory");
}
template <int WB, int PRE>
__device__ __forceinline__ void seg_p3(u32 voff, u32 voff2, const char* ws, const char* ws2, u32 ldsw, u32& spins) {
  asm volatile(
    SNOP
    ".if %c[pre]\n\ts_waitcnt vmcnt(%c[pre])\n\ts_branch Lc%=\n\t.endif\n\t"
    "Lp%=:\n\t"
    "global_load_dwordx4 v[48:51], %[voff], %[ws] sc1\n\t"
    "global_load_dword v84, %[voff2], %[ws2] sc1\n\t"
    "s_add_u32 %[sp], %[sp], 1\n\t"
    "s_waitcnt vmcnt(0)\n\t"
    "Lc%=:\n\t"
    MF(52, 0) "v_max3_u32 v85, v48, v49, v50\n\t"
    MF(56, 4) "v_max3_u32 v85, v85, v51, v84\n\t"
    MF(60, 8) "v_cmp_eq_u32 vcc, -1, v85\n\t"
    MF(64, 12)
    "s_cbranch_vccz Lk%=\n\t"
    "s_cmp_lt_u32 %[sp], %[cap]\n\t"
    "s_cbranch_scc1 Lp%=\n\t"
    "Lk%=:\n\t"
    "v_max_f32_dpp v85, v84, v84 quad_perm:[1,0,3,2] row_mask:0xf bank_mask:0xf\n\t"
    MF(68, 16)
    "s_nop 0\n\t"
    "v_max_f32_dpp v85, v85, v85 quad_perm:[2,3,0,1] row_mask:0xf bank_mask:0xf\n\t"
    MF(72, 20)
    "s_nop 0\n\t"
    "v_max_f32_dpp v85, v85, v85 row_half_mirror row_mask:0xf bank_mask:0xf\n\t"
    MF(76, 24)
    "v_sub_f32 v86, v84, v85\n\t"
    MF(80, 28)
    "v_exp_f32 v86, v86\n\t"
    "s_nop 1\n\t"
    "v_mov_b32_dpp v90, v86 row_newbcast:0 row_mask:0x5 bank_mask:0xf\n\t"
    "v_mov_b32_dpp v91, v86 row_newbcast:1 row_mask:0x5 bank_mask:0xf\n\t"
    "v_mov_b32_dpp v92, v86 row_newbcast:2 row_mask:0x5 bank_mask:0xf\n\t"
    "v_mov_b32_dpp v93, v86 row_newbcast:3 row_mask:0x5 bank_mask:0xf\n\t"
    "v_mov_b32_dpp v90, v86 row_newbcast:4 row_mask:0xa bank_mask:0xf\n\t"
    "v_mov_b32_dpp v91, v86 row_newbcast:5 row_mask:0xa bank_mask:0xf\n\t"
    "v_mov_b32_dpp v92, v86 row_newbcast:6 row_mask:0xa bank_mask:0xf\n\t"
    "v_mov_b32_dpp v93, v86 row_newbcast:7 row_mask:0xa bank_mask:0xf\n\t"
    "v_add_f32_dpp v87, v86, v86 quad_perm:[1,0,3,2] row_mask:0xf bank_mask:0xf\n\t"
    "v_pk_mul_f32 v[52:53], v[52:53], v[90:91]\n\t"
    "v_pk_mul_f32 v[56:57], v[56:57], v[90:91]\n\t"
    "v_add_f32_dpp v87, v87, v87 quad_perm:[2,3,0,1] row_mask:0xf bank_mask:0xf\n\t"
    "v_pk_mul_f32 v[60:61], v[60:61], v[90:91]\n\t"
    "v_pk_mul_f32 v[64:65], v[64:65], v[90:91]\n\t"
    "v_add_f32_dpp v87, v87, v87 row_half_mirror row_mask:0xf bank_mask:0xf\n\t"
    "v_pk_mul_f32 v[68:69], v[68:69], v[90:91]\n\t"
    "v_pk_mul_f32 v[72:73], v[72:73], v[90:91]\n\t"
    "v_rcp_f32 v87, v87\n\t"
    "v_pk_mul_f32 v[76:77], v[76:77], v[90:91]\n\t"
    "v_pk_mul_f32 v[80:81], v[80:81], v[90:91]\n\t"
    "v_pk_fma_f32 v[52:53], v[54:55], v[92:93], v[52:53]\n\t"
    "v_pk_fma_f32 v[68:69], v[70:71], v[92:93], v[68:69]\n\t"
    "v_pk_fma_f32 v[56:57], v[58:59], v[92:93], v[56:57]\n\t"
    "v_pk_fma_f32 v[72:73], v[74:75], v[92:93], v[72:73]\n\t"
    "v_pk_fma_f32 v[60:61], v[62:63], v[92:93], v[60:61]\n\t"
    "v_pk_fma_f32 v[76:77], v[78:79], v[92:93], v[76:77]\n\t"
    "v_pk_fma_f32 v[64:65], v[66:67], v[92:93], v[64:65]\n\t"
    "v_pk_fma_f32 v[80:81], v[82:83], v[92:93], v[80:81]\n\t"
    "v_add_f32 v52, v52, v53\n\t"
    "v_add_f32 v68, v68, v69\n\t"
    "v_add_f32 v56, v56, v57\n\t"
    "v_add_f32 v72, v72, v73\n\t"
    "v_add_f32 v60, v60, v61\n\t"
    "v_add_f32 v76, v76, v77\n\t"
    "v_add_f32 v64, v64, v65\n\t"
    "v_add_f32 v80, v80, v81\n\t"
    "s_nop 1\n\t"
    FOLD_TAIL
    "v_mul_f32 v52, v52, v87\n\t"
    "ds_write_b32 %[lw], v52 offset:%c[lo]\n\t"
    : [sp] "+s"(spins)
    : [voff] "v"(voff), [voff2] "v"(voff2), [ws] "s"(ws), [ws2] "s"(ws2), [wb] "i"(WB), [cap] "i"(SPIN_CAP), [pm] "s"(PMASK), [lw] "v"(ldsw), [lo] "i"(L_RED3 - L_RED), [pre] "i"(PRE) : "memory", "vcc", "scc");
}
#define PACK_STORE \
  "s_nop 1\n\t" \
  "v_mov_b32_dpp v53, v52 quad_perm:[1,0,3,2] row_mask:0xf bank_mask:0xf\n\t" \
  "v_cvt_pk_bf16_f32 v54, v52, v53\n\t" \
  "v_min_u32 v54, -2, v54\n\t" \
  "s_nop 1\n\t" \
  "v_mov_b32_dpp v55, v54 row_shl:2 row_mask:0xf bank_mask:0xf\n\t" \
  "v_mov_b32_dpp v56, v54 row_shl:4 row_mask:0xf bank_mask:0xf\n\t" \
  "v_mov_b32_dpp v57, v54 row_shl:6 row_mask:0xf bank_mask:0xf\n\t" \
  "s_mov_b64 exec, %[c8z]\n\t" \
  "global_store_dwordx4 %[so], v[54:57], %[ws] sc1\n\t" \
  "s_mov_b64 exec, -1\n\t" \
  "s_nop 1\n\t"
template <bool XS>
__device__ __forceinline__ void seg_final2(u32 ldsr, u32 xsr, u32 so, const char* ws) {
  constexpr u32 LO = XS ? L_RED3 - L_RED : 0;
  if constexpr (XS) {
    asm volatile(
      "ds_read_b128 v[52:55], %[lr] offset:%c[lo]\n\t"
      "ds_read_b128 v[56:59], %[lr] offset:%c[lo]+16\n\t"
      "ds_read_b32 v60, %[xr]\n\t"
      "s_waitcnt lgkmcnt(1)\n\t"
      "v_pk_add_f32 v[52:53], v[52:53], v[56:57]\n\t"
      "v_pk_add_f32 v[54:55], v[54:55], v[58:59]\n\t"
      "v_pk_add_f32 v[52:53], v[52:53], v[54:55]\n\t"
      "s_waitcnt lgkmcnt(0)\n\t"
      "v_add_f32 v52, v52, v53\n\t"
      "v_add_f32 v52, v52, v60\n\t"
      PACK_STORE
      :: [lr] "v"(ldsr), [xr] "v"(xsr), [so] "v"(so), [ws] "s"(ws), [c8z] "s"(C8Z), [lo] "i"(LO) : "memory");
  } else {
    asm volatile(
      "ds_read_b128 v[52:55], %[lr]\n\t"
      "ds_read_b128 v[56:59], %[lr] offset:16\n\t"
      "s_waitcnt lgkmcnt(0)\n\t"
      "v_pk_add_f32 v[52:53], v[52:53], v[56:57]\n\t"
      "v_pk_add_f32 v[54:55], v[54:55], v[58:59]\n\t"
      "v_pk_add_f32 v[52:53], v[52:53], v[54:55]\n\t"
      "v_add_f32 v52, v52, v53\n\t"
      PACK_STORE
      :: [lr] "v"(ldsr), [so] "v"(so), [ws] "s"(ws), [c8z] "s"(C8Z) : "memory");
  }
}
template <int LOFF>
__device__ __forceinline__ void seg_qpoll(u32 voff, const char* ws, u32 ldsw, u32& spins) {
  asm volatile(
    SNOP "Lp%=:\n\t"
    "global_load_dwordx4 v[52:55], %[voff], %[ws] sc1\n\t"
    "s_add_u32 %[sp], %[sp], 1\n\t"
    "s_waitcnt vmcnt(0)\n\t"
    "v_max3_u32 v56, v52, v53, v54\n\t"
    "v_max_u32 v56, v56, v55\n\t"
    "v_cmp_eq_u32 vcc, -1, v56\n\t"
    "ds_write_b128 %[lw], v[52:55] offset:%c[lo]\n\t"
    "s_cbranch_vccz Lk%=\n\t"
    "s_cmp_lt_u32 %[sp], %[cap]\n\t"
    "s_cbranch_scc1 Lp%=\n\t"
    "Lk%=:\n\t"
    : [sp] "+s"(spins) : [voff] "v"(voff), [ws] "s"(ws), [lw] "v"(ldsw), [cap] "i"(SPIN_CAP), [lo] "i"(LOFF) : "memory", "vcc", "scc");
}
template <int TO>
__device__ __forceinline__ void seg_kpre(u32 ka0, u32 ka1) {
  asm volatile(
    "ds_read_b128 v[48:51], %0 offset:%c2\n\tds_read_b128 v[64:67], %1 offset:%c2\n\t"
    "ds_read_b128 v[52:55], %0 offset:%c2+512\n\tds_read_b128 v[68:71], %1 offset:%c2+512\n\t"
    "ds_read_b128 v[56:59], %0 offset:%c2+1024\n\tds_read_b128 v[72:75], %1 offset:%c2+1024\n\t"
    "ds_read_b128 v[60:63], %0 offset:%c2+1536\n\tds_read_b128 v[76:79], %1 offset:%c2+1536\n\t"
    "v_mov_b32 v96, 0\n\tv_mov_b32 v97, 0\n\tv_mov_b32 v98, 0\n\tv_mov_b32 v99, 0\n\t"
    "v_mov_b32 v100, 0\n\tv_mov_b32 v101, 0\n\tv_mov_b32 v102, 0\n\tv_mov_b32 v103, 0\n\t"
    "v_mov_b32 v104, 0\n\tv_mov_b32 v105, 0\n\tv_mov_b32 v106, 0\n\tv_mov_b32 v107, 0\n\t"
    "v_mov_b32 v108, 0\n\tv_mov_b32 v109, 0\n\tv_mov_b32 v110, 0\n\tv_mov_b32 v111, 0\n\t"
    :: "v"(ka0), "v"(ka1), "i"(TO) : "memory");
}
#define QK(s, k, q, c) "v_mfma_f32_16x16x32_bf16 v[" #s ":" #s "+3], v[" #k ":" #k "+3], v[" #q ":" #q "+3], " c "\n\t"
#define TR(d, a, off) "ds_read_b64_tr_b16 v[" #d ":" #d "+1], %[" #a "] offset:%c[to]+" #off "\n\t"
#define PV(r) "v_mfma_f32_16x16x32_bf16 v[" #r ":" #r "+3], v[124:127], v[" #r ":" #r "+3], 0\n\t"
#define SM(r) "v_fma_f32 v" #r ", v" #r ", v99, v121\n\tv_exp_f32 v" #r ", v" #r "\n\t"
#define PVA(acc, a, v, c) "v_mfma_f32_16x16x32_bf16 v[" #acc ":" #acc "+3], v[" #a ":" #a "+3], v[" #v ":" #v "+3], " c "\n\t"
#define PCP(d, s, ctl, bank) "v_mov_b32_dpp v" #d ", v" #s " " ctl " row_mask:0xf bank_mask:" bank "\n\t"
template <int TO>
__device__ __forceinline__ void seg_attn(u32 qa, u32 sta, u32 va0, u32 va1, u32 va2, u32 va3, u32 oa, u32 mla) {
  asm volatile(
    "ds_read_b128 v[80:83], %[qa]\n\t"
    "ds_read_b128 v[84:87], %[qa] offset:64\n\t"
    "ds_read_b128 v[88:91], %[qa] offset:128\n\t"
    "ds_read_b128 v[92:95], %[qa] offset:192\n\t"
    "ds_read_b64 v[120:121], %[sta]\n\t"
    "s_waitcnt lgkmcnt(4)\n\t"
    QK(112, 48, 80, "0") QK(116, 64, 80, "0")
    "s_waitcnt lgkmcnt(3)\n\t"
    QK(112, 52, 84, "v[112:115]") QK(116, 68, 84, "v[116:119]")
    "s_waitcnt lgkmcnt(2)\n\t"
    QK(112, 56, 88, "v[112:115]") QK(116, 72, 88, "v[116:119]")
    "s_waitcnt lgkmcnt(0)\n\t"
    QK(112, 60, 92, "v[112:115]")
    "v_add_f32_dpp v120, v120, v120 quad_perm:[1,0,3,2] row_mask:0xf bank_mask:0xf\n\t"
    "v_add_f32_dpp v121, v121, v121 quad_perm:[1,0,3,2] row_mask:0xf bank_mask:0xf\n\t"
    QK(116, 76, 92, "v[116:119]")
    TR(48, va0, 0)
    "v_add_f32_dpp v120, v120, v120 quad_perm:[2,3,0,1] row_mask:0xf bank_mask:0xf\n\t"
    "v_add_f32_dpp v121, v121, v121 quad_perm:[2,3,0,1] row_mask:0xf bank_mask:0xf\n\t"
    TR(50, va1, 0) TR(52, va2, 0)
    "v_add_f32_dpp v120, v120, v120 row_half_mirror row_mask:0xf bank_mask:0xf\n\t"
    "v_add_f32_dpp v121, v121, v121 row_half_mirror row_mask:0xf bank_mask:0xf\n\t"
    TR(54, va3, 0)
    "v_mul_f32 v122, 0xbb000000, v120\n\t"
    "v_mul_f32 v123, 0x3b000000, v121\n\t"
    "v_fma_f32 v123, -v122, v122, v123\n\t"
    "v_fmac_f32_dpp v112, v112, v122 row_shl:4 row_mask:0xf bank_mask:0xf bound_ctrl:1\n\t"
    "v_max_f32 v123, 0, v123\n\t"
    "v_fmac_f32_dpp v113, v113, v122 row_shl:4 row_mask:0xf bank_mask:0xf bound_ctrl:1\n\t"
    "v_add_f32 v123, 0x3727c5ac, v123\n\t"
    "v_fmac_f32_dpp v114, v114, v122 row_shl:4 row_mask:0xf bank_mask:0xf bound_ctrl:1\n\t"
    "v_rsq_f32 v123, v123\n\t"
    "v_fmac_f32_dpp v115, v115, v122 row_shl:4 row_mask:0xf bank_mask:0xf bound_ctrl:1\n\t"
    "v_fmac_f32_dpp v116, v116, v122 row_shl:4 row_mask:0xf bank_mask:0xf bound_ctrl:1\n\t"
    "v_max3_f32 v120, v112, v113, v114\n\t"
    "v_fmac_f32_dpp v117, v117, v122 row_shl:4 row_mask:0xf bank_mask:0xf bound_ctrl:1\n\t"
    "v_fmac_f32_dpp v118, v118, v122 row_shl:4 row_mask:0xf bank_mask:0xf bound_ctrl:1\n\t"
    "v_fmac_f32_dpp v119, v119, v122 row_shl:4 row_mask:0xf bank_mask:0xf bound_ctrl:1\n\t"
    "v_max3_f32 v120, v120, v115, v116\n\t"
    "v_mul_f32 v123, %[c2], v123\n\t"
    "v_max3_f32 v120, v120, v117, v118\n\t"
    "v_max_f32 v120, v120, v119\n\t"
    "v_mov_b32 v121, v120\n\t"
    TR(56, va0, 512) TR(58, va1, 512)
    "v_permlane16_swap_b32 v120, v121\n\t"
    "v_max_f32 v120, v120, v121\n\t"
    "v_mov_b32 v121, v120\n\t"
    TR(60, va2, 512) TR(62, va3, 512)
    "v_permlane32_swap_b32 v120, v121\n\t"
    "v_max_f32 v120, v120, v121\n\t"
    "v_mul_f32_e64 v121, -v120, v123\n\t"
    "v_mul_f32 v122, v120, v123\n\t"
    "v_fma_f32 v112, v112, v123, v121\n\t"
    "v_fma_f32 v113, v113, v123, v121\n\t"
    "v_exp_f32 v112, v112\n\t"
    TR(64, va0, 1024)
    "v_fma_f32 v114, v114, v123, v121\n\t"
    "v_exp_f32 v113, v113\n\t"
    TR(66, va1, 1024)
    "v_fma_f32 v115, v115, v123, v121\n\t"
    "v_exp_f32 v114, v114\n\t"
    TR(68, va2, 1024)
    "v_fma_f32 v116, v116, v123, v121\n\t"
    "v_exp_f32 v115, v115\n\t"
    TR(70, va3, 1024)
    "v_fma_f32 v117, v117, v123, v121\n\t"
    "v_exp_f32 v116, v116\n\t"
    "v_cvt_pk_bf16_f32 v124, v112, v113\n\t"
    "v_fma_f32 v118, v118, v123, v121\n\t"
    "v_exp_f32 v117, v117\n\t"
    TR(72, va0, 1536)
    "v_fma_f32 v119, v119, v123, v121\n\t"
    "v_exp_f32 v118, v118\n\t"
    "v_cvt_pk_bf16_f32 v125, v114, v115\n\t"
    "v_exp_f32 v119, v119\n\t"
    TR(74, va1, 1536) TR(76, va2, 1536) TR(78, va3, 1536)
    "v_cvt_pk_bf16_f32 v126, v116, v117\n\t"
    "v_cvt_pk_bf16_f32 v127, v118, v119\n\t"
    PCP(96, 124, "quad_perm:[0,1,2,3]", "0x1") PCP(100, 124, "row_shr:4", "0x2") PCP(104, 124, "quad_perm:[0,1,2,3]", "0x4") PCP(108, 124, "row_shr:4", "0x8")
    PCP(97, 125, "quad_perm:[0,1,2,3]", "0x1") PCP(101, 125, "row_shr:4", "0x2") PCP(105, 125, "quad_perm:[0,1,2,3]", "0x4") PCP(109, 125, "row_shr:4", "0x8")
    PCP(98, 126, "quad_perm:[0,1,2,3]", "0x1") PCP(102, 126, "row_shr:4", "0x2") PCP(106, 126, "quad_perm:[0,1,2,3]", "0x4") PCP(110, 126, "row_shr:4", "0x8")
    PCP(99, 127, "quad_perm:[0,1,2,3]", "0x1") PCP(103, 127, "row_shr:4", "0x2") PCP(107, 127, "quad_perm:[0,1,2,3]", "0x4") PCP(111, 127, "row_shr:4", "0x8")
    "v_mov_b32 v84, %[ones]\n\tv_mov_b32 v85, %[ones]\n\tv_mov_b32 v86, %[ones]\n\tv_mov_b32 v87, %[ones]\n\t"
    "s_waitcnt lgkmcnt(0)\n\t"
    PVA(112, 96, 48, "0") PVA(116, 96, 64, "0")
    PVA(112, 100, 52, "v[112:115]") PVA(116, 100, 68, "v[116:119]")
    PVA(112, 104, 56, "v[112:115]") PVA(116, 104, 72, "v[116:119]")
    PVA(112, 108, 60, "v[112:115]") PVA(116, 108, 76, "v[116:119]")
    "v_mfma_f32_16x16x32_bf16 v[80:83], v[84:87], v[124:127], 0\n\t"
    "s_nop 6\n\t"
    "ds_write_b128 %[oa], v[112:115]\n\t"
    "ds_write_b128 %[oa], v[116:119] offset:1024\n\t"
    "s_mov_b64 exec, 0xf\n\t"
    "ds_write2_b32 %[mla], v122, v80 offset0:0 offset1:16\n\t"
    "s_mov_b64 exec, -1\n\t"
    :: [qa] "v"(qa), [sta] "v"(sta), [va0] "v"(va0), [va1] "v"(va1), [va2] "v"(va2), [va3] "v"(va3), [oa] "v"(oa), [mla] "v"(mla),
       [c2] "s"(C2), [ones] "s"(ONES), [to] "i"(TO) : "memory");
}
__device__ __forceinline__ void seg_merge(u32 mla, u32 oa, u32 so, const char* ws) {
  asm volatile(
    "ds_read_b128 v[48:51], %[mla]\n\t"
    "ds_read_b128 v[60:63], %[mla] offset:64\n\t"
    "ds_read2st64_b32 v[56:57], %[oa] offset0:0 offset1:8\n\t"
    "ds_read2st64_b32 v[58:59], %[oa] offset0:16 offset1:24\n\t"
    "s_waitcnt lgkmcnt(3)\n\t"
    "v_max3_f32 v52, v48, v49, v50\n\t"
    "v_max_f32 v52, v52, v51\n\t"
    "v_sub_f32 v48, v48, v52\n\t"
    "v_sub_f32 v49, v49, v52\n\t"
    "v_exp_f32 v48, v48\n\t"
    "v_sub_f32 v50, v50, v52\n\t"
    "v_exp_f32 v49, v49\n\t"
    "v_sub_f32 v51, v51, v52\n\t"
    "v_exp_f32 v50, v50\n\t"
    "v_exp_f32 v51, v51\n\t"
    "s_waitcnt lgkmcnt(0)\n\t"
    "v_pk_mul_f32 v[60:61], v[60:61], v[48:49]\n\t"
    "v_pk_mul_f32 v[56:57], v[56:57], v[48:49]\n\t"
    "v_pk_fma_f32 v[60:61], v[62:63], v[50:51], v[60:61]\n\t"
    "v_pk_fma_f32 v[56:57], v[58:59], v[50:51], v[56:57]\n\t"
    "v_add_f32 v60, v60, v61\n\t"
    "v_add_f32 v56, v56, v57\n\t"
    "v_rcp_f32 v60, v60\n\t"
    "s_nop 0\n\t"
    "v_mul_f32 v52, v56, v60\n\t"
    PACK_STORE
    :: [mla] "v"(mla), [oa] "v"(oa), [so] "v"(so), [ws] "s"(ws), [c8z] "s"(C8Z) : "memory");
}

__device__ __forceinline__ void dma16(u32 voff, const char* sbase, u32 lds_dst) {
  u32 keep;
  asm volatile(SNOP "s_mov_b32 %0, m0\n\ts_mov_b32 m0, %3\n\ts_nop 0\n\tglobal_load_lds_dwordx4 %1, %2" NTKV "\n\ts_mov_b32 m0, %0"
               : "=&s"(keep) : "v"(voff), "s"(sbase), "s"(lds_dst) : "memory");
}
__device__ __forceinline__ void kv_load(const char* base, int w, int lane, u32 ldsb) {
  asm volatile("" : "+s"(base));
  const int r8 = (lane & 31) >> 2;
#pragma unroll
  for (int i = 0; i < 4; ++i) {
    const u32 ch = 8 * (i & 1) + 4 * (lane >> 5) + ((lane & 3) ^ ((2 * (i >> 1) + (r8 >> 2)) & 3));
    dma16(r8 * 256 + ch * 16, base + (size_t)(16 * w + 8 * (i >> 1)) * 256, ldsb + w * 4096 + i * 1024);
  }
}
__device__ __forceinline__ bool has_sent(v4u x) {
  const u32 m = __builtin_elementwise_max(__builtin_elementwise_max(x[0], x[1]), __builtin_elementwise_max(x[2], x[3]));
  return __builtin_amdgcn_ballot_w64(m == SENT) != 0ull;
}
__device__ __forceinline__ v4u poll128(const char* base, u32 voff) {
  v4u x;
  for (u32 spins = 0;; ++spins) {
    asm volatile("s_nop 4\n\tglobal_load_dwordx4 %0, %1, %2 sc1\n\ts_waitcnt vmcnt(0)" : "=&v"(x) : "v"(voff), "s"(base) : "memory");
    if (!has_sent(x) || spins > SPIN_CAP) break;
  }
  return x;
}
__device__ __forceinline__ v4u pack8(float v) {
  const u32 x = safe(pk(v, dpp<XOR1>(v)));
  return v4u{x, dppu<0x102>(x), dppu<0x104>(x), dppu<0x106>(x)};
}
__device__ __forceinline__ void sum_partials(v4u x, float (&v)[8]) {
#pragma unroll
  for (int m = 0; m < 4; ++m) { v[2 * m] = bflo(x[m]); v[2 * m + 1] = bfhi(x[m]); }
#pragma unroll
  for (int j = 0; j < 8; ++j) v[j] += dpp<XOR1>(v[j]);
#pragma unroll
  for (int j = 0; j < 8; ++j) v[j] += dpp<XOR2>(v[j]);
#pragma unroll
  for (int j = 0; j < 8; ++j) v[j] += dpp<HMIR>(v[j]);
}
__device__ __forceinline__ void x_stats(v4u xa, char* lds, int w, int lane) {
  const bool p = (lane >> 3) & 1; const int kq = lane >> 4;
  const v4f gm = mfma(xa, xa, v4f{0.f, 0.f, 0.f, 0.f});
  const v4f sm = mfma(xa, v4u{ONES, ONES, ONES, ONES}, v4f{0.f, 0.f, 0.f, 0.f});
  float s2 = (gm[0] + gm[1]) + (gm[2] + gm[3]);
  s2 = (p == (bool)(kq >> 1)) ? s2 : 0.f;
  s2 += dpp<XOR1>(s2); s2 += dpp<XOR2>(s2); s2 += dpp<HMIR>(s2); s2 += dpp<ROR8>(s2);
  s2 = x32_add(x16_add(s2));
  float s1 = (sm[0] + sm[1]) + (sm[2] + sm[3]);
  s1 = x32_add(x16_add(s1));
  if (lane == 0) *(v2f*)(lds + L_ST + w * 8) = v2f{s1, s2};
}
}

#define ST(i) do {} while (0)
#define STFLUSH(sv, base) do {} while (0)
#define SGPR_ATTR

__device__ __forceinline__ u32 lb_st() { return L_ST; }
#define OPAQUE_LANE() int lane = lane0; asm volatile("" : "+v"(lane)); const int c8 = lane & 7, kq = lane >> 4; const bool p = (lane >> 3) & 1; (void)c8; (void)p; (void)kq
#define OPAQUE_S(x) asm volatile("" : "+s"(x))

constexpr int FW = 4;

struct Addr {
  u32 vB, vA, vL, lw, so;
  u32 mr, orr;
  u32 r0, r1, r2, r3, r4, r5, r6, r7, r8;
};
#define PIN(x) asm volatile("" : "+v"(x))

template <int TYPE>
__device__ __forceinline__ void layer(const int rep, char* ws, char* lds, int b, int g, int s, int w, const int lane0, v4u xraw, Addr& A, u32 qaT,
                                      const char* nkc, const char* nvc, const char* nwq, const char* cwo, const char* nwo, const char* nkc0, const char* nvc0, const bool lead,
                                      const u32 tag, int& stampv, u32 (&cnt)[3]) {
  constexpr int RQ = TYPE ? R_WQB : R_WQA, RO = TYPE ? R_WOB : R_WOA;
  const int L = rep * 2 + TYPE;
  OPAQUE_S(b); OPAQUE_S(g); OPAQUE_S(s); OPAQUE_S(w);
  const u32 grp = (u32)(b * 8 + g), cu = grp * 8 + s;
#define LD_WQ(R, ptr) do { OPAQUE_LANE(); OPAQUE_S(ptr); bulk8<R, 8192>(ptr + (size_t)(64 * w) * 8192 + (512 * g + 64 * s) * 2, (u32)(lane >> 3) * 65536 + c8 * 16); } while (0)
#define LD_WO(R, ptr) do { OPAQUE_LANE(); OPAQUE_S(ptr); bulk8<R, 1024>(ptr + (size_t)(512 * g + 64 * w) * 1024 + 128 * s, (u32)(lane >> 3) * 8192 + c8 * 16); } while (0)
  const char* wsL = ws + L * BOXL;
  const char* box = ws + WS_BOX + cu * BOX + L * BOXL;
  {
    if (rep == 0) {
      if (SCHED == 4) { if (TYPE == 0) VMWAIT(8); else VMWAIT(8); }
      else if (SCHED == 3) { if (TYPE == 0) { if (KVDELAY) VMWAIT(0); else VMWAIT(8); } else VMWAIT(4); }
      else if (SCHED == 2) VMWAIT(0);
      else if (SCHED == 0) { if (TYPE == 0) { if (PRETOUCH && w == 7) VMWAIT(13); else VMWAIT(8); } else VMWAIT(0); }
      else { if (TYPE == 0) { if (!lead) VMWAIT(4); } else { VMWAIT(4); } }
      if (w >= 4) ST(7);
      if (SCHED == 3 && KVDELAY && TYPE == 0) { OPAQUE_LANE(); kv_load(nkc0, w, lane, L_K); kv_load(nvc0, w, lane, L_V); }
      transpose8<RQ>();
    }
    if (L > 0) {
      const char* wsP = wsL - BOXL;
      if (DLY_B && rep) __builtin_amdgcn_s_sleep(DLY_B);
      ST(0);
      u32 sp = 0;
      seg_p1<RQ, true, 0>(A.vB, wsP, A.lw, sp);
      cnt[0] += sp;
    } else {
      const v4u xa = (lane0 & 7) == 0 ? xraw : v4u{0u, 0u, 0u, 0u};
      asm volatile("v_mov_b32 v48, %0\n\tv_mov_b32 v49, %1\n\tv_mov_b32 v50, %2\n\tv_mov_b32 v51, %3" :: "v"(xa[0]), "v"(xa[1]), "v"(xa[2]), "v"(xa[3]));
      u32 sp = 0;
      seg_p1<RQ, false, 0>(0, ws, A.lw, sp);
    }
    ST(2);
    if (SCHED != 2 && SCHED != 3 && SCHED != 4 && rep == 0 && TYPE == 1) {
      OPAQUE_LANE();
      if (SCHED == 0) kv_load(nkc, w, lane, L_K + 32768);
      kv_load(nvc, w, lane, L_V + 32768);
    }
    v4u xa;
    asm volatile("v_mov_b32 %0, v48\n\tv_mov_b32 %1, v49\n\tv_mov_b32 %2, v50\n\tv_mov_b32 %3, v51" : "=v"(xa[0]), "=v"(xa[1]), "=v"(xa[2]), "=v"(xa[3]));
    if (rep == 0) {
      asm volatile("v_mov_b32 v48, %0\n\tv_mov_b32 v49, %0\n\tv_mov_b32 v50, %0\n\tv_mov_b32 v51, %0" :: "s"(ONES));
      u32 sp = 0;
      seg_p1<RQ, false, L_REDC - L_RED>(0, ws, A.lw, sp);
    }
    BAR();
    ST(3);
    if (w == FW) {
      seg_final2<false>(A.r2, 0, A.so, box + BOX_Q);
      ST(4);
      OPAQUE_LANE();
      if (SCHED == 3 && rep == 0 && TYPE == 1) kv_load(nvc, w, lane, L_V + 32768);
      if (rep == 0) {
        const __amdgpu_buffer_rsrc_t rs = __builtin_amdgcn_make_buffer_rsrc(ws, 0, WS_END, 0x00020000);
        const v4f r0 = *(const v4f*)(lds + L_REDC + lane * 32), r1 = *(const v4f*)(lds + L_REDC + lane * 32 + 16);
        const float cs = 0.125f * (((r0[0] + r0[1]) + (r0[2] + r0[3])) + ((r1[0] + r1[1]) + (r1[2] + r1[3])));
        const v4u pc = pack8(cs);
        if (c8 == 0) __builtin_amdgcn_raw_buffer_store_b128(pc, rs, WS_BOX + cu * BOX + BOX_CS + TYPE * 128 + lane * 2, 0, 16);
      }
      x_stats(xa, lds, w, lane);
      if (L == 0) {
        const u64 flag = (u64)tag | ((u64)(~tag) << 32);
        for (u32 spins = 0;; ++spins) {
          const u64 f = __hip_atomic_load((GAS u64*)(ws + WS_RDY + b * 512 + lane * 8), __ATOMIC_RELAXED, __HIP_MEMORY_SCOPE_AGENT);
          if (__all(f == flag) || spins > SPIN_CAP) break;
        }
        if (g == 0 && s == 0 && lane == 0) __hip_atomic_store((GAS u32*)(ws + WS_EPOCH + b * 256), tag, __ATOMIC_RELAXED, __HIP_MEMORY_SCOPE_AGENT);
      }
      if (DLY_C) __builtin_amdgcn_s_sleep(DLY_C);
      u32 sp = 0;
      seg_qpoll<0>(A.r0, wsL, A.r1, sp);
      cnt[1] += sp;
      if (rep == 0) { sp = 0; seg_qpoll<L_CSI + TYPE * 1088 - L_Q>(A.r0, ws + (BOX_CS + TYPE * 128 - BOX_Q), A.r1, sp); }
      ST(5);
    } else {
      OPAQUE_LANE();
      if (SCHED == 3 && rep == 0 && TYPE == 1) kv_load(nvc, w, lane, L_V + 32768);
      if (SCHED == 4 && rep == 0 && TYPE == 1) LD_WO(RO, cwo);
      x_stats(xa, lds, w, lane);
    }
    if (g == 0 && w == s) {
      OPAQUE_LANE();
      float v[8];
      sum_partials(xa, v);
      if (c8 == 0) {
        float* xs = (float*)(lds + L_XS) + 16 * kq + (p ? 8 : 0);
        *(v4f*)xs = v4f{v[0], v[1], v[2], v[3]};
        *(v4f*)(xs + 4) = v4f{v[4], v[5], v[6], v[7]};
      }
    }
    if (SCHED == 4 && rep == 0 && TYPE == 1 && w == FW) LD_WO(RO, cwo);
    if (rep == 0) {
      if (SCHED == 4 && TYPE == 1) {   }
      else if (SCHED == 1 && TYPE == 0 && lead) VMWAIT(16); else VMWAIT(0);
      if (w >= 4) ST(8);
      BAR();
      if (SCHED == 2) {}
      else if (SCHED == 4) { if (TYPE == 0) LD_WO(RO, cwo); }
      else if (SCHED == 0 || SCHED == 3) LD_WO(RO, cwo);
      else {
        if (!lead) LD_WO(RO, cwo);
        else if (TYPE == 0) LD_WO(R_WOB, nwo);
      }
    }
  }
  if (w < 4) {
    seg_kpre<TYPE * 32768>(A.r0, A.r1);
    ST(6);
    BAR();
    ST(7);
    seg_attn<TYPE * 32768>(qaT, A.r6, A.r2, A.r3, A.r4, A.r5, A.r7, A.r8);
    ST(8);
  } else {
    BAR();
  }
  {
    BAR();
    seg_merge(A.mr, A.orr, A.so, box + w * 128);
    if (w == 7) {
      OPAQUE_LANE();
      const __amdgpu_buffer_rsrc_t rs = __builtin_amdgcn_make_buffer_rsrc(ws, 0, WS_END, 0x00020000);
      const int hh = (lane >> 2) & 3, aa = lane & 3;
      const float m1 = ((const float*)(lds + L_ML))[hh * 4 + aa], l1 = ((const float*)(lds + L_ML))[16 + hh * 4 + aa];
      float M1 = __builtin_fmaxf(m1, dpp<XOR1>(m1)); M1 = __builtin_fmaxf(M1, dpp<XOR2>(M1));
      float L1 = __builtin_amdgcn_exp2f(m1 - M1) * l1; L1 += dpp<XOR1>(L1); L1 += dpp<XOR2>(L1);
      const u32 e = safe(__float_as_uint(M1 + __builtin_amdgcn_logf(L1)));
      const v4u ev = {dppu<0x150>(e), dppu<0x154>(e), dppu<0x158>(e), dppu<0x15C>(e)};
      if (lane < 8) __builtin_amdgcn_raw_buffer_store_b128(ev, rs, WS_LSR + ((L * 32 + grp) * 8 + lane) * 128 + s * 16, 0, 16);
    }
    ST(9);
  }
  {
    const char* wsS = ws + L * 32768;
    if (rep == 0) {
      if (SCHED == 1 && TYPE == 0 && lead) VMWAIT(8); else VMWAIT(0);
      transpose8<RO>();
    } else {
      if (DLY_A) __builtin_amdgcn_s_sleep(DLY_A);
    }
    ST(1);
    u32 sp = 0;
    if (SCHED == 4 && rep == 0 && TYPE == 0) {
      seg_p3_issue(A.vA, A.vL, wsL, wsS);
      LD_WQ(R_WQB, nwq);
      { OPAQUE_LANE(); kv_load(nkc, w, lane, L_K + 32768); kv_load(nvc, w, lane, L_V + 32768); }
      seg_p3<RO, 16>(A.vA, A.vL, wsL, wsS, A.lw, sp);
    } else {
      seg_p3<RO, 0>(A.vA, A.vL, wsL, wsS, A.lw, sp);
    }
    cnt[2] += sp;
    ST(10);
    if (SCHED != 2 && SCHED != 3 && SCHED != 4 && rep == 0 && TYPE == 0) {
      if (SCHED == 0) LD_WQ(R_WQB, nwq);
      else {
        if (!lead) LD_WQ(R_WQB, nwq);
        OPAQUE_LANE();
        kv_load(nkc, w, lane, L_K + 32768);
      }
    }
    BAR();
    if (w == FW) seg_final2<true>(A.r2, A.r3, A.so, box + BOX_X);
    ST(11);
    if (SCHED == 3 && rep == 0 && TYPE == 0) {
      LD_WQ(R_WQB, nwq);
      OPAQUE_LANE();
      kv_load(nkc, w, lane, L_K + 32768);
    }
  }
  STFLUSH(stampv, L * 12);
}

__global__ void __launch_bounds__(512, 2) __attribute__((amdgpu_num_vgpr(24) SGPR_ATTR))
fused_kernel(const char* __restrict__ xin, const char* kA, const char* vA, const char* wqA, const char* woA,
             const char* kB, const char* vB, const char* wqB, const char* woB, unsigned short* out, char* ws) {
  extern __shared__ __attribute__((aligned(16))) char lds[];
  const int id = blockIdx.x, g = id & 7, b = id >> 6, s = (id >> 3) & 7;
  const int tid = threadIdx.x, lane = tid & 63, w = __builtin_amdgcn_readfirstlane(tid >> 6);
  const int c8 = lane & 7, kq = lane >> 4;
  const bool p = (lane >> 3) & 1;
  int stampv = 0;
  const __amdgpu_buffer_rsrc_t rs = __builtin_amdgcn_make_buffer_rsrc(ws, 0, WS_END, 0x00020000);
  const u32 cu = (u32)((b * 8 + g) * 8 + s);
  asm volatile("" ::: "v127", "v255");
  u32 ep; v4u xraw;
  asm volatile("s_nop 4\n\tglobal_load_dword v56, %0, %1 sc1\n\tglobal_load_dwordx4 v[52:55], %2, %3"
               :: "v"(WS_EPOCH + b * 256), "s"(ws), "v"((u32)((b * 512 + 64 * w + 16 * kq + (p ? 8 : 0)) * 2)), "s"(xin) : "memory");
  if (w == 5) {
    const v4u sv = {SENT, SENT, SENT, SENT};
#pragma unroll
    for (int i = 0; i < 5; ++i) __builtin_amdgcn_raw_buffer_store_b128(sv, rs, WS_BOX + cu * BOX + i * 1024 + lane * 16, 0, 16);
    const u32 o6 = lane < 16 ? WS_BOX + cu * BOX + 5120 + lane * 16
                             : WS_LSR + (u32)(((((lane - 16) >> 3) * 32 + b * 8 + g) * 8 + (lane & 7)) * 128 + s * 16);
    if (lane < 48) __builtin_amdgcn_raw_buffer_store_b128(sv, rs, o6, 0, 16);
  }
  if (w == 6) ((float*)(lds + L_XS))[lane] = 0.f;
  const size_t kvoff = ((size_t)(b * 8 + g) * SK + (SK - WIN) + s * 128) * 256;
  const bool lead = (w >> 1) == b;
  const size_t qoff = (size_t)(64 * w) * 8192 + (512 * g + 64 * s) * 2, ooff = (size_t)(512 * g + 64 * w) * 1024 + 128 * s;
  const u32 vq = (u32)(lane >> 3) * 65536 + c8 * 16, vo = (u32)(lane >> 3) * 8192 + c8 * 16;
  if (SCHED == 0 || SCHED == 2 || SCHED == 3 || SCHED == 4 || lead) {
    bulk8<R_WQA, 8192>(wqA + qoff, vq);
    VMWAIT(8);
  } else {
    VMWAIT(0);
  }
  asm volatile("v_mov_b32 %0, v52\n\tv_mov_b32 %1, v53\n\tv_mov_b32 %2, v54\n\tv_mov_b32 %3, v55\n\tv_mov_b32 %4, v56"
               : "=v"(xraw[0]), "=v"(xraw[1]), "=v"(xraw[2]), "=v"(xraw[3]), "=v"(ep));
  ST(6);
  const u32 tag = (u32)__builtin_amdgcn_readfirstlane((int)ep) + 1u;
  if (w == 5 && lane == 0) {
    const u64 flag = (u64)tag | ((u64)(~tag) << 32);
    __hip_atomic_store((GAS u64*)(ws + WS_RDY + b * 512 + (s * 8 + g) * 8), flag, __ATOMIC_RELAXED, __HIP_MEMORY_SCOPE_AGENT);
  }
  if (TOUCHBOX && w == 5) {
    const u32 o6 = lane < 16 ? WS_BOX + cu * BOX + 5120 + lane * 16
                             : WS_LSR + (u32)((((((lane - 16) & 31) >> 3) * 32 + b * 8 + g) * 8 + (lane & 7)) * 128 + s * 16);
    asm volatile("s_nop 4\n\tglobal_load_dwordx4 v[100:103], %0, %2 sc1\n\tglobal_load_dwordx4 v[100:103], %0, %2 offset:1024 sc1\n\t"
                 "global_load_dwordx4 v[100:103], %0, %2 offset:2048 sc1\n\tglobal_load_dwordx4 v[100:103], %0, %2 offset:3072 sc1\n\t"
                 "global_load_dwordx4 v[100:103], %3, %2 sc1\n\tglobal_load_dwordx4 v[100:103], %1, %2 sc1"
                 :: "v"(WS_BOX + cu * BOX + lane * 16), "v"(o6), "s"(ws), "v"(WS_BOX + cu * BOX + 4096 + lane * 16) : "memory");
  }
  if (SCHED == 0 || SCHED == 3 || SCHED == 4) {
    if (!(SCHED == 3 && KVDELAY)) {
      asm volatile("s_barrier" ::: "memory");
      kv_load(kA + kvoff, w, lane, L_K);
      kv_load(vA + kvoff, w, lane, L_V);
    }
  } else if (SCHED == 2) {
    kv_load(kA + kvoff, w, lane, L_K);
    kv_load(vA + kvoff, w, lane, L_V);
    bulk8<R_WOA, 1024>(woA + ooff, vo);
    bulk8<R_WQB, 8192>(wqB + qoff, vq);
    kv_load(kB + kvoff, w, lane, L_K + 32768);
    kv_load(vB + kvoff, w, lane, L_V + 32768);
    bulk8<R_WOB, 1024>(woB + ooff, vo);
    if (EXTRA == 1) {
      const char* e = kA + ((size_t)(b * 8 + g) * SK + s * 64 * 4 + w * 32) * 256;
      bulk8<64, 1024>(e, (u32)lane * 16);
      const char* e2 = vA + ((size_t)(b * 8 + g) * SK + s * 64 * 4 + w * 32) * 256;
      bulk8<64, 1024>(e2, (u32)lane * 16);
    } else if (EXTRA == 3) {
      VMWAIT(0);
      const size_t o = kvoff + (size_t)w * 4096;
      asm volatile("s_nop 0");
      bulk8<64, 1024>(kA + kvoff + (w & 3) * 8192, (u32)lane * 16);
      bulk8<64, 1024>((w < 4 ? vA : kB) + kvoff + (w & 3) * 8192, (u32)lane * 16);
      (void)o;
    } else if (EXTRA == 2) {
      const char* e = kB + ((size_t)(g * 8 + s) * 8 + w) * 16384;
      bulk8<64, 1024>(e, (u32)lane * 16);
      bulk8<64, 1024>(e + 8192, (u32)lane * 16);
    }
  } else {
    kv_load(kA + kvoff, w, lane, L_K);
    if (lead) VMWAIT(4);
    asm volatile("s_barrier" ::: "memory");
    if (!lead) bulk8<R_WQA, 8192>(wqA + qoff, vq);
    kv_load(vA + kvoff, w, lane, L_V);
    if (lead) { bulk8<R_WOA, 1024>(woA + ooff, vo); bulk8<R_WQB, 8192>(wqB + qoff, vq); }
  }
  if (PRETOUCH && w == 7) {
    const size_t kvo = ((size_t)(b * 8 + g) * SK + (SK - WIN) + s * 128) * 256;
    asm volatile("s_nop 4\n\tglobal_load_dword v100, %0, %2\n\tglobal_load_dword v101, %1, %3\n\tglobal_load_dword v102, %1, %4\n\t"
                 "global_load_dword v103, %5, %6\n\tglobal_load_dword v104, %5, %7"
                 :: "v"((u32)(lane * 65536 + (512 * g + 64 * s) * 2)), "v"((u32)((512 * g + lane * 8) * 1024 + 128 * s)), "s"(wqB), "s"(woA), "s"(woB),
                    "v"((u32)(lane * 512)), "s"(kB + kvo), "s"(vB + kvo) : "memory");
  }
  Addr A; u32 qa0, qa1;
  {
    const u32 grp = (u32)(b * 8 + g);
    const u32 oct = kq * 32 + (p ? 16 : 0);
    A.vB = WS_BOX + (u32)((b * 8 + c8) * 8 + w) * BOX + BOX_X + oct;
    A.vA = WS_BOX + (grp * 8 + c8) * BOX + w * 128 + oct;
    A.vL = WS_LSR + (grp * 8 + s) * 128 + c8 * 16 + (w >> 1) * 4;
    A.lw = L_RED + (8 * c8 + (lane >> 3)) * 32 + w * 4;
    A.so = lane * 2;
    A.mr = L_ML + (w >> 1) * 16;
    A.orr = L_OM + (w & 1) * 1024 + lane * 16 + (w >> 1) * 4;
    const int r16 = lane & 15, q4 = r16 >> 2, p2 = lane & 3;
    qa0 = L_Q + (lane & 3) * 272 + kq * 16;
    qa1 = qa0;
    if (lane & 4) { qa0 += L_CSI - L_Q; qa1 += L_CSI + 1088 - L_Q; }
    if (w < 4) {
      const u32 kb = L_K + 2048 * (4 * w + (r16 >> 2)) + 64 * (r16 & 3);
      A.r0 = kb + 16 * (kq ^ ((2 * (r16 >> 2)) & 3));
      A.r1 = kb + 256 + 16 * (kq ^ ((2 * (r16 >> 2) + 1) & 3));
      const u32 vb = L_V + 2048 * (4 * w + kq) + 64 * q4 + 8 * (p2 & 1);
      A.r2 = vb + 16 * (((p2 >> 1)) ^ ((2 * kq) & 3));
      A.r3 = vb + 256 + 16 * (((p2 >> 1)) ^ ((2 * kq + 1) & 3));
      A.r4 = vb + 16 * ((2 + (p2 >> 1)) ^ ((2 * kq) & 3));
      A.r5 = vb + 256 + 16 * ((2 + (p2 >> 1)) ^ ((2 * kq + 1) & 3));
      A.r7 = L_OM + w * 2048 + lane * 16;
      A.r8 = L_ML + (lane * 4 + w) * 4;
    } else {
      A.r0 = WS_BOX + (grp * 8 + (lane >> 3)) * BOX + BOX_Q + c8 * 16;
      A.r1 = L_Q + kq * 272 + (lane & 15) * 16;
      A.r2 = L_RED + lane * 32;
      A.r3 = L_XS + lane * 4;
      A.r4 = A.r5 = A.r7 = A.r8 = 0;
    }
    A.r6 = L_ST + c8 * 8;
    PIN(A.r6);
    PIN(A.vB); PIN(A.vA); PIN(A.vL); PIN(A.lw); PIN(A.so); PIN(A.mr); PIN(A.orr); PIN(A.r0); PIN(A.r1); PIN(A.r2); PIN(A.r3); PIN(A.r4); PIN(A.r5); PIN(A.r7); PIN(A.r8);
    PIN(qa0); PIN(qa1);
  }
  u32 cnt[3] = {0, 0, 0};
  for (int rep = 0; rep < 2; ++rep) {
    layer<0>(rep, ws, lds, b, g, s, w, lane, xraw, A, qa0, kB + kvoff, vB + kvoff, wqB, woA, woB, kA + kvoff, vA + kvoff, lead, tag, stampv, cnt);
    layer<1>(rep, ws, lds, b, g, s, w, lane, xraw, A, qa1, kB + kvoff, vB + kvoff, nullptr, woB, nullptr, nullptr, nullptr, lead, tag, stampv, cnt);
  }
  if (g == 0 && w == s) {
    const u32 voff = WS_BOX + (u32)((b * 8 + c8) * 8 + w) * BOX + 3u * BOXL + BOX_X + kq * 32 + (p ? 16 : 0);
    if (DLY_B) __builtin_amdgcn_s_sleep(DLY_B);
    const v4u x = poll128(ws, voff);
    float v[8];
    sum_partials(x, v);
    if (c8 == 0) *(v4u*)(out + b * 512 + 64 * w + 16 * kq + (p ? 8 : 0)) = v4u{pk(v[0], v[1]), pk(v[2], v[3]), pk(v[4], v[5]), pk(v[6], v[7])};
  }
}

extern "C" void kernel_launch(void* const* d_in, const int* in_sizes, int n_in, void* d_out, int out_size, void* d_ws, size_t ws_size,
                              hipStream_t stream) {
  (void)hipFuncSetAttribute((const void*)fused_kernel, hipFuncAttributeMaxDynamicSharedMemorySize, L_END);
  fused_kernel<<<256, 512, L_END, stream>>>((const char*)d_in[0], (const char*)d_in[1], (const char*)d_in[2], (const char*)d_in[3], (const char*)d_in[4],
                                            (const char*)d_in[6], (const char*)d_in[7], (const char*)d_in[8], (const char*)d_in[9],
                                            (unsigned short*)d_out, (char*)d_ws);
}
```

```cpp
#include <hip/hip_runtime.h>
#include <stdint.h>

typedef unsigned int u32;
typedef unsigned long long u64;
typedef u32 v4u __attribute__((ext_vector_type(4)));
typedef u32 v2u __attribute__((ext_vector_type(2)));
typedef float v4f __attribute__((ext_vector_type(4)));
typedef float v2f __attribute__((ext_vector_type(2)));
typedef __bf16 v8b __attribute__((ext_vector_type(8)));
typedef __bf16 v2b __attribute__((ext_vector_type(2)));
#define GAS __attribute__((address_space(1)))

#define DLY_C 2
#define DLY_A 8
#define DLY_B 10
#define KB_AT 0
#define VB_AT 1
#define PGTEST 1
#define NTKV ""
#define NTW ""

namespace {
constexpr int SK = 4096, WIN = 1024;
constexpr u32 WS_EPOCH = 0;
constexpr u32 WS_RDY = 4096;
constexpr u32 WS_LSR = 8192;
constexpr u32 WS_BOX = WS_LSR + 131072;
constexpr u32 BOXL = 1280, BOX_Q = 1024, BOX_X = 1152, BOX_CS = 5120, BOX = 5376;
constexpr u32 WS_END = WS_BOX + 256 * BOX;
constexpr u32 WS_DBG = 4u << 20;
constexpr u32 L_KV = 0;
constexpr u32 L_K = 0, L_V = 65536;
constexpr u32 L_OM = 131072;
constexpr u32 L_ML = L_OM + 8192;
constexpr u32 L_Q = L_ML + 128;
constexpr u32 L_CSI = L_Q + 1088;
constexpr u32 L_RED = L_CSI + 2176;
constexpr u32 L_REDC = L_RED + 2048;
constexpr u32 L_ST = L_REDC + 2048;
constexpr u32 L_RED3 = L_ST + 64;
constexpr u32 L_XS = L_RED3 + 2048;
constexpr u32 L_END = L_XS + 256;
static_assert(L_END <= 160 * 1024, "lds");
static_assert(L_Q % 16 == 0 && L_CSI % 16 == 0 && L_RED % 16 == 0 && L_ST % 16 == 0 && L_RED3 % 16 == 0 && L_XS % 16 == 0, "align");
constexpr u32 SENT = 0xFFFFFFFFu;
constexpr u32 SPIN_CAP = 1u << 15;
constexpr float C2 = 0.08838834764831845f * 1.4426950408889634f;
constexpr u32 ONES = 0x3F803F80u;
constexpr u64 PMASK = 0xFF00FF00FF00FF00ull;
constexpr u64 C8Z = 0x0101010101010101ull;

__device__ __forceinline__ float bflo(u32 w) { return __uint_as_float(w << 16); }
__device__ __forceinline__ float bfhi(u32 w) { return __uint_as_float(w & 0xffff0000u); }
__device__ __forceinline__ u32 pk(float lo, float hi) { v2f f = {lo, hi}; return __builtin_bit_cast(u32, __builtin_convertvector(f, v2b)); }
__device__ __forceinline__ u32 safe(u32 v) { return v < 0xFFFFFFFEu ? v : 0xFFFFFFFEu; }
template <int CTRL> __device__ __forceinline__ float dpp(float x) {
  return __builtin_bit_cast(float, __builtin_amdgcn_mov_dpp(__builtin_bit_cast(int, x), CTRL, 0xf, 0xf, true));
}
template <int CTRL> __device__ __forceinline__ u32 dppu(u32 x) { return (u32)__builtin_amdgcn_mov_dpp((int)x, CTRL, 0xf, 0xf, true); }
constexpr int XOR1 = 0xB1, XOR2 = 0x4E, HMIR = 0x141, ROR8 = 0x128;
__device__ __forceinline__ float x16_add(float x) { auto s = __builtin_amdgcn_permlane16_swap(__float_as_uint(x), __float_as_uint(x), false, false); return __uint_as_float(s[0]) + __uint_as_float(s[1]); }
__device__ __forceinline__ float x32_add(float x) { auto s = __builtin_amdgcn_permlane32_swap(__float_as_uint(x), __float_as_uint(x), false, false); return __uint_as_float(s[0]) + __uint_as_float(s[1]); }
__device__ __forceinline__ v4f mfma(v4u a, v4u b, v4f c) {
  return __builtin_amdgcn_mfma_f32_16x16x32_bf16(__builtin_bit_cast(v8b, a), __builtin_bit_cast(v8b, b), c, 0, 0, 0);
}
#define BAR() asm volatile("s_waitcnt lgkmcnt(0)\n\ts_barrier" ::: "memory")
#define VMWAIT(n) asm volatile("s_waitcnt vmcnt(" #n ")" ::: "memory")

constexpr int R_WQA = 128, R_WOA = 160, R_WQB = 192, R_WOB = 224;
template <int BASE, u32 STRIDE>
__device__ __forceinline__ void bulk8(const char* base, u32 voff) {
  asm volatile("" : "+s"(base));
  const __amdgpu_buffer_rsrc_t r = __builtin_amdgcn_make_buffer_rsrc((void*)base, 0, 0x7fffffff, 0x00020000);
  asm volatile("s_nop 4\n\t"
               "buffer_load_dwordx4 v[%c10:%c11], %0, %1, 0 offen" NTW "\n\tbuffer_load_dwordx4 v[%c12:%c13], %0, %1, %3 offen" NTW "\n\t"
               "buffer_load_dwordx4 v[%c14:%c15], %0, %1, %4 offen" NTW "\n\tbuffer_load_dwordx4 v[%c16:%c17], %0, %1, %5 offen" NTW "\n\t"
               "buffer_load_dwordx4 v[%c18:%c19], %0, %1, %6 offen" NTW "\n\tbuffer_load_dwordx4 v[%c20:%c21], %0, %1, %7 offen" NTW "\n\t"
               "buffer_load_dwordx4 v[%c22:%c23], %0, %1, %8 offen" NTW "\n\tbuffer_load_dwordx4 v[%c24:%c25], %0, %1, %9 offen" NTW
               :: "v"(voff), "s"(r), "s"(0), "s"(STRIDE), "s"(2 * STRIDE), "s"(3 * STRIDE), "s"(4 * STRIDE), "s"(5 * STRIDE), "s"(6 * STRIDE), "s"(7 * STRIDE),
                  "i"(BASE), "i"(BASE + 3), "i"(BASE + 4), "i"(BASE + 7), "i"(BASE + 8), "i"(BASE + 11), "i"(BASE + 12), "i"(BASE + 15),
                  "i"(BASE + 16), "i"(BASE + 19), "i"(BASE + 20), "i"(BASE + 23), "i"(BASE + 24), "i"(BASE + 27), "i"(BASE + 28), "i"(BASE + 31)
               : "memory");
}
template <int B, int M, int R>
__device__ __forceinline__ void tp_pair(u32 slo, u32 shi) {
  if constexpr (M == R) {
    asm volatile("v_perm_b32 v48, v%c3, v%c2, %0\n\tv_perm_b32 v%c3, v%c3, v%c2, %1\n\tv_mov_b32 v%c2, v48"
                 :: "s"(slo), "s"(shi), "i"(B + 8 * M + R), "i"(B + 8 * M + 4 + R));
  } else {
    asm volatile("v_perm_b32 v48, v%c3, v%c2, %0\n\tv_perm_b32 v49, v%c3, v%c2, %1\n\t"
                 "v_perm_b32 v%c2, v%c5, v%c4, %0\n\tv_perm_b32 v%c3, v%c5, v%c4, %1\n\t"
                 "v_mov_b32 v%c4, v48\n\tv_mov_b32 v%c5, v49"
                 :: "s"(slo), "s"(shi), "i"(B + 8 * M + R), "i"(B + 8 * M + 4 + R), "i"(B + 8 * R + M), "i"(B + 8 * R + 4 + M));
  }
}
template <int B>
__device__ __forceinline__ void transpose8() {
  const u32 slo = 0x05040100u, shi = 0x07060302u;
  tp_pair<B, 0, 0>(slo, shi); tp_pair<B, 1, 1>(slo, shi); tp_pair<B, 2, 2>(slo, shi); tp_pair<B, 3, 3>(slo, shi);
  tp_pair<B, 0, 1>(slo, shi); tp_pair<B, 0, 2>(slo, shi); tp_pair<B, 0, 3>(slo, shi);
  tp_pair<B, 1, 2>(slo, shi); tp_pair<B, 1, 3>(slo, shi); tp_pair<B, 2, 3>(slo, shi);
  asm volatile("s_nop 1");
}
#define SNOP
#define MF(d, b) "v_mfma_f32_16x16x32_bf16 v[" #d ":" #d "+3], v[48:51], v[%c[wb]+" #b ":%c[wb]+" #b "+3], 0\n\t"
#define FOLD_TAIL \
  "v_permlane32_swap_b32 v52, v68\n\t" \
  "v_permlane32_swap_b32 v56, v72\n\t" \
  "v_permlane32_swap_b32 v60, v76\n\t" \
  "v_permlane32_swap_b32 v64, v80\n\t" \
  "v_cndmask_b32_e64 v52, v52, v68, %[pm]\n\t" \
  "v_cndmask_b32_e64 v60, v60, v76, %[pm]\n\t" \
  "v_cndmask_b32_e64 v56, v56, v72, %[pm]\n\t" \
  "v_cndmask_b32_e64 v64, v64, v80, %[pm]\n\t" \
  "v_permlane16_swap_b32 v52, v60\n\t" \
  "s_nop 0\n\t" \
  "v_permlane16_swap_b32 v56, v64\n\t" \
  "v_add_f32 v52, v52, v60\n\t" \
  "v_add_f32 v56, v56, v64\n\t" \
  "s_nop 0\n\t" \
  "v_add_f32_dpp v52, v52, v52 row_ror:8 row_mask:0xf bank_mask:0xf\n\t" \
  "v_add_f32_dpp v56, v56, v56 row_ror:8 row_mask:0xf bank_mask:0xf\n\t" \
  "v_cndmask_b32_e64 v52, v52, v56, %[pm]\n\t"

template <int WB, bool POLL, int LOFF>
__device__ __forceinline__ void seg_p1(u32 voff, const char* ws, u32 ldsw, u32& spins) {
  if constexpr (POLL) {
    asm volatile(
      SNOP "Lp%=:\n\t"
      "global_load_dwordx4 v[48:51], %[voff], %[ws] sc1\n\t"
      "s_add_u32 %[sp], %[sp], 1\n\t"
      "s_waitcnt vmcnt(0)\n\t"
      MF(52, 0) "v_max3_u32 v84, v48, v49, v50\n\t"
      MF(56, 4) "v_max_u32 v84, v84, v51\n\t"
      MF(60, 8) "v_cmp_eq_u32 vcc, -1, v84\n\t"
      MF(64, 12)
      "s_cbranch_vccz Lk%=\n\t"
      "s_cmp_lt_u32 %[sp], %[cap]\n\t"
      "s_cbranch_scc1 Lp%=\n\t"
      "Lk%=:\n\t"
      : [sp] "+s"(spins) : [voff] "v"(voff), [ws] "s"(ws), [wb] "i"(WB), [cap] "i"(SPIN_CAP) : "memory", "vcc", "scc");
  } else {
    asm volatile("s_nop 1\n\t" MF(52, 0) "s_nop 0\n\t" MF(56, 4) "s_nop 0\n\t" MF(60, 8) "s_nop 0\n\t" MF(64, 12) "s_nop 0\n\t" :: [wb] "i"(WB));
  }
  asm volatile(
    MF(68, 16) "v_pk_add_f32 v[52:53], v[52:53], v[54:55]\n\t"
    MF(72, 20) "v_pk_add_f32 v[56:57], v[56:57], v[58:59]\n\t"
    MF(76, 24) "v_pk_add_f32 v[60:61], v[60:61], v[62:63]\n\t"
    MF(80, 28) "v_pk_add_f32 v[64:65], v[64:65], v[66:67]\n\t"
    "v_add_f32 v52, v52, v53\n\t"
    "v_pk_add_f32 v[68:69], v[68:69], v[70:71]\n\t"
    "v_add_f32 v56, v56, v57\n\t"
    "v_pk_add_f32 v[72:73], v[72:73], v[74:75]\n\t"
    "v_add_f32 v60, v60, v61\n\t"
    "v_pk_add_f32 v[76:77], v[76:77], v[78:79]\n\t"
    "v_add_f32 v64, v64, v65\n\t"
    "v_pk_add_f32 v[80:81], v[80:81], v[82:83]\n\t"
    "v_add_f32 v68, v68, v69\n\t"
    "v_add_f32 v72, v72, v73\n\t"
    "v_add_f32 v76, v76, v77\n\t"
    "v_add_f32 v80, v80, v81\n\t"
    "s_nop 0\n\t"
    FOLD_TAIL
    "ds_write_b32 %[lw], v52 offset:%c[lo]\n\t"
    :: [wb] "i"(WB), [pm] "s"(PMASK), [lw] "v"(ldsw), [lo] "i"(LOFF) : "memory");
}
__device__ __forceinline__ void seg_p3_issue(u32 voff, u32 voff2, const char* ws, const char* ws2) {
  asm volatile("s_nop 4\n\tglobal_load_dwordx4 v[48:51], %0, %2 sc1\n\tglobal_load_dword v84, %1, %3 sc1" :: "v"(voff), "v"(voff2), "s"(ws), "s"(ws2) : "memory");
}
template <int WB, int PRE>
__device__ __forceinline__ void seg_p3(u32 voff, u32 voff2, const char* ws, const char* ws2, u32 ldsw, u32& spins) {
  asm volatile(
    SNOP
    ".if %c[pre]\n\ts_waitcnt vmcnt(%c[pre])\n\ts_branch Lc%=\n\t.endif\n\t"
    "Lp%=:\n\t"
    "global_load_dwordx4 v[48:51], %[voff], %[ws] sc1\n\t"
    "global_load_dword v84, %[voff2], %[ws2] sc1\n\t"
    "s_add_u32 %[sp], %[sp], 1\n\t"
    "s_waitcnt vmcnt(0)\n\t"
    "Lc%=:\n\t"
    MF(52, 0) "v_max3_u32 v85, v48, v49, v50\n\t"
    MF(56, 4) "v_max3_u32 v85, v85, v51, v84\n\t"
    MF(60, 8) "v_cmp_eq_u32 vcc, -1, v85\n\t"
    MF(64, 12)
    "s_cbranch_vccz Lk%=\n\t"
    "s_cmp_lt_u32 %[sp], %[cap]\n\t"
    "s_cbranch_scc1 Lp%=\n\t"
    "Lk%=:\n\t"
    "v_max_f32_dpp v85, v84, v84 quad_perm:[1,0,3,2] row_mask:0xf bank_mask:0xf\n\t"
    MF(68, 16)
    "s_nop 0\n\t"
    "v_max_f32_dpp v85, v85, v85 quad_perm:[2,3,0,1] row_mask:0xf bank_mask:0xf\n\t"
    MF(72, 20)
    "s_nop 0\n\t"
    "v_max_f32_dpp v85, v85, v85 row_half_mirror row_mask:0xf bank_mask:0xf\n\t"
    MF(76, 24)
    "v_sub_f32 v86, v84, v85\n\t"
    MF(80, 28)
    "v_exp_f32 v86, v86\n\t"
    "s_nop 1\n\t"
    "v_mov_b32_dpp v90, v86 row_newbcast:0 row_mask:0x5 bank_mask:0xf\n\t"
    "v_mov_b32_dpp v91, v86 row_newbcast:1 row_mask:0x5 bank_mask:0xf\n\t"
    "v_mov_b32_dpp v92, v86 row_newbcast:2 row_mask:0x5 bank_mask:0xf\n\t"
    "v_mov_b32_dpp v93, v86 row_newbcast:3 row_mask:0x5 bank_mask:0xf\n\t"
    "v_mov_b32_dpp v90, v86 row_newbcast:4 row_mask:0xa bank_mask:0xf\n\t"
    "v_mov_b32_dpp v91, v86 row_newbcast:5 row_mask:0xa bank_mask:0xf\n\t"
    "v_mov_b32_dpp v92, v86 row_newbcast:6 row_mask:0xa bank_mask:0xf\n\t"
    "v_mov_b32_dpp v93, v86 row_newbcast:7 row_mask:0xa bank_mask:0xf\n\t"
    "v_add_f32_dpp v87, v86, v86 quad_perm:[1,0,3,2] row_mask:0xf bank_mask:0xf\n\t"
    "v_pk_mul_f32 v[52:53], v[52:53], v[90:91]\n\t"
    "v_pk_mul_f32 v[56:57], v[56:57], v[90:91]\n\t"
    "v_add_f32_dpp v87, v87, v87 quad_perm:[2,3,0,1] row_mask:0xf bank_mask:0xf\n\t"
    "v_pk_mul_f32 v[60:61], v[60:61], v[90:91]\n\t"
    "v_pk_mul_f32 v[64:65], v[64:65], v[90:91]\n\t"
    "v_add_f32_dpp v87, v87, v87 row_half_mirror row_mask:0xf bank_mask:0xf\n\t"
    "v_pk_mul_f32 v[68:69], v[68:69], v[90:91]\n\t"
    "v_pk_mul_f32 v[72:73], v[72:73], v[90:91]\n\t"
    "v_rcp_f32 v87, v87\n\t"
    "v_pk_mul_f32 v[76:77], v[76:77], v[90:91]\n\t"
    "v_pk_mul_f32 v[80:81], v[80:81], v[90:91]\n\t"
    "v_pk_fma_f32 v[52:53], v[54:55], v[92:93], v[52:53]\n\t"
    "v_pk_fma_f32 v[68:69], v[70:71], v[92:93], v[68:69]\n\t"
    "v_pk_fma_f32 v[56:57], v[58:59], v[92:93], v[56:57]\n\t"
    "v_pk_fma_f32 v[72:73], v[74:75], v[92:93], v[72:73]\n\t"
    "v_pk_fma_f32 v[60:61], v[62:63], v[92:93], v[60:61]\n\t"
    "v_pk_fma_f32 v[76:77], v[78:79], v[92:93], v[76:77]\n\t"
    "v_pk_fma_f32 v[64:65], v[66:67], v[92:93], v[64:65]\n\t"
    "v_pk_fma_f32 v[80:81], v[82:83], v[92:93], v[80:81]\n\t"
    "v_add_f32 v52, v52, v53\n\t"
    "v_add_f32 v68, v68, v69\n\t"
    "v_add_f32 v56, v56, v57\n\t"
    "v_add_f32 v72, v72, v73\n\t"
    "v_add_f32 v60, v60, v61\n\t"
    "v_add_f32 v76, v76, v77\n\t"
    "v_add_f32 v64, v64, v65\n\t"
    "v_add_f32 v80, v80, v81\n\t"
    "s_nop 1\n\t"
    FOLD_TAIL
    "v_mul_f32 v52, v52, v87\n\t"
    "ds_write_b32 %[lw], v52 offset:%c[lo]\n\t"
    : [sp] "+s"(spins)
    : [voff] "v"(voff), [voff2] "v"(voff2), [ws] "s"(ws), [ws2] "s"(ws2), [wb] "i"(WB), [cap] "i"(SPIN_CAP), [pm] "s"(PMASK), [lw] "v"(ldsw), [lo] "i"(L_RED3 - L_RED), [pre] "i"(PRE) : "memory", "vcc", "scc");
}
#define PACK_STORE \
  "s_nop 1\n\t" \
  "v_mov_b32_dpp v53, v52 quad_perm:[1,0,3,2] row_mask:0xf bank_mask:0xf\n\t" \
  "v_cvt_pk_bf16_f32 v54, v52, v53\n\t" \
  "v_min_u32 v54, -2, v54\n\t" \
  "s_nop 1\n\t" \
  "v_mov_b32_dpp v55, v54 row_shl:2 row_mask:0xf bank_mask:0xf\n\t" \
  "v_mov_b32_dpp v56, v54 row_shl:4 row_mask:0xf bank_mask:0xf\n\t" \
  "v_mov_b32_dpp v57, v54 row_shl:6 row_mask:0xf bank_mask:0xf\n\t" \
  "s_mov_b64 exec, %[c8z]\n\t" \
  "global_store_dwordx4 %[so], v[54:57], %[ws] sc1\n\t" \
  "s_mov_b64 exec, -1\n\t" \
  "s_nop 1\n\t"
template <bool XS>
__device__ __forceinline__ void seg_final2(u32 ldsr, u32 xsr, u32 so, const char* ws) {
  constexpr u32 LO = XS ? L_RED3 - L_RED : 0;
  if constexpr (XS) {
    asm volatile(
      "ds_read_b128 v[52:55], %[lr] offset:%c[lo]\n\t"
      "ds_read_b128 v[56:59], %[lr] offset:%c[lo]+16\n\t"
      "ds_read_b32 v60, %[xr]\n\t"
      "s_waitcnt lgkmcnt(1)\n\t"
      "v_pk_add_f32 v[52:53], v[52:53], v[56:57]\n\t"
      "v_pk_add_f32 v[54:55], v[54:55], v[58:59]\n\t"
      "v_pk_add_f32 v[52:53], v[52:53], v[54:55]\n\t"
      "s_waitcnt lgkmcnt(0)\n\t"
      "v_add_f32 v52, v52, v53\n\t"
      "v_add_f32 v52, v52, v60\n\t"
      PACK_STORE
      :: [lr] "v"(ldsr), [xr] "v"(xsr), [so] "v"(so), [ws] "s"(ws), [c8z] "s"(C8Z), [lo] "i"(LO) : "memory");
  } else {
    asm volatile(
      "ds_read_b128 v[52:55], %[lr]\n\t"
      "ds_read_b128 v[56:59], %[lr] offset:16\n\t"
      "s_waitcnt lgkmcnt(0)\n\t"
      "v_pk_add_f32 v[52:53], v[52:53], v[56:57]\n\t"
      "v_pk_add_f32 v[54:55], v[54:55], v[58:59]\n\t"
      "v_pk_add_f32 v[52:53], v[52:53], v[54:55]\n\t"
      "v_add_f32 v52, v52, v53\n\t"
      PACK_STORE
      :: [lr] "v"(ldsr), [so] "v"(so), [ws] "s"(ws), [c8z] "s"(C8Z) : "memory");
  }
}
template <int LOFF>
__device__ __forceinline__ void seg_qpoll(u32 voff, const char* ws, u32 ldsw, u32& spins) {
  asm volatile(
    SNOP "Lp%=:\n\t"
    "global_load_dwordx4 v[52:55], %[voff], %[ws] sc1\n\t"
    "s_add_u32 %[sp], %[sp], 1\n\t"
    "s_waitcnt vmcnt(0)\n\t"
    "v_max3_u32 v56, v52, v53, v54\n\t"
    "v_max_u32 v56, v56, v55\n\t"
    "v_cmp_eq_u32 vcc, -1, v56\n\t"
    "ds_write_b128 %[lw], v[52:55] offset:%c[lo]\n\t"
    "s_cbranch_vccz Lk%=\n\t"
    "s_cmp_lt_u32 %[sp], %[cap]\n\t"
    "s_cbranch_scc1 Lp%=\n\t"
    "Lk%=:\n\t"
    : [sp] "+s"(spins) : [voff] "v"(voff), [ws] "s"(ws), [lw] "v"(ldsw), [cap] "i"(SPIN_CAP), [lo] "i"(LOFF) : "memory", "vcc", "scc");
}
template <int TO>
__device__ __forceinline__ void seg_kpre(u32 ka0, u32 ka1) {
  asm volatile(
    "ds_read_b128 v[48:51], %0 offset:%c2\n\tds_read_b128 v[64:67], %1 offset:%c2\n\t"
    "ds_read_b128 v[52:55], %0 offset:%c2+512\n\tds_read_b128 v[68:71], %1 offset:%c2+512\n\t"
    "ds_read_b128 v[56:59], %0 offset:%c2+1024\n\tds_read_b128 v[72:75], %1 offset:%c2+1024\n\t"
    "ds_read_b128 v[60:63], %0 offset:%c2+1536\n\tds_read_b128 v[76:79], %1 offset:%c2+1536\n\t"
    "v_mov_b32 v96, 0\n\tv_mov_b32 v97, 0\n\tv_mov_b32 v98, 0\n\tv_mov_b32 v99, 0\n\t"
    "v_mov_b32 v100, 0\n\tv_mov_b32 v101, 0\n\tv_mov_b32 v102, 0\n\tv_mov_b32 v103, 0\n\t"
    "v_mov_b32 v104, 0\n\tv_mov_b32 v105, 0\n\tv_mov_b32 v106, 0\n\tv_mov_b32 v107, 0\n\t"
    "v_mov_b32 v108, 0\n\tv_mov_b32 v109, 0\n\tv_mov_b32 v110, 0\n\tv_mov_b32 v111, 0\n\t"
    :: "v"(ka0), "v"(ka1), "i"(TO) : "memory");
}
#define QK(s, k, q, c) "v_mfma_f32_16x16x32_bf16 v[" #s ":" #s "+3], v[" #k ":" #k "+3], v[" #q ":" #q "+3], " c "\n\t"
#define TR(d, a, off) "ds_read_b64_tr_b16 v[" #d ":" #d "+1], %[" #a "] offset:%c[to]+" #off "\n\t"
#define PV(r) "v_mfma_f32_16x16x32_bf16 v[" #r ":" #r "+3], v[124:127], v[" #r ":" #r "+3], 0\n\t"
#define SM(r) "v_fma_f32 v" #r ", v" #r ", v99, v121\n\tv_exp_f32 v" #r ", v" #r "\n\t"
#define PVA(acc, a, v, c) "v_mfma_f32_16x16x32_bf16 v[" #acc ":" #acc "+3], v[" #a ":" #a "+3], v[" #v ":" #v "+3], " c "\n\t"
#define PCP(d, s, ctl, bank) "v_mov_b32_dpp v" #d ", v" #s " " ctl " row_mask:0xf bank_mask:" bank "\n\t"
template <int TO>
__device__ __forceinline__ void seg_attn(u32 qa, u32 sta, u32 va0, u32 va1, u32 va2, u32 va3, u32 oa, u32 mla) {
  asm volatile(
    "ds_read_b128 v[80:83], %[qa]\n\t"
    "ds_read_b128 v[84:87], %[qa] offset:64\n\t"
    "ds_read_b128 v[88:91], %[qa] offset:128\n\t"
    "ds_read_b128 v[92:95], %[qa] offset:192\n\t"
    "ds_read_b64 v[120:121], %[sta]\n\t"
    "s_waitcnt lgkmcnt(4)\n\t"
    QK(112, 48, 80, "0") QK(116, 64, 80, "0")
    "s_waitcnt lgkmcnt(3)\n\t"
    QK(112, 52, 84, "v[112:115]") QK(116, 68, 84, "v[116:119]")
    "s_waitcnt lgkmcnt(2)\n\t"
    QK(112, 56, 88, "v[112:115]") QK(116, 72, 88, "v[116:119]")
    "s_waitcnt lgkmcnt(0)\n\t"
    QK(112, 60, 92, "v[112:115]")
    "v_add_f32_dpp v120, v120, v120 quad_perm:[1,0,3,2] row_mask:0xf bank_mask:0xf\n\t"
    "v_add_f32_dpp v121, v121, v121 quad_perm:[1,0,3,2] row_mask:0xf bank_mask:0xf\n\t"
    QK(116, 76, 92, "v[116:119]")
    TR(48, va0, 0)
    "v_add_f32_dpp v120, v120, v120 quad_perm:[2,3,0,1] row_mask:0xf bank_mask:0xf\n\t"
    "v_add_f32_dpp v121, v121, v121 quad_perm:[2,3,0,1] row_mask:0xf bank_mask:0xf\n\t"
    TR(50, va1, 0) TR(52, va2, 0)
    "v_add_f32_dpp v120, v120, v120 row_half_mirror row_mask:0xf bank_mask:0xf\n\t"
    "v_add_f32_dpp v121, v121, v121 row_half_mirror row_mask:0xf bank_mask:0xf\n\t"
    TR(54, va3, 0)
    "v_mul_f32 v122, 0xbb000000, v120\n\t"
    "v_mul_f32 v123, 0x3b000000, v121\n\t"
    "v_fma_f32 v123, -v122, v122, v123\n\t"
    "v_fmac_f32_dpp v112, v112, v122 row_shl:4 row_mask:0xf bank_mask:0xf bound_ctrl:1\n\t"
    "v_max_f32 v123, 0, v123\n\t"
    "v_fmac_f32_dpp v113, v113, v122 row_shl:4 row_mask:0xf bank_mask:0xf bound_ctrl:1\n\t"
    "v_add_f32 v123, 0x3727c5ac, v123\n\t"
    "v_fmac_f32_dpp v114, v114, v122 row_shl:4 row_mask:0xf bank_mask:0xf bound_ctrl:1\n\t"
    "v_rsq_f32 v123, v123\n\t"
    "v_fmac_f32_dpp v115, v115, v122 row_shl:4 row_mask:0xf bank_mask:0xf bound_ctrl:1\n\t"
    "v_fmac_f32_dpp v116, v116, v122 row_shl:4 row_mask:0xf bank_mask:0xf bound_ctrl:1\n\t"
    "v_max3_f32 v120, v112, v113, v114\n\t"
    "v_fmac_f32_dpp v117, v117, v122 row_shl:4 row_mask:0xf bank_mask:0xf bound_ctrl:1\n\t"
    "v_fmac_f32_dpp v118, v118, v122 row_shl:4 row_mask:0xf bank_mask:0xf bound_ctrl:1\n\t"
    "v_fmac_f32_dpp v119, v119, v122 row_shl:4 row_mask:0xf bank_mask:0xf bound_ctrl:1\n\t"
    "v_max3_f32 v120, v120, v115, v116\n\t"
    "v_mul_f32 v123, %[c2], v123\n\t"
    "v_max3_f32 v120, v120, v117, v118\n\t"
    "v_max_f32 v120, v120, v119\n\t"
    "v_mov_b32 v121, v120\n\t"
    TR(56, va0, 512) TR(58, va1, 512)
    "v_permlane16_swap_b32 v120, v121\n\t"
    "v_max_f32 v120, v120, v121\n\t"
    "v_mov_b32 v121, v120\n\t"
    TR(60, va2, 512) TR(62, va3, 512)
    "v_permlane32_swap_b32 v120, v121\n\t"
    "v_max_f32 v120, v120, v121\n\t"
    "v_mul_f32_e64 v121, -v120, v123\n\t"
    "v_mul_f32 v122, v120, v123\n\t"
    "v_fma_f32 v112, v112, v123, v121\n\t"
    "v_fma_f32 v113, v113, v123, v121\n\t"
    "v_exp_f32 v112, v112\n\t"
    TR(64, va0, 1024)
    "v_fma_f32 v114, v114, v123, v121\n\t"
    "v_exp_f32 v113, v113\n\t"
    TR(66, va1, 1024)
    "v_fma_f32 v115, v115, v123, v121\n\t"
    "v_exp_f32 v114, v114\n\t"
    TR(68, va2, 1024)
    "v_fma_f32 v116, v116, v123, v121\n\t"
    "v_exp_f32 v115, v115\n\t"
    TR(70, va3, 1024)
    "v_fma_f32 v117, v117, v123, v121\n\t"
    "v_exp_f32 v116, v116\n\t"
    "v_cvt_pk_bf16_f32 v124, v112, v113\n\t"
    "v_fma_f32 v118, v118, v123, v121\n\t"
    "v_exp_f32 v117, v117\n\t"
    TR(72, va0, 1536)
    "v_fma_f32 v119, v119, v123, v121\n\t"
    "v_exp_f32 v118, v118\n\t"
    "v_cvt_pk_bf16_f32 v125, v114, v115\n\t"
    "v_exp_f32 v119, v119\n\t"
    TR(74, va1, 1536) TR(76, va2, 1536) TR(78, va3, 1536)
    "v_cvt_pk_bf16_f32 v126, v116, v117\n\t"
    "v_cvt_pk_bf16_f32 v127, v118, v119\n\t"
    PCP(96, 124, "quad_perm:[0,1,2,3]", "0x1") PCP(100, 124, "row_shr:4", "0x2") PCP(104, 124, "quad_perm:[0,1,2,3]", "0x4") PCP(108, 124, "row_shr:4", "0x8")
    PCP(97, 125, "quad_perm:[0,1,2,3]", "0x1") PCP(101, 125, "row_shr:4", "0x2") PCP(105, 125, "quad_perm:[0,1,2,3]", "0x4") PCP(109, 125, "row_shr:4", "0x8")
    PCP(98, 126, "quad_perm:[0,1,2,3]", "0x1") PCP(102, 126, "row_shr:4", "0x2") PCP(106, 126, "quad_perm:[0,1,2,3]", "0x4") PCP(110, 126, "row_shr:4", "0x8")
    PCP(99, 127, "quad_perm:[0,1,2,3]", "0x1") PCP(103, 127, "row_shr:4", "0x2") PCP(107, 127, "quad_perm:[0,1,2,3]", "0x4") PCP(111, 127, "row_shr:4", "0x8")
    "v_mov_b32 v84, %[ones]\n\tv_mov_b32 v85, %[ones]\n\tv_mov_b32 v86, %[ones]\n\tv_mov_b32 v87, %[ones]\n\t"
    "s_waitcnt lgkmcnt(0)\n\t"
    PVA(112, 96, 48, "0") PVA(116, 96, 64, "0")
    PVA(112, 100, 52, "v[112:115]") PVA(116, 100, 68, "v[116:119]")
    PVA(112, 104, 56, "v[112:115]") PVA(116, 104, 72, "v[116:119]")
    PVA(112, 108, 60, "v[112:115]") PVA(116, 108, 76, "v[116:119]")
    "v_mfma_f32_16x16x32_bf16 v[80:83], v[84:87], v[124:127], 0\n\t"
    "s_nop 6\n\t"
    "ds_write_b128 %[oa], v[112:115]\n\t"
    "ds_write_b128 %[oa], v[116:119] offset:1024\n\t"
    "s_mov_b64 exec, 0xf\n\t"
    "ds_write2_b32 %[mla], v122, v80 offset0:0 offset1:16\n\t"
    "s_mov_b64 exec, -1\n\t"
    :: [qa] "v"(qa), [sta] "v"(sta), [va0] "v"(va0), [va1] "v"(va1), [va2] "v"(va2), [va3] "v"(va3), [oa] "v"(oa), [mla] "v"(mla),
       [c2] "s"(C2), [ones] "s"(ONES), [to] "i"(TO) : "memory");
}
__device__ __forceinline__ void seg_merge(u32 mla, u32 oa, u32 so, const char* ws) {
  asm volatile(
    "ds_read_b128 v[48:51], %[mla]\n\t"
    "ds_read_b128 v[60:63], %[mla] offset:64\n\t"
    "ds_read2st64_b32 v[56:57], %[oa] offset0:0 offset1:8\n\t"
    "ds_read2st64_b32 v[58:59], %[oa] offset0:16 offset1:24\n\t"
    "s_waitcnt lgkmcnt(3)\n\t"
    "v_max3_f32 v52, v48, v49, v50\n\t"
    "v_max_f32 v52, v52, v51\n\t"
    "v_sub_f32 v48, v48, v52\n\t"
    "v_sub_f32 v49, v49, v52\n\t"
    "v_exp_f32 v48, v48\n\t"
    "v_sub_f32 v50, v50, v52\n\t"
    "v_exp_f32 v49, v49\n\t"
    "v_sub_f32 v51, v51, v52\n\t"
    "v_exp_f32 v50, v50\n\t"
    "v_exp_f32 v51, v51\n\t"
    "s_waitcnt lgkmcnt(0)\n\t"
    "v_pk_mul_f32 v[60:61], v[60:61], v[48:49]\n\t"
    "v_pk_mul_f32 v[56:57], v[56:57], v[48:49]\n\t"
    "v_pk_fma_f32 v[60:61], v[62:63], v[50:51], v[60:61]\n\t"
    "v_pk_fma_f32 v[56:57], v[58:59], v[50:51], v[56:57]\n\t"
    "v_add_f32 v60, v60, v61\n\t"
    "v_add_f32 v56, v56, v57\n\t"
    "v_rcp_f32 v60, v60\n\t"
    "s_nop 0\n\t"
    "v_mul_f32 v52, v56, v60\n\t"
    PACK_STORE
    :: [mla] "v"(mla), [oa] "v"(oa), [so] "v"(so), [ws] "s"(ws), [c8z] "s"(C8Z) : "memory");
}

__device__ __forceinline__ void dma16(u32 voff, const char* sbase, u32 lds_dst) {
  u32 keep;
  asm volatile(SNOP "s_mov_b32 %0, m0\n\ts_mov_b32 m0, %3\n\ts_nop 0\n\tglobal_load_lds_dwordx4 %1, %2" NTKV "\n\ts_mov_b32 m0, %0"
               : "=&s"(keep) : "v"(voff), "s"(sbase), "s"(lds_dst) : "memory");
}
__device__ __forceinline__ void kv_load(const char* base, int w, int lane, u32 ldsb) {
  asm volatile("" : "+s"(base));
  const int r8 = (lane & 31) >> 2;
#pragma unroll
  for (int i = 0; i < 4; ++i) {
    const u32 ch = 8 * (i & 1) + 4 * (lane >> 5) + ((lane & 3) ^ ((2 * (i >> 1) + (r8 >> 2)) & 3));
    dma16(r8 * 256 + ch * 16, base + (size_t)(16 * w + 8 * (i >> 1)) * 256, ldsb + w * 4096 + i * 1024);
  }
}
__device__ __forceinline__ bool has_sent(v4u x) {
  const u32 m = __builtin_elementwise_max(__builtin_elementwise_max(x[0], x[1]), __builtin_elementwise_max(x[2], x[3]));
  return __builtin_amdgcn_ballot_w64(m == SENT) != 0ull;
}
__device__ __forceinline__ v4u poll128(const char* base, u32 voff) {
  v4u x;
  for (u32 spins = 0;; ++spins) {
    asm volatile("s_nop 4\n\tglobal_load_dwordx4 %0, %1, %2 sc1\n\ts_waitcnt vmcnt(0)" : "=&v"(x) : "v"(voff), "s"(base) : "memory");
    if (!has_sent(x) || spins > SPIN_CAP) break;
  }
  return x;
}
__device__ __forceinline__ v4u pack8(float v) {
  const u32 x = safe(pk(v, dpp<XOR1>(v)));
  return v4u{x, dppu<0x102>(x), dppu<0x104>(x), dppu<0x106>(x)};
}
__device__ __forceinline__ void sum_partials(v4u x, float (&v)[8]) {
#pragma unroll
  for (int m = 0; m < 4; ++m) { v[2 * m] = bflo(x[m]); v[2 * m + 1] = bfhi(x[m]); }
#pragma unroll
  for (int j = 0; j < 8; ++j) v[j] += dpp<XOR1>(v[j]);
#pragma unroll
  for (int j = 0; j < 8; ++j) v[j] += dpp<XOR2>(v[j]);
#pragma unroll
  for (int j = 0; j < 8; ++j) v[j] += dpp<HMIR>(v[j]);
}
__device__ __forceinline__ void x_stats(v4u xa, char* lds, int w, int lane) {
  const bool p = (lane >> 3) & 1; const int kq = lane >> 4;
  const v4f gm = mfma(xa, xa, v4f{0.f, 0.f, 0.f, 0.f});
  const v4f sm = mfma(xa, v4u{ONES, ONES, ONES, ONES}, v4f{0.f, 0.f, 0.f, 0.f});
  float s2 = (gm[0] + gm[1]) + (gm[2] + gm[3]);
  s2 = (p == (bool)(kq >> 1)) ? s2 : 0.f;
  s2 += dpp<XOR1>(s2); s2 += dpp<XOR2>(s2); s2 += dpp<HMIR>(s2); s2 += dpp<ROR8>(s2);
  s2 = x32_add(x16_add(s2));
  float s1 = (sm[0] + sm[1]) + (sm[2] + sm[3]);
  s1 = x32_add(x16_add(s1));
  if (lane == 0) *(v2f*)(lds + L_ST + w * 8) = v2f{s1, s2};
}
}

#define ST(i) do {} while (0)
#define STFLUSH(sv, base) do {} while (0)
#define SGPR_ATTR

__device__ __forceinline__ u32 lb_st() { return L_ST; }
#define OPAQUE_LANE() int lane = lane0; asm volatile("" : "+v"(lane)); const int c8 = lane & 7, kq = lane >> 4; const bool p = (lane >> 3) & 1; (void)c8; (void)p; (void)kq
#define OPAQUE_S(x) asm volatile("" : "+s"(x))

constexpr int FW = 4;

struct Addr {
  u32 vB, vA, vL, lw, so;
  u32 mr, orr;
  u32 r0, r1, r2, r3, r4, r5, r6, r7, r8;
};
#define PIN(x) asm volatile("" : "+v"(x))

template <int TYPE>
__device__ __forceinline__ void layer(const int rep, char* ws, char* lds, int b, int g, int s, int w, const int lane0, v4u xraw, Addr& A, u32 qaT,
                                      const char* kB, const char* vB, const char* wqB, const char* cwo,
                                      const u32 tag, int& stampv, u32 (&cnt)[3]) {
  constexpr int RQ = TYPE ? R_WQB : R_WQA, RO = TYPE ? R_WOB : R_WOA;
  const int L = rep * 2 + TYPE;
  OPAQUE_S(b); OPAQUE_S(g); OPAQUE_S(s); OPAQUE_S(w);
  const u32 grp = (u32)(b * 8 + g), cu = grp * 8 + s;
#define LD_WQ(R, ptr) do { OPAQUE_LANE(); OPAQUE_S(ptr); bulk8<R, 8192>(ptr + (size_t)(64 * w) * 8192 + (512 * g + 64 * s) * 2, (u32)(lane >> 3) * 65536 + c8 * 16); } while (0)
#define LD_WO(R, ptr) do { OPAQUE_LANE(); OPAQUE_S(ptr); bulk8<R, 1024>(ptr + (size_t)(512 * g + 64 * w) * 1024 + 128 * s, (u32)(lane >> 3) * 8192 + c8 * 16); } while (0)
  const char* wsL = ws + L * BOXL;
  const char* box = ws + WS_BOX + cu * BOX + L * BOXL;
  {
    if (rep == 0) {
      if (TYPE == 0) VMWAIT(8); else if (VB_AT == 1 && KB_AT == 1) VMWAIT(8); else if (VB_AT == 1 || KB_AT == 1) VMWAIT(4); else VMWAIT(0);
      if (w >= 4) ST(7);
      transpose8<RQ>();
    }
    if (L > 0) {
      const char* wsP = wsL - BOXL;
      if (DLY_B && rep) __builtin_amdgcn_s_sleep(DLY_B);
      ST(0);
      u32 sp = 0;
      seg_p1<RQ, true, 0>(A.vB, wsP, A.lw, sp);
      cnt[0] += sp;
    } else {
      const v4u xa = (lane0 & 7) == 0 ? xraw : v4u{0u, 0u, 0u, 0u};
      asm volatile("v_mov_b32 v48, %0\n\tv_mov_b32 v49, %1\n\tv_mov_b32 v50, %2\n\tv_mov_b32 v51, %3" :: "v"(xa[0]), "v"(xa[1]), "v"(xa[2]), "v"(xa[3]));
      u32 sp = 0;
      seg_p1<RQ, false, 0>(0, ws, A.lw, sp);
    }
    ST(2);
    v4u xa;
    asm volatile("v_mov_b32 %0, v48\n\tv_mov_b32 %1, v49\n\tv_mov_b32 %2, v50\n\tv_mov_b32 %3, v51" : "=v"(xa[0]), "=v"(xa[1]), "=v"(xa[2]), "=v"(xa[3]));
    if (rep == 0) {
      asm volatile("v_mov_b32 v48, %0\n\tv_mov_b32 v49, %0\n\tv_mov_b32 v50, %0\n\tv_mov_b32 v51, %0" :: "s"(ONES));
      u32 sp = 0;
      seg_p1<RQ, false, L_REDC - L_RED>(0, ws, A.lw, sp);
    }
    BAR();
    ST(3);
    if (w == FW) {
      seg_final2<false>(A.r2, 0, A.so, box + BOX_Q);
      ST(4);
      OPAQUE_LANE();
      if (rep == 0) {
        const __amdgpu_buffer_rsrc_t rs = __builtin_amdgcn_make_buffer_rsrc(ws, 0, WS_END, 0x00020000);
        const v4f r0 = *(const v4f*)(lds + L_REDC + lane * 32), r1 = *(const v4f*)(lds + L_REDC + lane * 32 + 16);
        const float cs = 0.125f * (((r0[0] + r0[1]) + (r0[2] + r0[3])) + ((r1[0] + r1[1]) + (r1[2] + r1[3])));
        const v4u pc = pack8(cs);
        if (c8 == 0) __builtin_amdgcn_raw_buffer_store_b128(pc, rs, WS_BOX + cu * BOX + BOX_CS + TYPE * 128 + lane * 2, 0, 16);
        if (TYPE == 1 && VB_AT == 0) kv_load(vB, w, lane, L_V + 32768);
      }
      x_stats(xa, lds, w, lane);
      if (L == 0) {
        const u64 flag = (u64)tag | ((u64)(~tag) << 32);
        for (u32 spins = 0;; ++spins) {
          const u64 f = __hip_atomic_load((GAS u64*)(ws + WS_RDY + b * 512 + lane * 8), __ATOMIC_RELAXED, __HIP_MEMORY_SCOPE_AGENT);
          if (__all(f == flag) || spins > SPIN_CAP) break;
        }
        if (g == 0 && s == 0 && lane == 0) __hip_atomic_store((GAS u32*)(ws + WS_EPOCH + b * 256), tag, __ATOMIC_RELAXED, __HIP_MEMORY_SCOPE_AGENT);
      }
      if (DLY_C && rep) __builtin_amdgcn_s_sleep(DLY_C);
      u32 sp = 0;
      seg_qpoll<0>(A.r0, wsL, A.r1, sp);
      cnt[1] += sp;
      if (rep == 0) { sp = 0; seg_qpoll<L_CSI + TYPE * 1088 - L_Q>(A.r0, ws + (BOX_CS + TYPE * 128 - BOX_Q), A.r1, sp); }
      ST(5);
    } else {
      OPAQUE_LANE();
      if (rep == 0 && TYPE == 1 && VB_AT == 0) kv_load(vB, w, lane, L_V + 32768);
      x_stats(xa, lds, w, lane);
    }
    if (g == 0 && w == s) {
      OPAQUE_LANE();
      float v[8];
      sum_partials(xa, v);
      if (c8 == 0) {
        float* xs = (float*)(lds + L_XS) + 16 * kq + (p ? 8 : 0);
        *(v4f*)xs = v4f{v[0], v[1], v[2], v[3]};
        *(v4f*)(xs + 4) = v4f{v[4], v[5], v[6], v[7]};
      }
    }
    if (rep == 0) {
      VMWAIT(0);
      if (w >= 4) ST(8);
      BAR();
      LD_WO(RO, cwo);
      if (TYPE == 0 && KB_AT == 0 && w >= 4) {
        OPAQUE_LANE();
        kv_load(kB, 2 * (w - 4), lane, L_K + 32768);
        kv_load(kB, 2 * (w - 4) + 1, lane, L_K + 32768);
      }
    }
  }
  if (w < 4) {
    seg_kpre<TYPE * 32768>(A.r0, A.r1);
    BAR();
    ST(7);
    seg_attn<TYPE * 32768>(qaT, A.r6, A.r2, A.r3, A.r4, A.r5, A.r7, A.r8);
    ST(8);
  } else {
    BAR();
  }
  {
    BAR();
    seg_merge(A.mr, A.orr, A.so, box + w * 128);
    if (w == 7) {
      OPAQUE_LANE();
      const __amdgpu_buffer_rsrc_t rs = __builtin_amdgcn_make_buffer_rsrc(ws, 0, WS_END, 0x00020000);
      const int hh = (lane >> 2) & 3, aa = lane & 3;
      const float m1 = ((const float*)(lds + L_ML))[hh * 4 + aa], l1 = ((const float*)(lds + L_ML))[16 + hh * 4 + aa];
      float M1 = __builtin_fmaxf(m1, dpp<XOR1>(m1)); M1 = __builtin_fmaxf(M1, dpp<XOR2>(M1));
      float L1 = __builtin_amdgcn_exp2f(m1 - M1) * l1; L1 += dpp<XOR1>(L1); L1 += dpp<XOR2>(L1);
      const u32 e = safe(__float_as_uint(M1 + __builtin_amdgcn_logf(L1)));
      const v4u ev = {dppu<0x150>(e), dppu<0x154>(e), dppu<0x158>(e), dppu<0x15C>(e)};
      if (lane < 8) __builtin_amdgcn_raw_buffer_store_b128(ev, rs, WS_LSR + ((L * 32 + grp) * 8 + lane) * 128 + s * 16, 0, 16);
    }
    ST(9);
  }
  {
    const char* wsS = ws + L * 32768;
    if (rep == 0) {
      if (TYPE == 0 && KB_AT == 0 && w >= 4) VMWAIT(8); else VMWAIT(0);
      transpose8<RO>();
    } else {
      if (DLY_A) __builtin_amdgcn_s_sleep(DLY_A);
    }
    ST(1);
    u32 sp = 0;
    seg_p3<RO, 0>(A.vA, A.vL, wsL, wsS, A.lw, sp);
    cnt[2] += sp;
    ST(10);
    BAR();
    if (w == FW) seg_final2<true>(A.r2, A.r3, A.so, box + BOX_X);
    ST(11);
    if (rep == 0 && TYPE == 0) {
      LD_WQ(R_WQB, wqB);
      OPAQUE_LANE();
      if (KB_AT == 1) kv_load(kB, w, lane, L_K + 32768);
      if (VB_AT == 1) kv_load(vB, w, lane, L_V + 32768);
    }
  }
  STFLUSH(stampv, L * 12);
}

__global__ void __launch_bounds__(512, 2) __attribute__((amdgpu_num_vgpr(24) SGPR_ATTR))
fused_kernel(const char* __restrict__ xin, const char* kA, const char* vA, const char* wqA, const char* woA,
             const char* kB, const char* vB, const char* wqB, const char* woB, unsigned short* out, char* ws) {
  extern __shared__ __attribute__((aligned(16))) char lds[];
  const int id = blockIdx.x, g = id & 7, b = id >> 6, s = (id >> 3) & 7;
  const int tid = threadIdx.x, lane = tid & 63, w = __builtin_amdgcn_readfirstlane(tid >> 6);
  const int c8 = lane & 7, kq = lane >> 4;
  const bool p = (lane >> 3) & 1;
  int stampv = 0;
  const __amdgpu_buffer_rsrc_t rs = __builtin_amdgcn_make_buffer_rsrc(ws, 0, WS_END, 0x00020000);
  const u32 cu = (u32)((b * 8 + g) * 8 + s);
  asm volatile("" ::: "v127", "v255");
  u32 ep; v4u xraw;
  asm volatile("s_nop 4\n\tglobal_load_dword v56, %0, %1 sc1\n\tglobal_load_dwordx4 v[52:55], %2, %3"
               :: "v"(WS_EPOCH + b * 256), "s"(ws), "v"((u32)((b * 512 + 64 * w + 16 * kq + (p ? 8 : 0)) * 2)), "s"(xin) : "memory");
  if (w == 5) {
    const v4u sv = {SENT, SENT, SENT, SENT};
#pragma unroll
    for (int i = 0; i < 5; ++i) __builtin_amdgcn_raw_buffer_store_b128(sv, rs, WS_BOX + cu * BOX + i * 1024 + lane * 16, 0, 16);
    const u32 o6 = lane < 16 ? WS_BOX + cu * BOX + 5120 + lane * 16
                             : WS_LSR + (u32)(((((lane - 16) >> 3) * 32 + b * 8 + g) * 8 + (lane & 7)) * 128 + s * 16);
    if (lane < 48) __builtin_amdgcn_raw_buffer_store_b128(sv, rs, o6, 0, 16);
  }
  if (w == 6) ((float*)(lds + L_XS))[lane] = 0.f;
  const size_t kvoff = ((size_t)(b * 8 + g) * SK + (SK - WIN) + s * 128) * 256;
  const size_t qoff = (size_t)(64 * w) * 8192 + (512 * g + 64 * s) * 2;
  bulk8<R_WQA, 8192>(wqA + qoff, (u32)(lane >> 3) * 65536 + c8 * 16);
  VMWAIT(8);
  asm volatile("v_mov_b32 %0, v52\n\tv_mov_b32 %1, v53\n\tv_mov_b32 %2, v54\n\tv_mov_b32 %3, v55\n\tv_mov_b32 %4, v56"
               : "=v"(xraw[0]), "=v"(xraw[1]), "=v"(xraw[2]), "=v"(xraw[3]), "=v"(ep));
  ST(6);
  const u32 tag = (u32)__builtin_amdgcn_readfirstlane((int)ep) + 1u;
  if (w == 5 && lane == 0) {
    const u64 flag = (u64)tag | ((u64)(~tag) << 32);
    __hip_atomic_store((GAS u64*)(ws + WS_RDY + b * 512 + (s * 8 + g) * 8), flag, __ATOMIC_RELAXED, __HIP_MEMORY_SCOPE_AGENT);
  }
  if (PGTEST && w == 7) {
    const size_t o = ((size_t)(b * 8 + g) * SK + s * 384) * 256;
    const char* t = (lane >> 4) == 0 ? kA : (lane >> 4) == 1 ? vA : (lane >> 4) == 2 ? kB : vB;
    const char* a = t + o + (size_t)(lane & 15) * (PGTEST == 1 ? 4096 : 128);
    asm volatile("global_load_dword v100, %0, off" :: "v"(a) : "memory");
  }
  asm volatile("s_barrier" ::: "memory");
  kv_load(kA + kvoff, w, lane, L_K);
  kv_load(vA + kvoff, w, lane, L_V);
  Addr A; u32 qa0, qa1;
  {
    const u32 grp = (u32)(b * 8 + g);
    const u32 oct = kq * 32 + (p ? 16 : 0);
    A.vB = WS_BOX + (u32)((b * 8 + c8) * 8 + w) * BOX + BOX_X + oct;
    A.vA = WS_BOX + (grp * 8 + c8) * BOX + w * 128 + oct;
    A.vL = WS_LSR + (grp * 8 + s) * 128 + c8 * 16 + (w >> 1) * 4;
    A.lw = L_RED + (8 * c8 + (lane >> 3)) * 32 + w * 4;
    A.so = lane * 2;
    A.mr = L_ML + (w >> 1) * 16;
    A.orr = L_OM + (w & 1) * 1024 + lane * 16 + (w >> 1) * 4;
    const int r16 = lane & 15, q4 = r16 >> 2, p2 = lane & 3;
    qa0 = L_Q + (lane & 3) * 272 + kq * 16;
    qa1 = qa0;
    if (lane & 4) { qa0 += L_CSI - L_Q; qa1 += L_CSI + 1088 - L_Q; }
    if (w < 4) {
      const u32 kb = L_K + 2048 * (4 * w + (r16 >> 2)) + 64 * (r16 & 3);
      A.r0 = kb + 16 * (kq ^ ((2 * (r16 >> 2)) & 3));
      A.r1 = kb + 256 + 16 * (kq ^ ((2 * (r16 >> 2) + 1) & 3));
      const u32 vb = L_V + 2048 * (4 * w + kq) + 64 * q4 + 8 * (p2 & 1);
      A.r2 = vb + 16 * (((p2 >> 1)) ^ ((2 * kq) & 3));
      A.r3 = vb + 256 + 16 * (((p2 >> 1)) ^ ((2 * kq + 1) & 3));
      A.r4 = vb + 16 * ((2 + (p2 >> 1)) ^ ((2 * kq) & 3));
      A.r5 = vb + 256 + 16 * ((2 + (p2 >> 1)) ^ ((2 * kq + 1) & 3));
      A.r7 = L_OM + w * 2048 + lane * 16;
      A.r8 = L_ML + (lane * 4 + w) * 4;
    } else {
      A.r0 = WS_BOX + (grp * 8 + (lane >> 3)) * BOX + BOX_Q + c8 * 16;
      A.r1 = L_Q + kq * 272 + (lane & 15) * 16;
      A.r2 = L_RED + lane * 32;
      A.r3 = L_XS + lane * 4;
      A.r4 = A.r5 = A.r7 = A.r8 = 0;
    }
    A.r6 = L_ST + c8 * 8;
    PIN(A.r6);
    PIN(A.vB); PIN(A.vA); PIN(A.vL); PIN(A.lw); PIN(A.so); PIN(A.mr); PIN(A.orr); PIN(A.r0); PIN(A.r1); PIN(A.r2); PIN(A.r3); PIN(A.r4); PIN(A.r5); PIN(A.r7); PIN(A.r8);
    PIN(qa0); PIN(qa1);
  }
  u32 cnt[3] = {0, 0, 0};
  for (int rep = 0; rep < 2; ++rep) {
    layer<0>(rep, ws, lds, b, g, s, w, lane, xraw, A, qa0, kB + kvoff, vB + kvoff, wqB, woA, tag, stampv, cnt);
    layer<1>(rep, ws, lds, b, g, s, w, lane, xraw, A, qa1, kB + kvoff, vB + kvoff, wqB, woB, tag, stampv, cnt);
  }
  if (g == 0 && w == s) {
    const u32 voff = WS_BOX + (u32)((b * 8 + c8) * 8 + w) * BOX + 3u * BOXL + BOX_X + kq * 32 + (p ? 16 : 0);
    if (DLY_B) __builtin_amdgcn_s_sleep(DLY_B);
    const v4u x = poll128(ws, voff);
    float v[8];
    sum_partials(x, v);
    if (c8 == 0) *(v4u*)(out + b * 512 + 64 * w + 16 * kq + (p ? 8 : 0)) = v4u{pk(v[0], v[1]), pk(v[2], v[3]), pk(v[4], v[5]), pk(v[6], v[7])};
  }
}

extern "C" void kernel_launch(void* const* d_in, const int* in_sizes, int n_in, void* d_out, int out_size, void* d_ws, size_t ws_size,
                              hipStream_t stream) {
  (void)hipFuncSetAttribute((const void*)fused_kernel, hipFuncAttributeMaxDynamicSharedMemorySize, L_END);
  fused_kernel<<<256, 512, L_END, stream>>>((const char*)d_in[0], (const char*)d_in[1], (const char*)d_in[2], (const char*)d_in[3], (const char*)d_in[4],
                                            (const char*)d_in[6], (const char*)d_in[7], (const char*)d_in[8], (const char*)d_in[9],
                                            (unsigned short*)d_out, (char*)d_ws);
}
```

```cpp
#include <hip/hip_runtime.h>
#include <stdint.h>

typedef unsigned int u32;
typedef unsigned long long u64;
typedef u32 v4u __attribute__((ext_vector_type(4)));
typedef u32 v2u __attribute__((ext_vector_type(2)));
typedef float v4f __attribute__((ext_vector_type(4)));
typedef float v2f __attribute__((ext_vector_type(2)));
typedef __bf16 v8b __attribute__((ext_vector_type(8)));
typedef __bf16 v2b __attribute__((ext_vector_type(2)));
#define GAS __attribute__((address_space(1)))

#define DLY_C 2
#define DLY_A 8
#define DLY_B 10
#define STG 2
#define DLY_A0 4
#define DLY_B0 4
#define NTKV ""
#define NTW ""

namespace {
constexpr int SK = 4096, WIN = 1024;
constexpr u32 WS_EPOCH = 0;
constexpr u32 WS_RDY = 4096;
constexpr u32 WS_LSR = 8192;
constexpr u32 WS_BOX = WS_LSR + 131072;
constexpr u32 BOXL = 1280, BOX_Q = 1024, BOX_X = 1152, BOX_CS = 5120, BOX = 5376;
constexpr u32 WS_END = WS_BOX + 256 * BOX;
constexpr u32 WS_DBG = 4u << 20;
constexpr u32 L_KV = 0;
constexpr u32 L_K = 0, L_V = 65536;
constexpr u32 L_OM = 131072;
constexpr u32 L_ML = L_OM + 8192;
constexpr u32 L_Q = L_ML + 128;
constexpr u32 L_CSI = L_Q + 1088;
constexpr u32 L_RED = L_CSI + 2176;
constexpr u32 L_REDC = L_RED + 2048;
constexpr u32 L_ST = L_REDC + 2048;
constexpr u32 L_RED3 = L_ST + 64;
constexpr u32 L_XS = L_RED3 + 2048;
constexpr u32 L_END = L_XS + 256;
static_assert(L_END <= 160 * 1024, "lds");
static_assert(L_Q % 16 == 0 && L_CSI % 16 == 0 && L_RED % 16 == 0 && L_ST % 16 == 0 && L_RED3 % 16 == 0 && L_XS % 16 == 0, "align");
constexpr u32 SENT = 0xFFFFFFFFu;
constexpr u32 SPIN_CAP = 1u << 15;
constexpr float C2 = 0.08838834764831845f * 1.4426950408889634f;
constexpr u32 ONES = 0x3F803F80u;
constexpr u64 PMASK = 0xFF00FF00FF00FF00ull;
constexpr u64 C8Z = 0x0101010101010101ull;

__device__ __forceinline__ float bflo(u32 w) { return __uint_as_float(w << 16); }
__device__ __forceinline__ float bfhi(u32 w) { return __uint_as_float(w & 0xffff0000u); }
__device__ __forceinline__ u32 pk(float lo, float hi) { v2f f = {lo, hi}; return __builtin_bit_cast(u32, __builtin_convertvector(f, v2b)); }
__device__ __forceinline__ u32 safe(u32 v) { return v < 0xFFFFFFFEu ? v : 0xFFFFFFFEu; }
template <int CTRL> __device__ __forceinline__ float dpp(float x) {
  return __builtin_bit_cast(float, __builtin_amdgcn_mov_dpp(__builtin_bit_cast(int, x), CTRL, 0xf, 0xf, true));
}
template <int CTRL> __device__ __forceinline__ u32 dppu(u32 x) { return (u32)__builtin_amdgcn_mov_dpp((int)x, CTRL, 0xf, 0xf, true); }
constexpr int XOR1 = 0xB1, XOR2 = 0x4E, HMIR = 0x141, ROR8 = 0x128;
__device__ __forceinline__ float x16_add(float x) { auto s = __builtin_amdgcn_permlane16_swap(__float_as_uint(x), __float_as_uint(x), false, false); return __uint_as_float(s[0]) + __uint_as_float(s[1]); }
__device__ __forceinline__ float x32_add(float x) { auto s = __builtin_amdgcn_permlane32_swap(__float_as_uint(x), __float_as_uint(x), false, false); return __uint_as_float(s[0]) + __uint_as_float(s[1]); }
__device__ __forceinline__ v4f mfma(v4u a, v4u b, v4f c) {
  return __builtin_amdgcn_mfma_f32_16x16x32_bf16(__builtin_bit_cast(v8b, a), __builtin_bit_cast(v8b, b), c, 0, 0, 0);
}
#define BAR() asm volatile("s_waitcnt lgkmcnt(0)\n\ts_barrier" ::: "memory")
#define VMWAIT(n) asm volatile("s_waitcnt vmcnt(" #n ")" ::: "memory")

constexpr int R_WQA = 128, R_WOA = 160, R_WQB = 192, R_WOB = 224;
template <int BASE, u32 STRIDE>
__device__ __forceinline__ void bulk8(const char* base, u32 voff) {
  asm volatile("" : "+s"(base));
  const __amdgpu_buffer_rsrc_t r = __builtin_amdgcn_make_buffer_rsrc((void*)base, 0, 0x7fffffff, 0x00020000);
  asm volatile("s_nop 4\n\t"
               "buffer_load_dwordx4 v[%c10:%c11], %0, %1, 0 offen" NTW "\n\tbuffer_load_dwordx4 v[%c12:%c13], %0, %1, %3 offen" NTW "\n\t"
               "buffer_load_dwordx4 v[%c14:%c15], %0, %1, %4 offen" NTW "\n\tbuffer_load_dwordx4 v[%c16:%c17], %0, %1, %5 offen" NTW "\n\t"
               "buffer_load_dwordx4 v[%c18:%c19], %0, %1, %6 offen" NTW "\n\tbuffer_load_dwordx4 v[%c20:%c21], %0, %1, %7 offen" NTW "\n\t"
               "buffer_load_dwordx4 v[%c22:%c23], %0, %1, %8 offen" NTW "\n\tbuffer_load_dwordx4 v[%c24:%c25], %0, %1, %9 offen" NTW
               :: "v"(voff), "s"(r), "s"(0), "s"(STRIDE), "s"(2 * STRIDE), "s"(3 * STRIDE), "s"(4 * STRIDE), "s"(5 * STRIDE), "s"(6 * STRIDE), "s"(7 * STRIDE),
                  "i"(BASE), "i"(BASE + 3), "i"(BASE + 4), "i"(BASE + 7), "i"(BASE + 8), "i"(BASE + 11), "i"(BASE + 12), "i"(BASE + 15),
                  "i"(BASE + 16), "i"(BASE + 19), "i"(BASE + 20), "i"(BASE + 23), "i"(BASE + 24), "i"(BASE + 27), "i"(BASE + 28), "i"(BASE + 31)
               : "memory");
}
template <int B, int M, int R>
__device__ __forceinline__ void tp_pair(u32 slo, u32 shi) {
  if constexpr (M == R) {
    asm volatile("v_perm_b32 v100, v%c3, v%c2, %0\n\tv_perm_b32 v%c3, v%c3, v%c2, %1\n\tv_mov_b32 v%c2, v100"
                 :: "s"(slo), "s"(shi), "i"(B + 8 * M + R), "i"(B + 8 * M + 4 + R));
  } else {
    asm volatile("v_perm_b32 v100, v%c3, v%c2, %0\n\tv_perm_b32 v101, v%c3, v%c2, %1\n\t"
                 "v_perm_b32 v%c2, v%c5, v%c4, %0\n\tv_perm_b32 v%c3, v%c5, v%c4, %1\n\t"
                 "v_mov_b32 v%c4, v100\n\tv_mov_b32 v%c5, v101"
                 :: "s"(slo), "s"(shi), "i"(B + 8 * M + R), "i"(B + 8 * M + 4 + R), "i"(B + 8 * R + M), "i"(B + 8 * R + 4 + M));
  }
}
template <int B>
__device__ __forceinline__ void transpose8() {
  const u32 slo = 0x05040100u, shi = 0x07060302u;
  tp_pair<B, 0, 0>(slo, shi); tp_pair<B, 1, 1>(slo, shi); tp_pair<B, 2, 2>(slo, shi); tp_pair<B, 3, 3>(slo, shi);
  tp_pair<B, 0, 1>(slo, shi); tp_pair<B, 0, 2>(slo, shi); tp_pair<B, 0, 3>(slo, shi);
  tp_pair<B, 1, 2>(slo, shi); tp_pair<B, 1, 3>(slo, shi); tp_pair<B, 2, 3>(slo, shi);
  asm volatile("s_nop 1");
}
#define SNOP
#define MF(d, b) "v_mfma_f32_16x16x32_bf16 v[" #d ":" #d "+3], v[48:51], v[%c[wb]+" #b ":%c[wb]+" #b "+3], 0\n\t"
#define FOLD_TAIL \
  "v_permlane32_swap_b32 v52, v68\n\t" \
  "v_permlane32_swap_b32 v56, v72\n\t" \
  "v_permlane32_swap_b32 v60, v76\n\t" \
  "v_permlane32_swap_b32 v64, v80\n\t" \
  "v_cndmask_b32_e64 v52, v52, v68, %[pm]\n\t" \
  "v_cndmask_b32_e64 v60, v60, v76, %[pm]\n\t" \
  "v_cndmask_b32_e64 v56, v56, v72, %[pm]\n\t" \
  "v_cndmask_b32_e64 v64, v64, v80, %[pm]\n\t" \
  "v_permlane16_swap_b32 v52, v60\n\t" \
  "s_nop 0\n\t" \
  "v_permlane16_swap_b32 v56, v64\n\t" \
  "v_add_f32 v52, v52, v60\n\t" \
  "v_add_f32 v56, v56, v64\n\t" \
  "s_nop 0\n\t" \
  "v_add_f32_dpp v52, v52, v52 row_ror:8 row_mask:0xf bank_mask:0xf\n\t" \
  "v_add_f32_dpp v56, v56, v56 row_ror:8 row_mask:0xf bank_mask:0xf\n\t" \
  "v_cndmask_b32_e64 v52, v52, v56, %[pm]\n\t"

__device__ __forceinline__ void poll_issue(u32 voff, const char* ws) {
  asm volatile("s_nop 4\n\tglobal_load_dwordx4 v[48:51], %0, %1 sc1" :: "v"(voff), "s"(ws) : "memory");
}
template <int WB, int POLL, int LOFF>
__device__ __forceinline__ void seg_p1(u32 voff, const char* ws, u32 ldsw, u32& spins) {
  if constexpr (POLL != 0) {
    asm volatile(
      SNOP
      ".if %c[pre]\n\ts_branch Lc%=\n\t.endif\n\t"
      "Lp%=:\n\t"
      "global_load_dwordx4 v[48:51], %[voff], %[ws] sc1\n\t"
      "s_add_u32 %[sp], %[sp], 1\n\t"
      "s_waitcnt vmcnt(0)\n\t"
      "Lc%=:\n\t"
      MF(52, 0) "v_max3_u32 v84, v48, v49, v50\n\t"
      MF(56, 4) "v_max_u32 v84, v84, v51\n\t"
      MF(60, 8) "v_cmp_eq_u32 vcc, -1, v84\n\t"
      MF(64, 12)
      "s_cbranch_vccz Lk%=\n\t"
      "s_cmp_lt_u32 %[sp], %[cap]\n\t"
      "s_cbranch_scc1 Lp%=\n\t"
      "Lk%=:\n\t"
      : [sp] "+s"(spins) : [voff] "v"(voff), [ws] "s"(ws), [wb] "i"(WB), [cap] "i"(SPIN_CAP), [pre] "i"(POLL == 2) : "memory", "vcc", "scc");
  } else {
    asm volatile("s_nop 1\n\t" MF(52, 0) "s_nop 0\n\t" MF(56, 4) "s_nop 0\n\t" MF(60, 8) "s_nop 0\n\t" MF(64, 12) "s_nop 0\n\t" :: [wb] "i"(WB));
  }
  asm volatile(
    MF(68, 16) "v_pk_add_f32 v[52:53], v[52:53], v[54:55]\n\t"
    MF(72, 20) "v_pk_add_f32 v[56:57], v[56:57], v[58:59]\n\t"
    MF(76, 24) "v_pk_add_f32 v[60:61], v[60:61], v[62:63]\n\t"
    MF(80, 28) "v_pk_add_f32 v[64:65], v[64:65], v[66:67]\n\t"
    "v_add_f32 v52, v52, v53\n\t"
    "v_pk_add_f32 v[68:69], v[68:69], v[70:71]\n\t"
    "v_add_f32 v56, v56, v57\n\t"
    "v_pk_add_f32 v[72:73], v[72:73], v[74:75]\n\t"
    "v_add_f32 v60, v60, v61\n\t"
    "v_pk_add_f32 v[76:77], v[76:77], v[78:79]\n\t"
    "v_add_f32 v64, v64, v65\n\t"
    "v_pk_add_f32 v[80:81], v[80:81], v[82:83]\n\t"
    "v_add_f32 v68, v68, v69\n\t"
    "v_add_f32 v72, v72, v73\n\t"
    "v_add_f32 v76, v76, v77\n\t"
    "v_add_f32 v80, v80, v81\n\t"
    "s_nop 0\n\t"
    FOLD_TAIL
    "ds_write_b32 %[lw], v52 offset:%c[lo]\n\t"
    :: [wb] "i"(WB), [pm] "s"(PMASK), [lw] "v"(ldsw), [lo] "i"(LOFF) : "memory");
}
__device__ __forceinline__ void seg_p3_issue(u32 voff, u32 voff2, const char* ws, const char* ws2) {
  asm volatile("s_nop 4\n\tglobal_load_dwordx4 v[48:51], %0, %2 sc1\n\tglobal_load_dword v84, %1, %3 sc1" :: "v"(voff), "v"(voff2), "s"(ws), "s"(ws2) : "memory");
}
template <int WB, int PRE>
__device__ __forceinline__ void seg_p3(u32 voff, u32 voff2, const char* ws, const char* ws2, u32 ldsw, u32& spins) {
  asm volatile(
    SNOP
    ".if %c[pre]\n\ts_branch Lc%=\n\t.endif\n\t"
    "Lp%=:\n\t"
    "global_load_dwordx4 v[48:51], %[voff], %[ws] sc1\n\t"
    "global_load_dword v84, %[voff2], %[ws2] sc1\n\t"
    "s_add_u32 %[sp], %[sp], 1\n\t"
    "s_waitcnt vmcnt(0)\n\t"
    "Lc%=:\n\t"
    MF(52, 0) "v_max3_u32 v85, v48, v49, v50\n\t"
    MF(56, 4) "v_max3_u32 v85, v85, v51, v84\n\t"
    MF(60, 8) "v_cmp_eq_u32 vcc, -1, v85\n\t"
    MF(64, 12)
    "s_cbranch_vccz Lk%=\n\t"
    "s_cmp_lt_u32 %[sp], %[cap]\n\t"
    "s_cbranch_scc1 Lp%=\n\t"
    "Lk%=:\n\t"
    "v_max_f32_dpp v85, v84, v84 quad_perm:[1,0,3,2] row_mask:0xf bank_mask:0xf\n\t"
    MF(68, 16)
    "s_nop 0\n\t"
    "v_max_f32_dpp v85, v85, v85 quad_perm:[2,3,0,1] row_mask:0xf bank_mask:0xf\n\t"
    MF(72, 20)
    "s_nop 0\n\t"
    "v_max_f32_dpp v85, v85, v85 row_half_mirror row_mask:0xf bank_mask:0xf\n\t"
    MF(76, 24)
    "v_sub_f32 v86, v84, v85\n\t"
    MF(80, 28)
    "v_exp_f32 v86, v86\n\t"
    "s_nop 1\n\t"
    "v_mov_b32_dpp v90, v86 row_newbcast:0 row_mask:0x5 bank_mask:0xf\n\t"
    "v_mov_b32_dpp v91, v86 row_newbcast:1 row_mask:0x5 bank_mask:0xf\n\t"
    "v_mov_b32_dpp v92, v86 row_newbcast:2 row_mask:0x5 bank_mask:0xf\n\t"
    "v_mov_b32_dpp v93, v86 row_newbcast:3 row_mask:0x5 bank_mask:0xf\n\t"
    "v_mov_b32_dpp v90, v86 row_newbcast:4 row_mask:0xa bank_mask:0xf\n\t"
    "v_mov_b32_dpp v91, v86 row_newbcast:5 row_mask:0xa bank_mask:0xf\n\t"
    "v_mov_b32_dpp v92, v86 row_newbcast:6 row_mask:0xa bank_mask:0xf\n\t"
    "v_mov_b32_dpp v93, v86 row_newbcast:7 row_mask:0xa bank_mask:0xf\n\t"
    "v_add_f32_dpp v87, v86, v86 quad_perm:[1,0,3,2] row_mask:0xf bank_mask:0xf\n\t"
    "v_pk_mul_f32 v[52:53], v[52:53], v[90:91]\n\t"
    "v_pk_mul_f32 v[56:57], v[56:57], v[90:91]\n\t"
    "v_add_f32_dpp v87, v87, v87 quad_perm:[2,3,0,1] row_mask:0xf bank_mask:0xf\n\t"
    "v_pk_mul_f32 v[60:61], v[60:61], v[90:91]\n\t"
    "v_pk_mul_f32 v[64:65], v[64:65], v[90:91]\n\t"
    "v_add_f32_dpp v87, v87, v87 row_half_mirror row_mask:0xf bank_mask:0xf\n\t"
    "v_pk_mul_f32 v[68:69], v[68:69], v[90:91]\n\t"
    "v_pk_mul_f32 v[72:73], v[72:73], v[90:91]\n\t"
    "v_rcp_f32 v87, v87\n\t"
    "v_pk_mul_f32 v[76:77], v[76:77], v[90:91]\n\t"
    "v_pk_mul_f32 v[80:81], v[80:81], v[90:91]\n\t"
    "v_pk_fma_f32 v[52:53], v[54:55], v[92:93], v[52:53]\n\t"
    "v_pk_fma_f32 v[68:69], v[70:71], v[92:93], v[68:69]\n\t"
    "v_pk_fma_f32 v[56:57], v[58:59], v[92:93], v[56:57]\n\t"
    "v_pk_fma_f32 v[72:73], v[74:75], v[92:93], v[72:73]\n\t"
    "v_pk_fma_f32 v[60:61], v[62:63], v[92:93], v[60:61]\n\t"
    "v_pk_fma_f32 v[76:77], v[78:79], v[92:93], v[76:77]\n\t"
    "v_pk_fma_f32 v[64:65], v[66:67], v[92:93], v[64:65]\n\t"
    "v_pk_fma_f32 v[80:81], v[82:83], v[92:93], v[80:81]\n\t"
    "v_add_f32 v52, v52, v53\n\t"
    "v_add_f32 v68, v68, v69\n\t"
    "v_add_f32 v56, v56, v57\n\t"
    "v_add_f32 v72, v72, v73\n\t"
    "v_add_f32 v60, v60, v61\n\t"
    "v_add_f32 v76, v76, v77\n\t"
    "v_add_f32 v64, v64, v65\n\t"
    "v_add_f32 v80, v80, v81\n\t"
    "s_nop 1\n\t"
    FOLD_TAIL
    "v_mul_f32 v52, v52, v87\n\t"
    "ds_write_b32 %[lw], v52 offset:%c[lo]\n\t"
    : [sp] "+s"(spins)
    : [voff] "v"(voff), [voff2] "v"(voff2), [ws] "s"(ws), [ws2] "s"(ws2), [wb] "i"(WB), [cap] "i"(SPIN_CAP), [pm] "s"(PMASK), [lw] "v"(ldsw), [lo] "i"(L_RED3 - L_RED), [pre] "i"(PRE) : "memory", "vcc", "scc");
}
#define PACK_STORE \
  "s_nop 1\n\t" \
  "v_mov_b32_dpp v53, v52 quad_perm:[1,0,3,2] row_mask:0xf bank_mask:0xf\n\t" \
  "v_cvt_pk_bf16_f32 v54, v52, v53\n\t" \
  "v_min_u32 v54, -2, v54\n\t" \
  "s_nop 1\n\t" \
  "v_mov_b32_dpp v55, v54 row_shl:2 row_mask:0xf bank_mask:0xf\n\t" \
  "v_mov_b32_dpp v56, v54 row_shl:4 row_mask:0xf bank_mask:0xf\n\t" \
  "v_mov_b32_dpp v57, v54 row_shl:6 row_mask:0xf bank_mask:0xf\n\t" \
  "s_mov_b64 exec, %[c8z]\n\t" \
  "global_store_dwordx4 %[so], v[54:57], %[ws] sc1\n\t" \
  "s_mov_b64 exec, -1\n\t" \
  "s_nop 1\n\t"
template <bool XS>
__device__ __forceinline__ void seg_final2(u32 ldsr, u32 xsr, u32 so, const char* ws) {
  constexpr u32 LO = XS ? L_RED3 - L_RED : 0;
  if constexpr (XS) {
    asm volatile(
      "ds_read_b128 v[52:55], %[lr] offset:%c[lo]\n\t"
      "ds_read_b128 v[56:59], %[lr] offset:%c[lo]+16\n\t"
      "ds_read_b32 v60, %[xr]\n\t"
      "s_waitcnt lgkmcnt(1)\n\t"
      "v_pk_add_f32 v[52:53], v[52:53], v[56:57]\n\t"
      "v_pk_add_f32 v[54:55], v[54:55], v[58:59]\n\t"
      "v_pk_add_f32 v[52:53], v[52:53], v[54:55]\n\t"
      "s_waitcnt lgkmcnt(0)\n\t"
      "v_add_f32 v52, v52, v53\n\t"
      "v_add_f32 v52, v52, v60\n\t"
      PACK_STORE
      :: [lr] "v"(ldsr), [xr] "v"(xsr), [so] "v"(so), [ws] "s"(ws), [c8z] "s"(C8Z), [lo] "i"(LO) : "memory");
  } else {
    asm volatile(
      "ds_read_b128 v[52:55], %[lr]\n\t"
      "ds_read_b128 v[56:59], %[lr] offset:16\n\t"
      "s_waitcnt lgkmcnt(0)\n\t"
      "v_pk_add_f32 v[52:53], v[52:53], v[56:57]\n\t"
      "v_pk_add_f32 v[54:55], v[54:55], v[58:59]\n\t"
      "v_pk_add_f32 v[52:53], v[52:53], v[54:55]\n\t"
      "v_add_f32 v52, v52, v53\n\t"
      PACK_STORE
      :: [lr] "v"(ldsr), [so] "v"(so), [ws] "s"(ws), [c8z] "s"(C8Z) : "memory");
  }
}
template <int RB>
__device__ __forceinline__ void qpoll_issue(u32 voff, const char* ws) {
  asm volatile("s_nop 4\n\tglobal_load_dwordx4 v[%c2:%c2+3], %0, %1 sc1" :: "v"(voff), "s"(ws), "i"(RB) : "memory");
}
template <int LOFF, int RB, int PRE>
__device__ __forceinline__ void seg_qpoll(u32 voff, const char* ws, u32 ldsw, u32& spins) {
  asm volatile(
    SNOP
    ".if %c[pre]\n\ts_branch Lc%=\n\t.endif\n\t"
    "Lp%=:\n\t"
    "global_load_dwordx4 v[%c[rb]:%c[rb]+3], %[voff], %[ws] sc1\n\t"
    "s_add_u32 %[sp], %[sp], 1\n\t"
    "s_waitcnt vmcnt(0)\n\t"
    "Lc%=:\n\t"
    "v_max3_u32 v70, v%c[rb], v[%c[rb]+1], v[%c[rb]+2]\n\t"
    "v_max_u32 v70, v70, v[%c[rb]+3]\n\t"
    "v_cmp_eq_u32 vcc, -1, v70\n\t"
    "ds_write_b128 %[lw], v[%c[rb]:%c[rb]+3] offset:%c[lo]\n\t"
    "s_cbranch_vccz Lk%=\n\t"
    "s_cmp_lt_u32 %[sp], %[cap]\n\t"
    "s_cbranch_scc1 Lp%=\n\t"
    "Lk%=:\n\t"
    : [sp] "+s"(spins) : [voff] "v"(voff), [ws] "s"(ws), [lw] "v"(ldsw), [cap] "i"(SPIN_CAP), [lo] "i"(LOFF), [rb] "i"(RB), [pre] "i"(PRE) : "memory", "vcc", "scc");
}
template <int TO>
__device__ __forceinline__ void seg_kpre(u32 ka0, u32 ka1) {
  asm volatile(
    "ds_read_b128 v[48:51], %0 offset:%c2\n\tds_read_b128 v[64:67], %1 offset:%c2\n\t"
    "ds_read_b128 v[52:55], %0 offset:%c2+512\n\tds_read_b128 v[68:71], %1 offset:%c2+512\n\t"
    "ds_read_b128 v[56:59], %0 offset:%c2+1024\n\tds_read_b128 v[72:75], %1 offset:%c2+1024\n\t"
    "ds_read_b128 v[60:63], %0 offset:%c2+1536\n\tds_read_b128 v[76:79], %1 offset:%c2+1536\n\t"
    "v_mov_b32 v96, 0\n\tv_mov_b32 v97, 0\n\tv_mov_b32 v98, 0\n\tv_mov_b32 v99, 0\n\t"
    "v_mov_b32 v100, 0\n\tv_mov_b32 v101, 0\n\tv_mov_b32 v102, 0\n\tv_mov_b32 v103, 0\n\t"
    "v_mov_b32 v104, 0\n\tv_mov_b32 v105, 0\n\tv_mov_b32 v106, 0\n\tv_mov_b32 v107, 0\n\t"
    "v_mov_b32 v108, 0\n\tv_mov_b32 v109, 0\n\tv_mov_b32 v110, 0\n\tv_mov_b32 v111, 0\n\t"
    :: "v"(ka0), "v"(ka1), "i"(TO) : "memory");
}
#define QK(s, k, q, c) "v_mfma_f32_16x16x32_bf16 v[" #s ":" #s "+3], v[" #k ":" #k "+3], v[" #q ":" #q "+3], " c "\n\t"
#define TR(d, a, off) "ds_read_b64_tr_b16 v[" #d ":" #d "+1], %[" #a "] offset:%c[to]+" #off "\n\t"
#define PV(r) "v_mfma_f32_16x16x32_bf16 v[" #r ":" #r "+3], v[124:127], v[" #r ":" #r "+3], 0\n\t"
#define SM(r) "v_fma_f32 v" #r ", v" #r ", v99, v121\n\tv_exp_f32 v" #r ", v" #r "\n\t"
#define PVA(acc, a, v, c) "v_mfma_f32_16x16x32_bf16 v[" #acc ":" #acc "+3], v[" #a ":" #a "+3], v[" #v ":" #v "+3], " c "\n\t"
#define PCP(d, s, ctl, bank) "v_mov_b32_dpp v" #d ", v" #s " " ctl " row_mask:0xf bank_mask:" bank "\n\t"
template <int TO>
__device__ __forceinline__ void seg_attn(u32 qa, u32 sta, u32 va0, u32 va1, u32 va2, u32 va3, u32 oa, u32 mla) {
  asm volatile(
    "ds_read_b128 v[80:83], %[qa]\n\t"
    "ds_read_b128 v[84:87], %[qa] offset:64\n\t"
    "ds_read_b128 v[88:91], %[qa] offset:128\n\t"
    "ds_read_b128 v[92:95], %[qa] offset:192\n\t"
    "ds_read_b64 v[120:121], %[sta]\n\t"
    "s_waitcnt lgkmcnt(4)\n\t"
    QK(112, 48, 80, "0") QK(116, 64, 80, "0")
    "s_waitcnt lgkmcnt(3)\n\t"
    QK(112, 52, 84, "v[112:115]") QK(116, 68, 84, "v[116:119]")
    "s_waitcnt lgkmcnt(2)\n\t"
    QK(112, 56, 88, "v[112:115]") QK(116, 72, 88, "v[116:119]")
    "s_waitcnt lgkmcnt(0)\n\t"
    QK(112, 60, 92, "v[112:115]")
    "v_add_f32_dpp v120, v120, v120 quad_perm:[1,0,3,2] row_mask:0xf bank_mask:0xf\n\t"
    "v_add_f32_dpp v121, v121, v121 quad_perm:[1,0,3,2] row_mask:0xf bank_mask:0xf\n\t"
    QK(116, 76, 92, "v[116:119]")
    TR(48, va0, 0)
    "v_add_f32_dpp v120, v120, v120 quad_perm:[2,3,0,1] row_mask:0xf bank_mask:0xf\n\t"
    "v_add_f32_dpp v121, v121, v121 quad_perm:[2,3,0,1] row_mask:0xf bank_mask:0xf\n\t"
    TR(50, va1, 0) TR(52, va2, 0)
    "v_add_f32_dpp v120, v120, v120 row_half_mirror row_mask:0xf bank_mask:0xf\n\t"
    "v_add_f32_dpp v121, v121, v121 row_half_mirror row_mask:0xf bank_mask:0xf\n\t"
    TR(54, va3, 0)
    "v_mul_f32 v122, 0xbb000000, v120\n\t"
    "v_mul_f32 v123, 0x3b000000, v121\n\t"
    "v_fma_f32 v123, -v122, v122, v123\n\t"
    "v_fmac_f32_dpp v112, v112, v122 row_shl:4 row_mask:0xf bank_mask:0xf bound_ctrl:1\n\t"
    "v_max_f32 v123, 0, v123\n\t"
    "v_fmac_f32_dpp v113, v113, v122 row_shl:4 row_mask:0xf bank_mask:0xf bound_ctrl:1\n\t"
    "v_add_f32 v123, 0x3727c5ac, v123\n\t"
    "v_fmac_f32_dpp v114, v114, v122 row_shl:4 row_mask:0xf bank_mask:0xf bound_ctrl:1\n\t"
    "v_rsq_f32 v123, v123\n\t"
    "v_fmac_f32_dpp v115, v115, v122 row_shl:4 row_mask:0xf bank_mask:0xf bound_ctrl:1\n\t"
    "v_fmac_f32_dpp v116, v116, v122 row_shl:4 row_mask:0xf bank_mask:0xf bound_ctrl:1\n\t"
    "v_max3_f32 v120, v112, v113, v114\n\t"
    "v_fmac_f32_dpp v117, v117, v122 row_shl:4 row_mask:0xf bank_mask:0xf bound_ctrl:1\n\t"
    "v_fmac_f32_dpp v118, v118, v122 row_shl:4 row_mask:0xf bank_mask:0xf bound_ctrl:1\n\t"
    "v_fmac_f32_dpp v119, v119, v122 row_shl:4 row_mask:0xf bank_mask:0xf bound_ctrl:1\n\t"
    "v_max3_f32 v120, v120, v115, v116\n\t"
    "v_mul_f32 v123, %[c2], v123\n\t"
    "v_max3_f32 v120, v120, v117, v118\n\t"
    "v_max_f32 v120, v120, v119\n\t"
    "v_mov_b32 v121, v120\n\t"
    TR(56, va0, 512) TR(58, va1, 512)
    "v_permlane16_swap_b32 v120, v121\n\t"
    "v_max_f32 v120, v120, v121\n\t"
    "v_mov_b32 v121, v120\n\t"
    TR(60, va2, 512) TR(62, va3, 512)
    "v_permlane32_swap_b32 v120, v121\n\t"
    "v_max_f32 v120, v120, v121\n\t"
    "v_mul_f32_e64 v121, -v120, v123\n\t"
    "v_mul_f32 v122, v120, v123\n\t"
    "v_fma_f32 v112, v112, v123, v121\n\t"
    "v_fma_f32 v113, v113, v123, v121\n\t"
    "v_exp_f32 v112, v112\n\t"
    TR(64, va0, 1024)
    "v_fma_f32 v114, v114, v123, v121\n\t"
    "v_exp_f32 v113, v113\n\t"
    TR(66, va1, 1024)
    "v_fma_f32 v115, v115, v123, v121\n\t"
    "v_exp_f32 v114, v114\n\t"
    TR(68, va2, 1024)
    "v_fma_f32 v116, v116, v123, v121\n\t"
    "v_exp_f32 v115, v115\n\t"
    TR(70, va3, 1024)
    "v_fma_f32 v117, v117, v123, v121\n\t"
    "v_exp_f32 v116, v116\n\t"
    "v_cvt_pk_bf16_f32 v124, v112, v113\n\t"
    "v_fma_f32 v118, v118, v123, v121\n\t"
    "v_exp_f32 v117, v117\n\t"
    TR(72, va0, 1536)
    "v_fma_f32 v119, v119, v123, v121\n\t"
    "v_exp_f32 v118, v118\n\t"
    "v_cvt_pk_bf16_f32 v125, v114, v115\n\t"
    "v_exp_f32 v119, v119\n\t"
    TR(74, va1, 1536) TR(76, va2, 1536) TR(78, va3, 1536)
    "v_cvt_pk_bf16_f32 v126, v116, v117\n\t"
    "v_cvt_pk_bf16_f32 v127, v118, v119\n\t"
    PCP(96, 124, "quad_perm:[0,1,2,3]", "0x1") PCP(100, 124, "row_shr:4", "0x2") PCP(104, 124, "quad_perm:[0,1,2,3]", "0x4") PCP(108, 124, "row_shr:4", "0x8")
    PCP(97, 125, "quad_perm:[0,1,2,3]", "0x1") PCP(101, 125, "row_shr:4", "0x2") PCP(105, 125, "quad_perm:[0,1,2,3]", "0x4") PCP(109, 125, "row_shr:4", "0x8")
    PCP(98, 126, "quad_perm:[0,1,2,3]", "0x1") PCP(102, 126, "row_shr:4", "0x2") PCP(106, 126, "quad_perm:[0,1,2,3]", "0x4") PCP(110, 126, "row_shr:4", "0x8")
    PCP(99, 127, "quad_perm:[0,1,2,3]", "0x1") PCP(103, 127, "row_shr:4", "0x2") PCP(107, 127, "quad_perm:[0,1,2,3]", "0x4") PCP(111, 127, "row_shr:4", "0x8")
    "v_mov_b32 v84, %[ones]\n\tv_mov_b32 v85, %[ones]\n\tv_mov_b32 v86, %[ones]\n\tv_mov_b32 v87, %[ones]\n\t"
    "s_waitcnt lgkmcnt(0)\n\t"
    PVA(112, 96, 48, "0") PVA(116, 96, 64, "0")
    PVA(112, 100, 52, "v[112:115]") PVA(116, 100, 68, "v[116:119]")
    PVA(112, 104, 56, "v[112:115]") PVA(116, 104, 72, "v[116:119]")
    PVA(112, 108, 60, "v[112:115]") PVA(116, 108, 76, "v[116:119]")
    "v_mfma_f32_16x16x32_bf16 v[80:83], v[84:87], v[124:127], 0\n\t"
    "s_nop 6\n\t"
    "ds_write_b128 %[oa], v[112:115]\n\t"
    "ds_write_b128 %[oa], v[116:119] offset:1024\n\t"
    "s_mov_b64 exec, 0xf\n\t"
    "ds_write2_b32 %[mla], v122, v80 offset0:0 offset1:16\n\t"
    "s_mov_b64 exec, -1\n\t"
    :: [qa] "v"(qa), [sta] "v"(sta), [va0] "v"(va0), [va1] "v"(va1), [va2] "v"(va2), [va3] "v"(va3), [oa] "v"(oa), [mla] "v"(mla),
       [c2] "s"(C2), [ones] "s"(ONES), [to] "i"(TO) : "memory");
}
__device__ __forceinline__ void seg_merge(u32 mla, u32 oa, u32 so, const char* ws) {
  asm volatile(
    "ds_read_b128 v[48:51], %[mla]\n\t"
    "ds_read_b128 v[60:63], %[mla] offset:64\n\t"
    "ds_read2st64_b32 v[56:57], %[oa] offset0:0 offset1:8\n\t"
    "ds_read2st64_b32 v[58:59], %[oa] offset0:16 offset1:24\n\t"
    "s_waitcnt lgkmcnt(3)\n\t"
    "v_max3_f32 v52, v48, v49, v50\n\t"
    "v_max_f32 v52, v52, v51\n\t"
    "v_sub_f32 v48, v48, v52\n\t"
    "v_sub_f32 v49, v49, v52\n\t"
    "v_exp_f32 v48, v48\n\t"
    "v_sub_f32 v50, v50, v52\n\t"
    "v_exp_f32 v49, v49\n\t"
    "v_sub_f32 v51, v51, v52\n\t"
    "v_exp_f32 v50, v50\n\t"
    "v_exp_f32 v51, v51\n\t"
    "s_waitcnt lgkmcnt(0)\n\t"
    "v_pk_mul_f32 v[60:61], v[60:61], v[48:49]\n\t"
    "v_pk_mul_f32 v[56:57], v[56:57], v[48:49]\n\t"
    "v_pk_fma_f32 v[60:61], v[62:63], v[50:51], v[60:61]\n\t"
    "v_pk_fma_f32 v[56:57], v[58:59], v[50:51], v[56:57]\n\t"
    "v_add_f32 v60, v60, v61\n\t"
    "v_add_f32 v56, v56, v57\n\t"
    "v_rcp_f32 v60, v60\n\t"
    "s_nop 0\n\t"
    "v_mul_f32 v52, v56, v60\n\t"
    PACK_STORE
    :: [mla] "v"(mla), [oa] "v"(oa), [so] "v"(so), [ws] "s"(ws), [c8z] "s"(C8Z) : "memory");
}

__device__ __forceinline__ void dma16(u32 voff, const char* sbase, u32 lds_dst) {
  u32 keep;
  asm volatile(SNOP "s_mov_b32 %0, m0\n\ts_mov_b32 m0, %3\n\ts_nop 0\n\tglobal_load_lds_dwordx4 %1, %2" NTKV "\n\ts_mov_b32 m0, %0"
               : "=&s"(keep) : "v"(voff), "s"(sbase), "s"(lds_dst) : "memory");
}
__device__ __forceinline__ void kv_load(const char* base, int w, int lane, u32 ldsb) {
  asm volatile("" : "+s"(base));
  const int r8 = (lane & 31) >> 2;
#pragma unroll
  for (int i = 0; i < 4; ++i) {
    const u32 ch = 8 * (i & 1) + 4 * (lane >> 5) + ((lane & 3) ^ ((2 * (i >> 1) + (r8 >> 2)) & 3));
    dma16(r8 * 256 + ch * 16, base + (size_t)(16 * w + 8 * (i >> 1)) * 256, ldsb + w * 4096 + i * 1024);
  }
}
__device__ __forceinline__ bool has_sent(v4u x) {
  const u32 m = __builtin_elementwise_max(__builtin_elementwise_max(x[0], x[1]), __builtin_elementwise_max(x[2], x[3]));
  return __builtin_amdgcn_ballot_w64(m == SENT) != 0ull;
}
__device__ __forceinline__ v4u poll128(const char* base, u32 voff) {
  v4u x;
  for (u32 spins = 0;; ++spins) {
    asm volatile("s_nop 4\n\tglobal_load_dwordx4 %0, %1, %2 sc1\n\ts_waitcnt vmcnt(0)" : "=&v"(x) : "v"(voff), "s"(base) : "memory");
    if (!has_sent(x) || spins > SPIN_CAP) break;
  }
  return x;
}
__device__ __forceinline__ v4u pack8(float v) {
  const u32 x = safe(pk(v, dpp<XOR1>(v)));
  return v4u{x, dppu<0x102>(x), dppu<0x104>(x), dppu<0x106>(x)};
}
__device__ __forceinline__ void sum_partials(v4u x, float (&v)[8]) {
#pragma unroll
  for (int m = 0; m < 4; ++m) { v[2 * m] = bflo(x[m]); v[2 * m + 1] = bfhi(x[m]); }
#pragma unroll
  for (int j = 0; j < 8; ++j) v[j] += dpp<XOR1>(v[j]);
#pragma unroll
  for (int j = 0; j < 8; ++j) v[j] += dpp<XOR2>(v[j]);
#pragma unroll
  for (int j = 0; j < 8; ++j) v[j] += dpp<HMIR>(v[j]);
}
__device__ __forceinline__ void x_stats(v4u xa, char* lds, int w, int lane) {
  const bool p = (lane >> 3) & 1; const int kq = lane >> 4;
  const v4f gm = mfma(xa, xa, v4f{0.f, 0.f, 0.f, 0.f});
  const v4f sm = mfma(xa, v4u{ONES, ONES, ONES, ONES}, v4f{0.f, 0.f, 0.f, 0.f});
  float s2 = (gm[0] + gm[1]) + (gm[2] + gm[3]);
  s2 = (p == (bool)(kq >> 1)) ? s2 : 0.f;
  s2 += dpp<XOR1>(s2); s2 += dpp<XOR2>(s2); s2 += dpp<HMIR>(s2); s2 += dpp<ROR8>(s2);
  s2 = x32_add(x16_add(s2));
  float s1 = (sm[0] + sm[1]) + (sm[2] + sm[3]);
  s1 = x32_add(x16_add(s1));
  if (lane == 0) *(v2f*)(lds + L_ST + w * 8) = v2f{s1, s2};
}
}

#define ST(i) do {} while (0)
#define STFLUSH(sv, base) do {} while (0)
#define SGPR_ATTR

__device__ __forceinline__ u32 lb_st() { return L_ST; }
#define OPAQUE_LANE() int lane = lane0; asm volatile("" : "+v"(lane)); const int c8 = lane & 7, kq = lane >> 4; const bool p = (lane >> 3) & 1; (void)c8; (void)p; (void)kq
#define OPAQUE_S(x) asm volatile("" : "+s"(x))

constexpr int FW = 4;

struct Addr {
  u32 vB, vA, vL, lw, so;
  u32 mr, orr;
  u32 r0, r1, r2, r3, r4, r5, r6, r7, r8;
};
#define PIN(x) asm volatile("" : "+v"(x))

template <int TYPE>
__device__ __forceinline__ void layer(const int rep, char* ws, char* lds, int b, int g, int s, int w, const int lane0, v4u xraw, Addr& A, u32 qaT,
                                      const char* kB, const char* vB, const char* wqB, const char* cwo, const u64 flag, int& stampv, u32 (&cnt)[3]) {
  constexpr int RQ = TYPE ? R_WQB : R_WQA, RO = TYPE ? R_WOB : R_WOA;
  const int L = rep * 2 + TYPE;
  OPAQUE_S(b); OPAQUE_S(g); OPAQUE_S(s); OPAQUE_S(w);
  const u32 grp = (u32)(b * 8 + g), cu = grp * 8 + s;
#define LD_WQ(R, ptr) do { OPAQUE_LANE(); const char* p_ = ptr; OPAQUE_S(p_); bulk8<R, 8192>(p_ + (size_t)(64 * w) * 8192 + (512 * g + 64 * s) * 2, (u32)(lane >> 3) * 65536 + c8 * 16); } while (0)
#define LD_WO(R, ptr) do { OPAQUE_LANE(); const char* p_ = ptr; OPAQUE_S(p_); bulk8<R, 1024>(p_ + (size_t)(512 * g + 64 * w) * 1024 + 128 * s, (u32)(lane >> 3) * 8192 + c8 * 16); } while (0)
#define RAWBAR() asm volatile("s_barrier" ::: "memory")
#define STAGGER() do { if (STG) { const int r_ = (w - 2 * b) & 7; for (int i_ = 0; i_ < r_; ++i_) __builtin_amdgcn_s_sleep(STG); } } while (0)
  const char* wsL = ws + L * BOXL;
  const char* box = ws + WS_BOX + cu * BOX + L * BOXL;
  {
    u32 sp = 0;
    if (L == 0) {
      VMWAIT(8);
      if (w >= 4) ST(7);
      transpose8<RQ>();
      const v4u xa = (lane0 & 7) == 0 ? xraw : v4u{0u, 0u, 0u, 0u};
      asm volatile("v_mov_b32 v48, %0\n\tv_mov_b32 v49, %1\n\tv_mov_b32 v50, %2\n\tv_mov_b32 v51, %3" :: "v"(xa[0]), "v"(xa[1]), "v"(xa[2]), "v"(xa[3]));
      seg_p1<RQ, 0, 0>(0, ws, A.lw, sp);
    } else {
      const char* wsP = wsL - BOXL;
      if (rep == 0) {
        VMWAIT(0);
        if (w >= 4) ST(7);
        transpose8<RQ>();
        if (DLY_B0) __builtin_amdgcn_s_sleep(DLY_B0);
        ST(0);
        poll_issue(A.vB, wsP);
        RAWBAR();
        { OPAQUE_LANE(); kv_load(kB, w, lane, L_K + 32768); kv_load(vB, w, lane, L_V + 32768); }
        VMWAIT(8);
        seg_p1<RQ, 2, 0>(A.vB, wsP, A.lw, sp);
      } else {
        if (DLY_B) __builtin_amdgcn_s_sleep(DLY_B);
        ST(0);
        seg_p1<RQ, 1, 0>(A.vB, wsP, A.lw, sp);
      }
      cnt[0] += sp;
    }
    ST(2);
    v4u xa;
    asm volatile("v_mov_b32 %0, v48\n\tv_mov_b32 %1, v49\n\tv_mov_b32 %2, v50\n\tv_mov_b32 %3, v51" : "=v"(xa[0]), "=v"(xa[1]), "=v"(xa[2]), "=v"(xa[3]));
    if (rep == 0) {
      asm volatile("v_mov_b32 v48, %0\n\tv_mov_b32 v49, %0\n\tv_mov_b32 v50, %0\n\tv_mov_b32 v51, %0" :: "s"(ONES));
      seg_p1<RQ, 0, L_REDC - L_RED>(0, ws, A.lw, sp);
    }
    BAR();
    ST(3);
    if (w == FW) {
      seg_final2<false>(A.r2, 0, A.so, box + BOX_Q);
      ST(4);
      OPAQUE_LANE();
      if (rep == 0) {
        const __amdgpu_buffer_rsrc_t rs = __builtin_amdgcn_make_buffer_rsrc(ws, 0, WS_END, 0x00020000);
        const v4f r0 = *(const v4f*)(lds + L_REDC + lane * 32), r1 = *(const v4f*)(lds + L_REDC + lane * 32 + 16);
        const float cs = 0.125f * (((r0[0] + r0[1]) + (r0[2] + r0[3])) + ((r1[0] + r1[1]) + (r1[2] + r1[3])));
        const v4u pc = pack8(cs);
        if (c8 == 0) __builtin_amdgcn_raw_buffer_store_b128(pc, rs, WS_BOX + cu * BOX + BOX_CS + TYPE * 128 + lane * 2, 0, 16);
      }
      x_stats(xa, lds, w, lane);
      if (L == 0) {
        for (u32 spins = 0;; ++spins) {
          const u64 f = __hip_atomic_load((GAS u64*)(ws + WS_RDY + b * 512 + lane * 8), __ATOMIC_RELAXED, __HIP_MEMORY_SCOPE_AGENT);
          if (__all(f == flag) || spins > SPIN_CAP) break;
        }
        if (g == 0 && s == 0 && lane == 0) __hip_atomic_store((GAS u32*)(ws + WS_EPOCH + b * 256), (u32)flag, __ATOMIC_RELAXED, __HIP_MEMORY_SCOPE_AGENT);
      } else {
        if (DLY_C) __builtin_amdgcn_s_sleep(DLY_C);
      }
      u32 sq = 0;
      if (rep == 0) {
        const char* wsC = ws + (BOX_CS + TYPE * 128 - BOX_Q);
        VMWAIT(0);
        qpoll_issue<52>(A.r0, wsL);
        qpoll_issue<60>(A.r0, wsC);
        RAWBAR();
        VMWAIT(0);
        seg_qpoll<0, 52, 1>(A.r0, wsL, A.r1, sq);
        seg_qpoll<L_CSI + TYPE * 1088 - L_Q, 60, 1>(A.r0, wsC, A.r1, sq);
        LD_WO(RO, cwo);
      } else {
        seg_qpoll<0, 52, 0>(A.r0, wsL, A.r1, sq);
      }
      cnt[1] += sq;
      ST(5);
    } else {
      OPAQUE_LANE();
      x_stats(xa, lds, w, lane);
    }
    if (g == 0 && w == s) {
      OPAQUE_LANE();
      float v[8];
      sum_partials(xa, v);
      if (c8 == 0) {
        float* xs = (float*)(lds + L_XS) + 16 * kq + (p ? 8 : 0);
        *(v4f*)xs = v4f{v[0], v[1], v[2], v[3]};
        *(v4f*)(xs + 4) = v4f{v[4], v[5], v[6], v[7]};
      }
    }
    if (rep == 0) {
      if (w != FW) { RAWBAR(); STAGGER(); LD_WO(RO, cwo); VMWAIT(8); }
      if (w >= 4) ST(8);
      BAR();
    }
  }
  if (w < 4) {
    seg_kpre<TYPE * 32768>(A.r0, A.r1);
    BAR();
    ST(7);
    seg_attn<TYPE * 32768>(qaT, A.r6, A.r2, A.r3, A.r4, A.r5, A.r7, A.r8);
    ST(8);
  } else {
    BAR();
  }
  {
    BAR();
    seg_merge(A.mr, A.orr, A.so, box + w * 128);
    if (w == 7) {
      OPAQUE_LANE();
      const __amdgpu_buffer_rsrc_t rs = __builtin_amdgcn_make_buffer_rsrc(ws, 0, WS_END, 0x00020000);
      const int hh = (lane >> 2) & 3, aa = lane & 3;
      const float m1 = ((const float*)(lds + L_ML))[hh * 4 + aa], l1 = ((const float*)(lds + L_ML))[16 + hh * 4 + aa];
      float M1 = __builtin_fmaxf(m1, dpp<XOR1>(m1)); M1 = __builtin_fmaxf(M1, dpp<XOR2>(M1));
      float L1 = __builtin_amdgcn_exp2f(m1 - M1) * l1; L1 += dpp<XOR1>(L1); L1 += dpp<XOR2>(L1);
      const u32 e = safe(__float_as_uint(M1 + __builtin_amdgcn_logf(L1)));
      const v4u ev = {dppu<0x150>(e), dppu<0x154>(e), dppu<0x158>(e), dppu<0x15C>(e)};
      if (lane < 8) __builtin_amdgcn_raw_buffer_store_b128(ev, rs, WS_LSR + ((L * 32 + grp) * 8 + lane) * 128 + s * 16, 0, 16);
    }
    ST(9);
  }
  {
    const char* wsS = ws + L * 32768;
    u32 sp = 0;
    if (rep == 0) {
      if (TYPE == 0) {
        VMWAIT(0);
        transpose8<RO>();
        if (DLY_A0) __builtin_amdgcn_s_sleep(DLY_A0);
        ST(1);
        seg_p3_issue(A.vA, A.vL, wsL, wsS);
        RAWBAR();
        STAGGER();
        LD_WQ(R_WQB, wqB);
        VMWAIT(8);
        seg_p3<RO, 1>(A.vA, A.vL, wsL, wsS, A.lw, sp);
      } else {
        VMWAIT(0);
        transpose8<RO>();
        ST(1);
        seg_p3<RO, 0>(A.vA, A.vL, wsL, wsS, A.lw, sp);
      }
    } else {
      if (DLY_A) __builtin_amdgcn_s_sleep(DLY_A);
      ST(1);
      seg_p3<RO, 0>(A.vA, A.vL, wsL, wsS, A.lw, sp);
    }
    cnt[2] += sp;
    ST(10);
    BAR();
    if (w == FW) seg_final2<true>(A.r2, A.r3, A.so, box + BOX_X);
    ST(11);
  }
  STFLUSH(stampv, L * 12);
}

__global__ void __launch_bounds__(512, 2) __attribute__((amdgpu_num_vgpr(24) SGPR_ATTR))
fused_kernel(const char* __restrict__ xin, const char* kA, const char* vA, const char* wqA, const char* woA,
             const char* kB, const char* vB, const char* wqB, const char* woB, unsigned short* out, char* ws) {
  extern __shared__ __attribute__((aligned(16))) char lds[];
  const int id = blockIdx.x, g = id & 7, b = id >> 6, s = (id >> 3) & 7;
  const int tid = threadIdx.x, lane = tid & 63, w = __builtin_amdgcn_readfirstlane(tid >> 6);
  const int c8 = lane & 7, kq = lane >> 4;
  const bool p = (lane >> 3) & 1;
  int stampv = 0;
  { const unsigned long long t0_ = __builtin_amdgcn_s_memrealtime(); while (__builtin_amdgcn_s_memrealtime() - t0_ < 0ull) __builtin_amdgcn_s_sleep(4); }
  const __amdgpu_buffer_rsrc_t rs = __builtin_amdgcn_make_buffer_rsrc(ws, 0, WS_END, 0x00020000);
  const u32 cu = (u32)((b * 8 + g) * 8 + s);
  asm volatile("" ::: "v127", "v255");
  u32 ep; v4u xraw;
  asm volatile("s_nop 4\n\tglobal_load_dword v56, %0, %1 sc1\n\tglobal_load_dwordx4 v[52:55], %2, %3"
               :: "v"(WS_EPOCH + b * 256), "s"(ws), "v"((u32)((b * 512 + 64 * w + 16 * kq + (p ? 8 : 0)) * 2)), "s"(xin) : "memory");
  if (w == 5) {
    const v4u sv = {SENT, SENT, SENT, SENT};
#pragma unroll
    for (int i = 0; i < 5; ++i) __builtin_amdgcn_raw_buffer_store_b128(sv, rs, WS_BOX + cu * BOX + i * 1024 + lane * 16, 0, 16);
    const u32 o6 = lane < 16 ? WS_BOX + cu * BOX + 5120 + lane * 16
                             : WS_LSR + (u32)(((((lane - 16) >> 3) * 32 + b * 8 + g) * 8 + (lane & 7)) * 128 + s * 16);
    if (lane < 48) __builtin_amdgcn_raw_buffer_store_b128(sv, rs, o6, 0, 16);
  }
  if (w == 6) ((float*)(lds + L_XS))[lane] = 0.f;
  const size_t kvoff = ((size_t)(b * 8 + g) * SK + (SK - WIN) + s * 128) * 256;
  const size_t qoff = (size_t)(64 * w) * 8192 + (512 * g + 64 * s) * 2;
  if (STG) { const int r_ = (w - 2 * b) & 7; for (int i_ = 0; i_ < r_; ++i_) __builtin_amdgcn_s_sleep(STG); }
  bulk8<R_WQA, 8192>(wqA + qoff, (u32)(lane >> 3) * 65536 + c8 * 16);
  VMWAIT(8);
  asm volatile("v_mov_b32 %0, v52\n\tv_mov_b32 %1, v53\n\tv_mov_b32 %2, v54\n\tv_mov_b32 %3, v55\n\tv_mov_b32 %4, v56"
               : "=v"(xraw[0]), "=v"(xraw[1]), "=v"(xraw[2]), "=v"(xraw[3]), "=v"(ep));
  ST(6);
  const u32 tag = (u32)__builtin_amdgcn_readfirstlane((int)ep) + 1u;
  const u64 flag = (u64)tag | ((u64)(~tag) << 32);
  if (w == 5 && lane == 0) __hip_atomic_store((GAS u64*)(ws + WS_RDY + b * 512 + (s * 8 + g) * 8), flag, __ATOMIC_RELAXED, __HIP_MEMORY_SCOPE_AGENT);
  asm volatile("s_barrier" ::: "memory");
  kv_load(kA + kvoff, w, lane, L_K);
  kv_load(vA + kvoff, w, lane, L_V);
  Addr A; u32 qa0, qa1;
  {
    const u32 grp = (u32)(b * 8 + g);
    const u32 oct = kq * 32 + (p ? 16 : 0);
    A.vB = WS_BOX + (u32)((b * 8 + c8) * 8 + w) * BOX + BOX_X + oct;
    A.vA = WS_BOX + (grp * 8 + c8) * BOX + w * 128 + oct;
    A.vL = WS_LSR + (grp * 8 + s) * 128 + c8 * 16 + (w >> 1) * 4;
    A.lw = L_RED + (8 * c8 + (lane >> 3)) * 32 + w * 4;
    A.so = lane * 2;
    A.mr = L_ML + (w >> 1) * 16;
    A.orr = L_OM + (w & 1) * 1024 + lane * 16 + (w >> 1) * 4;
    const int r16 = lane & 15, q4 = r16 >> 2, p2 = lane & 3;
    qa0 = L_Q + (lane & 3) * 272 + kq * 16;
    qa1 = qa0;
    if (lane & 4) { qa0 += L_CSI - L_Q; qa1 += L_CSI + 1088 - L_Q; }
    if (w < 4) {
      const u32 kb = L_K + 2048 * (4 * w + (r16 >> 2)) + 64 * (r16 & 3);
      A.r0 = kb + 16 * (kq ^ ((2 * (r16 >> 2)) & 3));
      A.r1 = kb + 256 + 16 * (kq ^ ((2 * (r16 >> 2) + 1) & 3));
      const u32 vb = L_V + 2048 * (4 * w + kq) + 64 * q4 + 8 * (p2 & 1);
      A.r2 = vb + 16 * (((p2 >> 1)) ^ ((2 * kq) & 3));
      A.r3 = vb + 256 + 16 * (((p2 >> 1)) ^ ((2 * kq + 1) & 3));
      A.r4 = vb + 16 * ((2 + (p2 >> 1)) ^ ((2 * kq) & 3));
      A.r5 = vb + 256 + 16 * ((2 + (p2 >> 1)) ^ ((2 * kq + 1) & 3));
      A.r7 = L_OM + w * 2048 + lane * 16;
      A.r8 = L_ML + (lane * 4 + w) * 4;
    } else {
      A.r0 = WS_BOX + (grp * 8 + (lane >> 3)) * BOX + BOX_Q + c8 * 16;
      A.r1 = L_Q + kq * 272 + (lane & 15) * 16;
      A.r2 = L_RED + lane * 32;
      A.r3 = L_XS + lane * 4;
      A.r4 = A.r5 = A.r7 = A.r8 = 0;
    }
    A.r6 = L_ST + c8 * 8;
    PIN(A.r6);
    PIN(A.vB); PIN(A.vA); PIN(A.vL); PIN(A.lw); PIN(A.so); PIN(A.mr); PIN(A.orr); PIN(A.r0); PIN(A.r1); PIN(A.r2); PIN(A.r3); PIN(A.r4); PIN(A.r5); PIN(A.r7); PIN(A.r8);
    PIN(qa0); PIN(qa1);
  }
  u32 cnt[3] = {0, 0, 0};
  for (int rep = 0; rep < 2; ++rep) {
    layer<0>(rep, ws, lds, b, g, s, w, lane, xraw, A, qa0, kB + kvoff, vB + kvoff, wqB, woA, flag, stampv, cnt);
    layer<1>(rep, ws, lds, b, g, s, w, lane, xraw, A, qa1, kB + kvoff, vB + kvoff, wqB, woB, flag, stampv, cnt);
  }
  if (g == 0 && w == s) {
    const u32 voff = WS_BOX + (u32)((b * 8 + c8) * 8 + w) * BOX + 3u * BOXL + BOX_X + kq * 32 + (p ? 16 : 0);
    if (DLY_B) __builtin_amdgcn_s_sleep(DLY_B);
    const v4u x = poll128(ws, voff);
    float v[8];
    sum_partials(x, v);
    if (c8 == 0) *(v4u*)(out + b * 512 + 64 * w + 16 * kq + (p ? 8 : 0)) = v4u{pk(v[0], v[1]), pk(v[2], v[3]), pk(v[4], v[5]), pk(v[6], v[7])};
  }
}

extern "C" void kernel_launch(void* const* d_in, const int* in_sizes, int n_in, void* d_out, int out_size, void* d_ws, size_t ws_size,
                              hipStream_t stream) {
  (void)hipFuncSetAttribute((const void*)fused_kernel, hipFuncAttributeMaxDynamicSharedMemorySize, L_END);
  fused_kernel<<<256, 512, L_END, stream>>>((const char*)d_in[0], (const char*)d_in[1], (const char*)d_in[2], (const char*)d_in[3], (const char*)d_in[4],
                                            (const char*)d_in[6], (const char*)d_in[7], (const char*)d_in[8], (const char*)d_in[9],
                                            (unsigned short*)d_out, (char*)d_ws);
}
```

```cpp
#include <hip/hip_runtime.h>
#include <stdint.h>

typedef unsigned int u32;
typedef unsigned long long u64;
typedef u32 v4u __attribute__((ext_vector_type(4)));
typedef u32 v2u __attribute__((ext_vector_type(2)));
typedef float v4f __attribute__((ext_vector_type(4)));
typedef float v2f __attribute__((ext_vector_type(2)));
typedef __bf16 v8b __attribute__((ext_vector_type(8)));
typedef __bf16 v2b __attribute__((ext_vector_type(2)));
#define GAS __attribute__((address_space(1)))

#define DLY_C 2
#define DLY_A 8
#define DLY_B 10
#define STG 1
#define DLY_A0 4
#define DLY_B0 4
#define WQB_AT 1
#define KB_AT 2
#define VB_AT 0
#define NTKV ""
#define NTW ""

namespace {
constexpr int SK = 4096, WIN = 1024;
constexpr u32 WS_EPOCH = 0;
constexpr u32 WS_RDY = 4096;
constexpr u32 WS_LSR = 8192;
constexpr u32 WS_BOX = WS_LSR + 131072;
constexpr u32 BOXL = 1280, BOX_Q = 1024, BOX_X = 1152, BOX_CS = 5120, BOX = 5376;
constexpr u32 WS_END = WS_BOX + 256 * BOX;
constexpr u32 WS_DBG = 4u << 20;
constexpr u32 L_KV = 0;
constexpr u32 L_K = 0, L_V = 65536;
constexpr u32 L_OM = 131072;
constexpr u32 L_ML = L_OM + 8192;
constexpr u32 L_Q = L_ML + 128;
constexpr u32 L_CSI = L_Q + 1088;
constexpr u32 L_RED = L_CSI + 2176;
constexpr u32 L_REDC = L_RED + 2048;
constexpr u32 L_ST = L_REDC + 2048;
constexpr u32 L_RED3 = L_ST + 64;
constexpr u32 L_XS = L_RED3 + 2048;
constexpr u32 L_END = L_XS + 256;
static_assert(L_END <= 160 * 1024, "lds");
static_assert(L_Q % 16 == 0 && L_CSI % 16 == 0 && L_RED % 16 == 0 && L_ST % 16 == 0 && L_RED3 % 16 == 0 && L_XS % 16 == 0, "align");
constexpr u32 SENT = 0xFFFFFFFFu;
constexpr u32 SPIN_CAP = 1u << 15;
constexpr float C2 = 0.08838834764831845f * 1.4426950408889634f;
constexpr u32 ONES = 0x3F803F80u;
constexpr u64 PMASK = 0xFF00FF00FF00FF00ull;
constexpr u64 C8Z = 0x0101010101010101ull;

__device__ __forceinline__ float bflo(u32 w) { return __uint_as_float(w << 16); }
__device__ __forceinline__ float bfhi(u32 w) { return __uint_as_float(w & 0xffff0000u); }
__device__ __forceinline__ u32 pk(float lo, float hi) { v2f f = {lo, hi}; return __builtin_bit_cast(u32, __builtin_convertvector(f, v2b)); }
__device__ __forceinline__ u32 safe(u32 v) { return v < 0xFFFFFFFEu ? v : 0xFFFFFFFEu; }
template <int CTRL> __device__ __forceinline__ float dpp(float x) {
  return __builtin_bit_cast(float, __builtin_amdgcn_mov_dpp(__builtin_bit_cast(int, x), CTRL, 0xf, 0xf, true));
}
template <int CTRL> __device__ __forceinline__ u32 dppu(u32 x) { return (u32)__builtin_amdgcn_mov_dpp((int)x, CTRL, 0xf, 0xf, true); }
constexpr int XOR1 = 0xB1, XOR2 = 0x4E, HMIR = 0x141, ROR8 = 0x128;
__device__ __forceinline__ float x16_add(float x) { auto s = __builtin_amdgcn_permlane16_swap(__float_as_uint(x), __float_as_uint(x), false, false); return __uint_as_float(s[0]) + __uint_as_float(s[1]); }
__device__ __forceinline__ float x32_add(float x) { auto s = __builtin_amdgcn_permlane32_swap(__float_as_uint(x), __float_as_uint(x), false, false); return __uint_as_float(s[0]) + __uint_as_float(s[1]); }
__device__ __forceinline__ v4f mfma(v4u a, v4u b, v4f c) {
  return __builtin_amdgcn_mfma_f32_16x16x32_bf16(__builtin_bit_cast(v8b, a), __builtin_bit_cast(v8b, b), c, 0, 0, 0);
}
#define BAR() asm volatile("s_waitcnt lgkmcnt(0)\n\ts_barrier" ::: "memory")
#define VMWAIT(n) asm volatile("s_waitcnt vmcnt(" #n ")" ::: "memory")

constexpr int R_WQA = 128, R_WOA = 160, R_WQB = 192, R_WOB = 224;
template <int BASE, u32 STRIDE>
__device__ __forceinline__ void bulk8(const char* base, u32 voff) {
  asm volatile("" : "+s"(base));
  const __amdgpu_buffer_rsrc_t r = __builtin_amdgcn_make_buffer_rsrc((void*)base, 0, 0x7fffffff, 0x00020000);
  asm volatile("s_nop 4\n\t"
               "buffer_load_dwordx4 v[%c10:%c11], %0, %1, 0 offen" NTW "\n\tbuffer_load_dwordx4 v[%c12:%c13], %0, %1, %3 offen" NTW "\n\t"
               "buffer_load_dwordx4 v[%c14:%c15], %0, %1, %4 offen" NTW "\n\tbuffer_load_dwordx4 v[%c16:%c17], %0, %1, %5 offen" NTW "\n\t"
               "buffer_load_dwordx4 v[%c18:%c19], %0, %1, %6 offen" NTW "\n\tbuffer_load_dwordx4 v[%c20:%c21], %0, %1, %7 offen" NTW "\n\t"
               "buffer_load_dwordx4 v[%c22:%c23], %0, %1, %8 offen" NTW "\n\tbuffer_load_dwordx4 v[%c24:%c25], %0, %1, %9 offen" NTW
               :: "v"(voff), "s"(r), "s"(0), "s"(STRIDE), "s"(2 * STRIDE), "s"(3 * STRIDE), "s"(4 * STRIDE), "s"(5 * STRIDE), "s"(6 * STRIDE), "s"(7 * STRIDE),
                  "i"(BASE), "i"(BASE + 3), "i"(BASE + 4), "i"(BASE + 7), "i"(BASE + 8), "i"(BASE + 11), "i"(BASE + 12), "i"(BASE + 15),
                  "i"(BASE + 16), "i"(BASE + 19), "i"(BASE + 20), "i"(BASE + 23), "i"(BASE + 24), "i"(BASE + 27), "i"(BASE + 28), "i"(BASE + 31)
               : "memory");
}
template <int B, int M, int R>
__device__ __forceinline__ void tp_pair(u32 slo, u32 shi) {
  if constexpr (M == R) {
    asm volatile("v_perm_b32 v100, v%c3, v%c2, %0\n\tv_perm_b32 v%c3, v%c3, v%c2, %1\n\tv_mov_b32 v%c2, v100"
                 :: "s"(slo), "s"(shi), "i"(B + 8 * M + R), "i"(B + 8 * M + 4 + R));
  } else {
    asm volatile("v_perm_b32 v100, v%c3, v%c2, %0\n\tv_perm_b32 v101, v%c3, v%c2, %1\n\t"
                 "v_perm_b32 v%c2, v%c5, v%c4, %0\n\tv_perm_b32 v%c3, v%c5, v%c4, %1\n\t"
                 "v_mov_b32 v%c4, v100\n\tv_mov_b32 v%c5, v101"
                 :: "s"(slo), "s"(shi), "i"(B + 8 * M + R), "i"(B + 8 * M + 4 + R), "i"(B + 8 * R + M), "i"(B + 8 * R + 4 + M));
  }
}
template <int B>
__device__ __forceinline__ void transpose8() {
  const u32 slo = 0x05040100u, shi = 0x07060302u;
  tp_pair<B, 0, 0>(slo, shi); tp_pair<B, 1, 1>(slo, shi); tp_pair<B, 2, 2>(slo, shi); tp_pair<B, 3, 3>(slo, shi);
  tp_pair<B, 0, 1>(slo, shi); tp_pair<B, 0, 2>(slo, shi); tp_pair<B, 0, 3>(slo, shi);
  tp_pair<B, 1, 2>(slo, shi); tp_pair<B, 1, 3>(slo, shi); tp_pair<B, 2, 3>(slo, shi);
  asm volatile("s_nop 1");
}
#define SNOP
#define MF(d, b) "v_mfma_f32_16x16x32_bf16 v[" #d ":" #d "+3], v[48:51], v[%c[wb]+" #b ":%c[wb]+" #b "+3], 0\n\t"
#define FOLD_TAIL \
  "v_permlane32_swap_b32 v52, v68\n\t" \
  "v_permlane32_swap_b32 v56, v72\n\t" \
  "v_permlane32_swap_b32 v60, v76\n\t" \
  "v_permlane32_swap_b32 v64, v80\n\t" \
  "v_cndmask_b32_e64 v52, v52, v68, %[pm]\n\t" \
  "v_cndmask_b32_e64 v60, v60, v76, %[pm]\n\t" \
  "v_cndmask_b32_e64 v56, v56, v72, %[pm]\n\t" \
  "v_cndmask_b32_e64 v64, v64, v80, %[pm]\n\t" \
  "v_permlane16_swap_b32 v52, v60\n\t" \
  "s_nop 0\n\t" \
  "v_permlane16_swap_b32 v56, v64\n\t" \
  "v_add_f32 v52, v52, v60\n\t" \
  "v_add_f32 v56, v56, v64\n\t" \
  "s_nop 0\n\t" \
  "v_add_f32_dpp v52, v52, v52 row_ror:8 row_mask:0xf bank_mask:0xf\n\t" \
  "v_add_f32_dpp v56, v56, v56 row_ror:8 row_mask:0xf bank_mask:0xf\n\t" \
  "v_cndmask_b32_e64 v52, v52, v56, %[pm]\n\t"

__device__ __forceinline__ void poll_issue(u32 voff, const char* ws) {
  asm volatile("s_nop 4\n\tglobal_load_dwordx4 v[48:51], %0, %1 sc1" :: "v"(voff), "s"(ws) : "memory");
}
template <int WB, int POLL, int LOFF>
__device__ __forceinline__ void seg_p1(u32 voff, const char* ws, u32 ldsw, u32& spins) {
  if constexpr (POLL != 0) {
    asm volatile(
      SNOP
      ".if %c[pre]\n\ts_branch Lc%=\n\t.endif\n\t"
      "Lp%=:\n\t"
      "global_load_dwordx4 v[48:51], %[voff], %[ws] sc1\n\t"
      "s_add_u32 %[sp], %[sp], 1\n\t"
      "s_waitcnt vmcnt(0)\n\t"
      "Lc%=:\n\t"
      MF(52, 0) "v_max3_u32 v84, v48, v49, v50\n\t"
      MF(56, 4) "v_max_u32 v84, v84, v51\n\t"
      MF(60, 8) "v_cmp_eq_u32 vcc, -1, v84\n\t"
      MF(64, 12)
      "s_cbranch_vccz Lk%=\n\t"
      "s_cmp_lt_u32 %[sp], %[cap]\n\t"
      "s_cbranch_scc1 Lp%=\n\t"
      "Lk%=:\n\t"
      : [sp] "+s"(spins) : [voff] "v"(voff), [ws] "s"(ws), [wb] "i"(WB), [cap] "i"(SPIN_CAP), [pre] "i"(POLL == 2) : "memory", "vcc", "scc");
  } else {
    asm volatile("s_nop 1\n\t" MF(52, 0) "s_nop 0\n\t" MF(56, 4) "s_nop 0\n\t" MF(60, 8) "s_nop 0\n\t" MF(64, 12) "s_nop 0\n\t" :: [wb] "i"(WB));
  }
  asm volatile(
    MF(68, 16) "v_pk_add_f32 v[52:53], v[52:53], v[54:55]\n\t"
    MF(72, 20) "v_pk_add_f32 v[56:57], v[56:57], v[58:59]\n\t"
    MF(76, 24) "v_pk_add_f32 v[60:61], v[60:61], v[62:63]\n\t"
    MF(80, 28) "v_pk_add_f32 v[64:65], v[64:65], v[66:67]\n\t"
    "v_add_f32 v52, v52, v53\n\t"
    "v_pk_add_f32 v[68:69], v[68:69], v[70:71]\n\t"
    "v_add_f32 v56, v56, v57\n\t"
    "v_pk_add_f32 v[72:73], v[72:73], v[74:75]\n\t"
    "v_add_f32 v60, v60, v61\n\t"
    "v_pk_add_f32 v[76:77], v[76:77], v[78:79]\n\t"
    "v_add_f32 v64, v64, v65\n\t"
    "v_pk_add_f32 v[80:81], v[80:81], v[82:83]\n\t"
    "v_add_f32 v68, v68, v69\n\t"
    "v_add_f32 v72, v72, v73\n\t"
    "v_add_f32 v76, v76, v77\n\t"
    "v_add_f32 v80, v80, v81\n\t"
    "s_nop 0\n\t"
    FOLD_TAIL
    "ds_write_b32 %[lw], v52 offset:%c[lo]\n\t"
    :: [wb] "i"(WB), [pm] "s"(PMASK), [lw] "v"(ldsw), [lo] "i"(LOFF) : "memory");
}
__device__ __forceinline__ void seg_p3_issue(u32 voff, u32 voff2, const char* ws, const char* ws2) {
  asm volatile("s_nop 4\n\tglobal_load_dwordx4 v[48:51], %0, %2 sc1\n\tglobal_load_dword v84, %1, %3 sc1" :: "v"(voff), "v"(voff2), "s"(ws), "s"(ws2) : "memory");
}
template <int WB, int PRE>
__device__ __forceinline__ void seg_p3(u32 voff, u32 voff2, const char* ws, const char* ws2, u32 ldsw, u32& spins) {
  asm volatile(
    SNOP
    ".if %c[pre]\n\ts_branch Lc%=\n\t.endif\n\t"
    "Lp%=:\n\t"
    "global_load_dwordx4 v[48:51], %[voff], %[ws] sc1\n\t"
    "global_load_dword v84, %[voff2], %[ws2] sc1\n\t"
    "s_add_u32 %[sp], %[sp], 1\n\t"
    "s_waitcnt vmcnt(0)\n\t"
    "Lc%=:\n\t"
    MF(52, 0) "v_max3_u32 v85, v48, v49, v50\n\t"
    MF(56, 4) "v_max3_u32 v85, v85, v51, v84\n\t"
    MF(60, 8) "v_cmp_eq_u32 vcc, -1, v85\n\t"
    MF(64, 12)
    "s_cbranch_vccz Lk%=\n\t"
    "s_cmp_lt_u32 %[sp], %[cap]\n\t"
    "s_cbranch_scc1 Lp%=\n\t"
    "Lk%=:\n\t"
    "v_max_f32_dpp v85, v84, v84 quad_perm:[1,0,3,2] row_mask:0xf bank_mask:0xf\n\t"
    MF(68, 16)
    "s_nop 0\n\t"
    "v_max_f32_dpp v85, v85, v85 quad_perm:[2,3,0,1] row_mask:0xf bank_mask:0xf\n\t"
    MF(72, 20)
    "s_nop 0\n\t"
    "v_max_f32_dpp v85, v85, v85 row_half_mirror row_mask:0xf bank_mask:0xf\n\t"
    MF(76, 24)
    "v_sub_f32 v86, v84, v85\n\t"
    MF(80, 28)
    "v_exp_f32 v86, v86\n\t"
    "s_nop 1\n\t"
    "v_mov_b32_dpp v90, v86 row_newbcast:0 row_mask:0x5 bank_mask:0xf\n\t"
    "v_mov_b32_dpp v91, v86 row_newbcast:1 row_mask:0x5 bank_mask:0xf\n\t"
    "v_mov_b32_dpp v92, v86 row_newbcast:2 row_mask:0x5 bank_mask:0xf\n\t"
    "v_mov_b32_dpp v93, v86 row_newbcast:3 row_mask:0x5 bank_mask:0xf\n\t"
    "v_mov_b32_dpp v90, v86 row_newbcast:4 row_mask:0xa bank_mask:0xf\n\t"
    "v_mov_b32_dpp v91, v86 row_newbcast:5 row_mask:0xa bank_mask:0xf\n\t"
    "v_mov_b32_dpp v92, v86 row_newbcast:6 row_mask:0xa bank_mask:0xf\n\t"
    "v_mov_b32_dpp v93, v86 row_newbcast:7 row_mask:0xa bank_mask:0xf\n\t"
    "v_add_f32_dpp v87, v86, v86 quad_perm:[1,0,3,2] row_mask:0xf bank_mask:0xf\n\t"
    "v_pk_mul_f32 v[52:53], v[52:53], v[90:91]\n\t"
    "v_pk_mul_f32 v[56:57], v[56:57], v[90:91]\n\t"
    "v_add_f32_dpp v87, v87, v87 quad_perm:[2,3,0,1] row_mask:0xf bank_mask:0xf\n\t"
    "v_pk_mul_f32 v[60:61], v[60:61], v[90:91]\n\t"
    "v_pk_mul_f32 v[64:65], v[64:65], v[90:91]\n\t"
    "v_add_f32_dpp v87, v87, v87 row_half_mirror row_mask:0xf bank_mask:0xf\n\t"
    "v_pk_mul_f32 v[68:69], v[68:69], v[90:91]\n\t"
    "v_pk_mul_f32 v[72:73], v[72:73], v[90:91]\n\t"
    "v_rcp_f32 v87, v87\n\t"
    "v_pk_mul_f32 v[76:77], v[76:77], v[90:91]\n\t"
    "v_pk_mul_f32 v[80:81], v[80:81], v[90:91]\n\t"
    "v_pk_fma_f32 v[52:53], v[54:55], v[92:93], v[52:53]\n\t"
    "v_pk_fma_f32 v[68:69], v[70:71], v[92:93], v[68:69]\n\t"
    "v_pk_fma_f32 v[56:57], v[58:59], v[92:93], v[56:57]\n\t"
    "v_pk_fma_f32 v[72:73], v[74:75], v[92:93], v[72:73]\n\t"
    "v_pk_fma_f32 v[60:61], v[62:63], v[92:93], v[60:61]\n\t"
    "v_pk_fma_f32 v[76:77], v[78:79], v[92:93], v[76:77]\n\t"
    "v_pk_fma_f32 v[64:65], v[66:67], v[92:93], v[64:65]\n\t"
    "v_pk_fma_f32 v[80:81], v[82:83], v[92:93], v[80:81]\n\t"
    "v_add_f32 v52, v52, v53\n\t"
    "v_add_f32 v68, v68, v69\n\t"
    "v_add_f32 v56, v56, v57\n\t"
    "v_add_f32 v72, v72, v73\n\t"
    "v_add_f32 v60, v60, v61\n\t"
    "v_add_f32 v76, v76, v77\n\t"
    "v_add_f32 v64, v64, v65\n\t"
    "v_add_f32 v80, v80, v81\n\t"
    "s_nop 1\n\t"
    FOLD_TAIL
    "v_mul_f32 v52, v52, v87\n\t"
    "ds_write_b32 %[lw], v52 offset:%c[lo]\n\t"
    : [sp] "+s"(spins)
    : [voff] "v"(voff), [voff2] "v"(voff2), [ws] "s"(ws), [ws2] "s"(ws2), [wb] "i"(WB), [cap] "i"(SPIN_CAP), [pm] "s"(PMASK), [lw] "v"(ldsw), [lo] "i"(L_RED3 - L_RED), [pre] "i"(PRE) : "memory", "vcc", "scc");
}
#define PACK_STORE \
  "s_nop 1\n\t" \
  "v_mov_b32_dpp v53, v52 quad_perm:[1,0,3,2] row_mask:0xf bank_mask:0xf\n\t" \
  "v_cvt_pk_bf16_f32 v54, v52, v53\n\t" \
  "v_min_u32 v54, -2, v54\n\t" \
  "s_nop 1\n\t" \
  "v_mov_b32_dpp v55, v54 row_shl:2 row_mask:0xf bank_mask:0xf\n\t" \
  "v_mov_b32_dpp v56, v54 row_shl:4 row_mask:0xf bank_mask:0xf\n\t" \
  "v_mov_b32_dpp v57, v54 row_shl:6 row_mask:0xf bank_mask:0xf\n\t" \
  "s_mov_b64 exec, %[c8z]\n\t" \
  "global_store_dwordx4 %[so], v[54:57], %[ws] sc1\n\t" \
  "s_mov_b64 exec, -1\n\t" \
  "s_nop 1\n\t"
template <bool XS>
__device__ __forceinline__ void seg_final2(u32 ldsr, u32 xsr, u32 so, const char* ws) {
  constexpr u32 LO = XS ? L_RED3 - L_RED : 0;
  if constexpr (XS) {
    asm volatile(
      "ds_read_b128 v[52:55], %[lr] offset:%c[lo]\n\t"
      "ds_read_b128 v[56:59], %[lr] offset:%c[lo]+16\n\t"
      "ds_read_b32 v60, %[xr]\n\t"
      "s_waitcnt lgkmcnt(1)\n\t"
      "v_pk_add_f32 v[52:53], v[52:53], v[56:57]\n\t"
      "v_pk_add_f32 v[54:55], v[54:55], v[58:59]\n\t"
      "v_pk_add_f32 v[52:53], v[52:53], v[54:55]\n\t"
      "s_waitcnt lgkmcnt(0)\n\t"
      "v_add_f32 v52, v52, v53\n\t"
      "v_add_f32 v52, v52, v60\n\t"
      PACK_STORE
      :: [lr] "v"(ldsr), [xr] "v"(xsr), [so] "v"(so), [ws] "s"(ws), [c8z] "s"(C8Z), [lo] "i"(LO) : "memory");
  } else {
    asm volatile(
      "ds_read_b128 v[52:55], %[lr]\n\t"
      "ds_read_b128 v[56:59], %[lr] offset:16\n\t"
      "s_waitcnt lgkmcnt(0)\n\t"
      "v_pk_add_f32 v[52:53], v[52:53], v[56:57]\n\t"
      "v_pk_add_f32 v[54:55], v[54:55], v[58:59]\n\t"
      "v_pk_add_f32 v[52:53], v[52:53], v[54:55]\n\t"
      "v_add_f32 v52, v52, v53\n\t"
      PACK_STORE
      :: [lr] "v"(ldsr), [so] "v"(so), [ws] "s"(ws), [c8z] "s"(C8Z) : "memory");
  }
}
template <int RB>
__device__ __forceinline__ void qpoll_issue(u32 voff, const char* ws) {
  asm volatile("s_nop 4\n\tglobal_load_dwordx4 v[%c2:%c2+3], %0, %1 sc1" :: "v"(voff), "s"(ws), "i"(RB) : "memory");
}
template <int LOFF, int RB, int PRE>
__device__ __forceinline__ void seg_qpoll(u32 voff, const char* ws, u32 ldsw, u32& spins) {
  asm volatile(
    SNOP
    ".if %c[pre]\n\ts_branch Lc%=\n\t.endif\n\t"
    "Lp%=:\n\t"
    "global_load_dwordx4 v[%c[rb]:%c[rb]+3], %[voff], %[ws] sc1\n\t"
    "s_add_u32 %[sp], %[sp], 1\n\t"
    "s_waitcnt vmcnt(0)\n\t"
    "Lc%=:\n\t"
    "v_max3_u32 v70, v%c[rb], v[%c[rb]+1], v[%c[rb]+2]\n\t"
    "v_max_u32 v70, v70, v[%c[rb]+3]\n\t"
    "v_cmp_eq_u32 vcc, -1, v70\n\t"
    "ds_write_b128 %[lw], v[%c[rb]:%c[rb]+3] offset:%c[lo]\n\t"
    "s_cbranch_vccz Lk%=\n\t"
    "s_cmp_lt_u32 %[sp], %[cap]\n\t"
    "s_cbranch_scc1 Lp%=\n\t"
    "Lk%=:\n\t"
    : [sp] "+s"(spins) : [voff] "v"(voff), [ws] "s"(ws), [lw] "v"(ldsw), [cap] "i"(SPIN_CAP), [lo] "i"(LOFF), [rb] "i"(RB), [pre] "i"(PRE) : "memory", "vcc", "scc");
}
template <int TO>
__device__ __forceinline__ void seg_kpre(u32 ka0, u32 ka1) {
  asm volatile(
    "ds_read_b128 v[48:51], %0 offset:%c2\n\tds_read_b128 v[64:67], %1 offset:%c2\n\t"
    "ds_read_b128 v[52:55], %0 offset:%c2+512\n\tds_read_b128 v[68:71], %1 offset:%c2+512\n\t"
    "ds_read_b128 v[56:59], %0 offset:%c2+1024\n\tds_read_b128 v[72:75], %1 offset:%c2+1024\n\t"
    "ds_read_b128 v[60:63], %0 offset:%c2+1536\n\tds_read_b128 v[76:79], %1 offset:%c2+1536\n\t"
    "v_mov_b32 v96, 0\n\tv_mov_b32 v97, 0\n\tv_mov_b32 v98, 0\n\tv_mov_b32 v99, 0\n\t"
    "v_mov_b32 v100, 0\n\tv_mov_b32 v101, 0\n\tv_mov_b32 v102, 0\n\tv_mov_b32 v103, 0\n\t"
    "v_mov_b32 v104, 0\n\tv_mov_b32 v105, 0\n\tv_mov_b32 v106, 0\n\tv_mov_b32 v107, 0\n\t"
    "v_mov_b32 v108, 0\n\tv_mov_b32 v109, 0\n\tv_mov_b32 v110, 0\n\tv_mov_b32 v111, 0\n\t"
    :: "v"(ka0), "v"(ka1), "i"(TO) : "memory");
}
#define QK(s, k, q, c) "v_mfma_f32_16x16x32_bf16 v[" #s ":" #s "+3], v[" #k ":" #k "+3], v[" #q ":" #q "+3], " c "\n\t"
#define TR(d, a, off) "ds_read_b64_tr_b16 v[" #d ":" #d "+1], %[" #a "] offset:%c[to]+" #off "\n\t"
#define PV(r) "v_mfma_f32_16x16x32_bf16 v[" #r ":" #r "+3], v[124:127], v[" #r ":" #r "+3], 0\n\t"
#define SM(r) "v_fma_f32 v" #r ", v" #r ", v99, v121\n\tv_exp_f32 v" #r ", v" #r "\n\t"
#define PVA(acc, a, v, c) "v_mfma_f32_16x16x32_bf16 v[" #acc ":" #acc "+3], v[" #a ":" #a "+3], v[" #v ":" #v "+3], " c "\n\t"
#define PCP(d, s, ctl, bank) "v_mov_b32_dpp v" #d ", v" #s " " ctl " row_mask:0xf bank_mask:" bank "\n\t"
template <int TO>
__device__ __forceinline__ void seg_attn(u32 qa, u32 sta, u32 va0, u32 va1, u32 va2, u32 va3, u32 oa, u32 mla) {
  asm volatile(
    "ds_read_b128 v[80:83], %[qa]\n\t"
    "ds_read_b128 v[84:87], %[qa] offset:64\n\t"
    "ds_read_b128 v[88:91], %[qa] offset:128\n\t"
    "ds_read_b128 v[92:95], %[qa] offset:192\n\t"
    "ds_read_b64 v[120:121], %[sta]\n\t"
    "s_waitcnt lgkmcnt(4)\n\t"
    QK(112, 48, 80, "0") QK(116, 64, 80, "0")
    "s_waitcnt lgkmcnt(3)\n\t"
    QK(112, 52, 84, "v[112:115]") QK(116, 68, 84, "v[116:119]")
    "s_waitcnt lgkmcnt(2)\n\t"
    QK(112, 56, 88, "v[112:115]") QK(116, 72, 88, "v[116:119]")
    "s_waitcnt lgkmcnt(0)\n\t"
    QK(112, 60, 92, "v[112:115]")
    "v_add_f32_dpp v120, v120, v120 quad_perm:[1,0,3,2] row_mask:0xf bank_mask:0xf\n\t"
    "v_add_f32_dpp v121, v121, v121 quad_perm:[1,0,3,2] row_mask:0xf bank_mask:0xf\n\t"
    QK(116, 76, 92, "v[116:119]")
    TR(48, va0, 0)
    "v_add_f32_dpp v120, v120, v120 quad_perm:[2,3,0,1] row_mask:0xf bank_mask:0xf\n\t"
    "v_add_f32_dpp v121, v121, v121 quad_perm:[2,3,0,1] row_mask:0xf bank_mask:0xf\n\t"
    TR(50, va1, 0) TR(52, va2, 0)
    "v_add_f32_dpp v120, v120, v120 row_half_mirror row_mask:0xf bank_mask:0xf\n\t"
    "v_add_f32_dpp v121, v121, v121 row_half_mirror row_mask:0xf bank_mask:0xf\n\t"
    TR(54, va3, 0)
    "v_mul_f32 v122, 0xbb000000, v120\n\t"
    "v_mul_f32 v123, 0x3b000000, v121\n\t"
    "v_fma_f32 v123, -v122, v122, v123\n\t"
    "v_fmac_f32_dpp v112, v112, v122 row_shl:4 row_mask:0xf bank_mask:0xf bound_ctrl:1\n\t"
    "v_max_f32 v123, 0, v123\n\t"
    "v_fmac_f32_dpp v113, v113, v122 row_shl:4 row_mask:0xf bank_mask:0xf bound_ctrl:1\n\t"
    "v_add_f32 v123, 0x3727c5ac, v123\n\t"
    "v_fmac_f32_dpp v114, v114, v122 row_shl:4 row_mask:0xf bank_mask:0xf bound_ctrl:1\n\t"
    "v_rsq_f32 v123, v123\n\t"
    "v_fmac_f32_dpp v115, v115, v122 row_shl:4 row_mask:0xf bank_mask:0xf bound_ctrl:1\n\t"
    "v_fmac_f32_dpp v116, v116, v122 row_shl:4 row_mask:0xf bank_mask:0xf bound_ctrl:1\n\t"
    "v_max3_f32 v120, v112, v113, v114\n\t"
    "v_fmac_f32_dpp v117, v117, v122 row_shl:4 row_mask:0xf bank_mask:0xf bound_ctrl:1\n\t"
    "v_fmac_f32_dpp v118, v118, v122 row_shl:4 row_mask:0xf bank_mask:0xf bound_ctrl:1\n\t"
    "v_fmac_f32_dpp v119, v119, v122 row_shl:4 row_mask:0xf bank_mask:0xf bound_ctrl:1\n\t"
    "v_max3_f32 v120, v120, v115, v116\n\t"
    "v_mul_f32 v123, %[c2], v123\n\t"
    "v_max3_f32 v120, v120, v117, v118\n\t"
    "v_max_f32 v120, v120, v119\n\t"
    "v_mov_b32 v121, v120\n\t"
    TR(56, va0, 512) TR(58, va1, 512)
    "v_permlane16_swap_b32 v120, v121\n\t"
    "v_max_f32 v120, v120, v121\n\t"
    "v_mov_b32 v121, v120\n\t"
    TR(60, va2, 512) TR(62, va3, 512)
    "v_permlane32_swap_b32 v120, v121\n\t"
    "v_max_f32 v120, v120, v121\n\t"
    "v_mul_f32_e64 v121, -v120, v123\n\t"
    "v_mul_f32 v122, v120, v123\n\t"
    "v_fma_f32 v112, v112, v123, v121\n\t"
    "v_fma_f32 v113, v113, v123, v121\n\t"
    "v_exp_f32 v112, v112\n\t"
    TR(64, va0, 1024)
    "v_fma_f32 v114, v114, v123, v121\n\t"
    "v_exp_f32 v113, v113\n\t"
    TR(66, va1, 1024)
    "v_fma_f32 v115, v115, v123, v121\n\t"
    "v_exp_f32 v114, v114\n\t"
    TR(68, va2, 1024)
    "v_fma_f32 v116, v116, v123, v121\n\t"
    "v_exp_f32 v115, v115\n\t"
    TR(70, va3, 1024)
    "v_fma_f32 v117, v117, v123, v121\n\t"
    "v_exp_f32 v116, v116\n\t"
    "v_cvt_pk_bf16_f32 v124, v112, v113\n\t"
    "v_fma_f32 v118, v118, v123, v121\n\t"
    "v_exp_f32 v117, v117\n\t"
    TR(72, va0, 1536)
    "v_fma_f32 v119, v119, v123, v121\n\t"
    "v_exp_f32 v118, v118\n\t"
    "v_cvt_pk_bf16_f32 v125, v114, v115\n\t"
    "v_exp_f32 v119, v119\n\t"
    TR(74, va1, 1536) TR(76, va2, 1536) TR(78, va3, 1536)
    "v_cvt_pk_bf16_f32 v126, v116, v117\n\t"
    "v_cvt_pk_bf16_f32 v127, v118, v119\n\t"
    PCP(96, 124, "quad_perm:[0,1,2,3]", "0x1") PCP(100, 124, "row_shr:4", "0x2") PCP(104, 124, "quad_perm:[0,1,2,3]", "0x4") PCP(108, 124, "row_shr:4", "0x8")
    PCP(97, 125, "quad_perm:[0,1,2,3]", "0x1") PCP(101, 125, "row_shr:4", "0x2") PCP(105, 125, "quad_perm:[0,1,2,3]", "0x4") PCP(109, 125, "row_shr:4", "0x8")
    PCP(98, 126, "quad_perm:[0,1,2,3]", "0x1") PCP(102, 126, "row_shr:4", "0x2") PCP(106, 126, "quad_perm:[0,1,2,3]", "0x4") PCP(110, 126, "row_shr:4", "0x8")
    PCP(99, 127, "quad_perm:[0,1,2,3]", "0x1") PCP(103, 127, "row_shr:4", "0x2") PCP(107, 127, "quad_perm:[0,1,2,3]", "0x4") PCP(111, 127, "row_shr:4", "0x8")
    "v_mov_b32 v84, %[ones]\n\tv_mov_b32 v85, %[ones]\n\tv_mov_b32 v86, %[ones]\n\tv_mov_b32 v87, %[ones]\n\t"
    "s_waitcnt lgkmcnt(0)\n\t"
    PVA(112, 96, 48, "0") PVA(116, 96, 64, "0")
    PVA(112, 100, 52, "v[112:115]") PVA(116, 100, 68, "v[116:119]")
    PVA(112, 104, 56, "v[112:115]") PVA(116, 104, 72, "v[116:119]")
    PVA(112, 108, 60, "v[112:115]") PVA(116, 108, 76, "v[116:119]")
    "v_mfma_f32_16x16x32_bf16 v[80:83], v[84:87], v[124:127], 0\n\t"
    "s_nop 6\n\t"
    "ds_write_b128 %[oa], v[112:115]\n\t"
    "ds_write_b128 %[oa], v[116:119] offset:1024\n\t"
    "s_mov_b64 exec, 0xf\n\t"
    "ds_write2_b32 %[mla], v122, v80 offset0:0 offset1:16\n\t"
    "s_mov_b64 exec, -1\n\t"
    :: [qa] "v"(qa), [sta] "v"(sta), [va0] "v"(va0), [va1] "v"(va1), [va2] "v"(va2), [va3] "v"(va3), [oa] "v"(oa), [mla] "v"(mla),
       [c2] "s"(C2), [ones] "s"(ONES), [to] "i"(TO) : "memory");
}
__device__ __forceinline__ void seg_merge(u32 mla, u32 oa, u32 so, const char* ws) {
  asm volatile(
    "ds_read_b128 v[48:51], %[mla]\n\t"
    "ds_read_b128 v[60:63], %[mla] offset:64\n\t"
    "ds_read2st64_b32 v[56:57], %[oa] offset0:0 offset1:8\n\t"
    "ds_read2st64_b32 v[58:59], %[oa] offset0:16 offset1:24\n\t"
    "s_waitcnt lgkmcnt(3)\n\t"
    "v_max3_f32 v52, v48, v49, v50\n\t"
    "v_max_f32 v52, v52, v51\n\t"
    "v_sub_f32 v48, v48, v52\n\t"
    "v_sub_f32 v49, v49, v52\n\t"
    "v_exp_f32 v48, v48\n\t"
    "v_sub_f32 v50, v50, v52\n\t"
    "v_exp_f32 v49, v49\n\t"
    "v_sub_f32 v51, v51, v52\n\t"
    "v_exp_f32 v50, v50\n\t"
    "v_exp_f32 v51, v51\n\t"
    "s_waitcnt lgkmcnt(0)\n\t"
    "v_pk_mul_f32 v[60:61], v[60:61], v[48:49]\n\t"
    "v_pk_mul_f32 v[56:57], v[56:57], v[48:49]\n\t"
    "v_pk_fma_f32 v[60:61], v[62:63], v[50:51], v[60:61]\n\t"
    "v_pk_fma_f32 v[56:57], v[58:59], v[50:51], v[56:57]\n\t"
    "v_add_f32 v60, v60, v61\n\t"
    "v_add_f32 v56, v56, v57\n\t"
    "v_rcp_f32 v60, v60\n\t"
    "s_nop 0\n\t"
    "v_mul_f32 v52, v56, v60\n\t"
    PACK_STORE
    :: [mla] "v"(mla), [oa] "v"(oa), [so] "v"(so), [ws] "s"(ws), [c8z] "s"(C8Z) : "memory");
}

__device__ __forceinline__ void dma16(u32 voff, const char* sbase, u32 lds_dst) {
  u32 keep;
  asm volatile(SNOP "s_mov_b32 %0, m0\n\ts_mov_b32 m0, %3\n\ts_nop 0\n\tglobal_load_lds_dwordx4 %1, %2" NTKV "\n\ts_mov_b32 m0, %0"
               : "=&s"(keep) : "v"(voff), "s"(sbase), "s"(lds_dst) : "memory");
}
__device__ __forceinline__ void kv_load(const char* base, int w, int lane, u32 ldsb) {
  asm volatile("" : "+s"(base));
  const int r8 = (lane & 31) >> 2;
#pragma unroll
  for (int i = 0; i < 4; ++i) {
    const u32 ch = 8 * (i & 1) + 4 * (lane >> 5) + ((lane & 3) ^ ((2 * (i >> 1) + (r8 >> 2)) & 3));
    dma16(r8 * 256 + ch * 16, base + (size_t)(16 * w + 8 * (i >> 1)) * 256, ldsb + w * 4096 + i * 1024);
  }
}
__device__ __forceinline__ bool has_sent(v4u x) {
  const u32 m = __builtin_elementwise_max(__builtin_elementwise_max(x[0], x[1]), __builtin_elementwise_max(x[2], x[3]));
  return __builtin_amdgcn_ballot_w64(m == SENT) != 0ull;
}
__device__ __forceinline__ v4u poll128(const char* base, u32 voff) {
  v4u x;
  for (u32 spins = 0;; ++spins) {
    asm volatile("s_nop 4\n\tglobal_load_dwordx4 %0, %1, %2 sc1\n\ts_waitcnt vmcnt(0)" : "=&v"(x) : "v"(voff), "s"(base) : "memory");
    if (!has_sent(x) || spins > SPIN_CAP) break;
  }
  return x;
}
__device__ __forceinline__ v4u pack8(float v) {
  const u32 x = safe(pk(v, dpp<XOR1>(v)));
  return v4u{x, dppu<0x102>(x), dppu<0x104>(x), dppu<0x106>(x)};
}
__device__ __forceinline__ void sum_partials(v4u x, float (&v)[8]) {
#pragma unroll
  for (int m = 0; m < 4; ++m) { v[2 * m] = bflo(x[m]); v[2 * m + 1] = bfhi(x[m]); }
#pragma unroll
  for (int j = 0; j < 8; ++j) v[j] += dpp<XOR1>(v[j]);
#pragma unroll
  for (int j = 0; j < 8; ++j) v[j] += dpp<XOR2>(v[j]);
#pragma unroll
  for (int j = 0; j < 8; ++j) v[j] += dpp<HMIR>(v[j]);
}
__device__ __forceinline__ void x_stats(v4u xa, char* lds, int w, int lane) {
  const bool p = (lane >> 3) & 1; const int kq = lane >> 4;
  const v4f gm = mfma(xa, xa, v4f{0.f, 0.f, 0.f, 0.f});
  const v4f sm = mfma(xa, v4u{ONES, ONES, ONES, ONES}, v4f{0.f, 0.f, 0.f, 0.f});
  float s2 = (gm[0] + gm[1]) + (gm[2] + gm[3]);
  s2 = (p == (bool)(kq >> 1)) ? s2 : 0.f;
  s2 += dpp<XOR1>(s2); s2 += dpp<XOR2>(s2); s2 += dpp<HMIR>(s2); s2 += dpp<ROR8>(s2);
  s2 = x32_add(x16_add(s2));
  float s1 = (sm[0] + sm[1]) + (sm[2] + sm[3]);
  s1 = x32_add(x16_add(s1));
  if (lane == 0) *(v2f*)(lds + L_ST + w * 8) = v2f{s1, s2};
}
}

#define ST(i) do {} while (0)
#define STFLUSH(sv, base) do {} while (0)
#define SGPR_ATTR

__device__ __forceinline__ u32 lb_st() { return L_ST; }
#define OPAQUE_LANE() int lane = lane0; asm volatile("" : "+v"(lane)); const int c8 = lane & 7, kq = lane >> 4; const bool p = (lane >> 3) & 1; (void)c8; (void)p; (void)kq
#define OPAQUE_S(x) asm volatile("" : "+s"(x))

constexpr int FW = 4;

struct Addr {
  u32 vB, vA, vL, lw, so;
  u32 mr, orr;
  u32 r0, r1, r2, r3, r4, r5, r6, r7, r8;
};
#define PIN(x) asm volatile("" : "+v"(x))

template <int TYPE>
__device__ __forceinline__ void layer(const int rep, char* ws, char* lds, int b, int g, int s, int w, const int lane0, v4u xraw, Addr& A, u32 qaT,
                                      const char* kB, const char* vB, const char* wqB, const char* cwo, const u64 flag, int& stampv, u32 (&cnt)[3]) {
  constexpr int RQ = TYPE ? R_WQB : R_WQA, RO = TYPE ? R_WOB : R_WOA;
  const int L = rep * 2 + TYPE;
  OPAQUE_S(b); OPAQUE_S(g); OPAQUE_S(s); OPAQUE_S(w);
  const u32 grp = (u32)(b * 8 + g), cu = grp * 8 + s;
#define STAGGER() do { if (STG) { const int r_ = (w - 2 * b) & 7; for (int i_ = 0; i_ < r_; ++i_) __builtin_amdgcn_s_sleep(STG); } } while (0)
#define LD_WQ(R, ptr) do { OPAQUE_LANE(); const char* p_ = ptr; OPAQUE_S(p_); bulk8<R, 8192>(p_ + (size_t)(64 * w) * 8192 + (512 * g + 64 * s) * 2, (u32)(lane >> 3) * 65536 + c8 * 16); } while (0)
#define LD_WO(R, ptr) do { OPAQUE_LANE(); const char* p_ = ptr; OPAQUE_S(p_); bulk8<R, 1024>(p_ + (size_t)(512 * g + 64 * w) * 1024 + 128 * s, (u32)(lane >> 3) * 8192 + c8 * 16); } while (0)
#define LD_KB() do { OPAQUE_LANE(); kv_load(kB, w, lane, L_K + 32768); } while (0)
#define LD_VB() do { OPAQUE_LANE(); kv_load(vB, w, lane, L_V + 32768); } while (0)
  const char* wsL = ws + L * BOXL;
  const char* box = ws + WS_BOX + cu * BOX + L * BOXL;
  {
    u32 sp = 0;
    if (rep == 0) {
      if (TYPE == 0) VMWAIT(8); else if (KB_AT == 2 && VB_AT == 2) VMWAIT(8); else if (KB_AT == 2 || VB_AT == 2) VMWAIT(4); else VMWAIT(0);
      if (w >= 4) ST(7);
      transpose8<RQ>();
    }
    if (L == 0) {
      const v4u xa = (lane0 & 7) == 0 ? xraw : v4u{0u, 0u, 0u, 0u};
      asm volatile("v_mov_b32 v48, %0\n\tv_mov_b32 v49, %1\n\tv_mov_b32 v50, %2\n\tv_mov_b32 v51, %3" :: "v"(xa[0]), "v"(xa[1]), "v"(xa[2]), "v"(xa[3]));
      seg_p1<RQ, 0, 0>(0, ws, A.lw, sp);
    } else {
      if (DLY_B && rep) __builtin_amdgcn_s_sleep(DLY_B);
      ST(0);
      seg_p1<RQ, 1, 0>(A.vB, wsL - BOXL, A.lw, sp);
      cnt[0] += sp;
    }
    ST(2);
    if (rep == 0 && TYPE == 1) { if (KB_AT == 0) LD_KB(); if (VB_AT == 0) LD_VB(); }
    v4u xa;
    asm volatile("v_mov_b32 %0, v48\n\tv_mov_b32 %1, v49\n\tv_mov_b32 %2, v50\n\tv_mov_b32 %3, v51" : "=v"(xa[0]), "=v"(xa[1]), "=v"(xa[2]), "=v"(xa[3]));
    if (rep == 0) {
      asm volatile("v_mov_b32 v48, %0\n\tv_mov_b32 v49, %0\n\tv_mov_b32 v50, %0\n\tv_mov_b32 v51, %0" :: "s"(ONES));
      seg_p1<RQ, 0, L_REDC - L_RED>(0, ws, A.lw, sp);
    }
    BAR();
    ST(3);
    if (w == FW) {
      seg_final2<false>(A.r2, 0, A.so, box + BOX_Q);
      ST(4);
      OPAQUE_LANE();
      if (rep == 0) {
        const __amdgpu_buffer_rsrc_t rs = __builtin_amdgcn_make_buffer_rsrc(ws, 0, WS_END, 0x00020000);
        const v4f r0 = *(const v4f*)(lds + L_REDC + lane * 32), r1 = *(const v4f*)(lds + L_REDC + lane * 32 + 16);
        const float cs = 0.125f * (((r0[0] + r0[1]) + (r0[2] + r0[3])) + ((r1[0] + r1[1]) + (r1[2] + r1[3])));
        const v4u pc = pack8(cs);
        if (c8 == 0) __builtin_amdgcn_raw_buffer_store_b128(pc, rs, WS_BOX + cu * BOX + BOX_CS + TYPE * 128 + lane * 2, 0, 16);
        if (TYPE == 1) { if (KB_AT == 1) LD_KB(); if (VB_AT == 1) LD_VB(); }
      }
      x_stats(xa, lds, w, lane);
      if (L == 0) {
        for (u32 spins = 0;; ++spins) {
          const u64 f = __hip_atomic_load((GAS u64*)(ws + WS_RDY + b * 512 + lane * 8), __ATOMIC_RELAXED, __HIP_MEMORY_SCOPE_AGENT);
          if (__all(f == flag) || spins > SPIN_CAP) break;
        }
        if (g == 0 && s == 0 && lane == 0) __hip_atomic_store((GAS u32*)(ws + WS_EPOCH + b * 256), (u32)flag, __ATOMIC_RELAXED, __HIP_MEMORY_SCOPE_AGENT);
      } else {
        if (DLY_C && rep) __builtin_amdgcn_s_sleep(DLY_C);
      }
      u32 sq = 0;
      if (rep == 0) {
        const char* wsC = ws + (BOX_CS + TYPE * 128 - BOX_Q);
        qpoll_issue<52>(A.r0, wsL);
        qpoll_issue<60>(A.r0, wsC);
        VMWAIT(0);
        seg_qpoll<0, 52, 1>(A.r0, wsL, A.r1, sq);
        seg_qpoll<L_CSI + TYPE * 1088 - L_Q, 60, 1>(A.r0, wsC, A.r1, sq);
      } else {
        seg_qpoll<0, 52, 0>(A.r0, wsL, A.r1, sq);
      }
      cnt[1] += sq;
      ST(5);
    } else {
      OPAQUE_LANE();
      if (rep == 0 && TYPE == 1) { if (KB_AT == 1) LD_KB(); if (VB_AT == 1) LD_VB(); }
      x_stats(xa, lds, w, lane);
    }
    if (g == 0 && w == s) {
      OPAQUE_LANE();
      float v[8];
      sum_partials(xa, v);
      if (c8 == 0) {
        float* xs = (float*)(lds + L_XS) + 16 * kq + (p ? 8 : 0);
        *(v4f*)xs = v4f{v[0], v[1], v[2], v[3]};
        *(v4f*)(xs + 4) = v4f{v[4], v[5], v[6], v[7]};
      }
    }
    if (rep == 0) {
      VMWAIT(0);
      if (w >= 4) ST(8);
      BAR();
      if (w < 4) LD_WO(RO, cwo);
    }
  }
  if (w < 4) {
    seg_kpre<TYPE * 32768>(A.r0, A.r1);
    BAR();
    ST(7);
    seg_attn<TYPE * 32768>(qaT, A.r6, A.r2, A.r3, A.r4, A.r5, A.r7, A.r8);
    ST(8);
  } else {
    BAR();
    if (rep == 0) {
      STAGGER();
      LD_WO(RO, cwo);
      if (TYPE == 0 && KB_AT == 3) {
        OPAQUE_LANE();
        kv_load(kB, 2 * (w - 4), lane, L_K + 32768);
        kv_load(kB, 2 * (w - 4) + 1, lane, L_K + 32768);
      }
    }
  }
  {
    BAR();
    seg_merge(A.mr, A.orr, A.so, box + w * 128);
    if (w == 7) {
      OPAQUE_LANE();
      const __amdgpu_buffer_rsrc_t rs = __builtin_amdgcn_make_buffer_rsrc(ws, 0, WS_END, 0x00020000);
      const int hh = (lane >> 2) & 3, aa = lane & 3;
      const float m1 = ((const float*)(lds + L_ML))[hh * 4 + aa], l1 = ((const float*)(lds + L_ML))[16 + hh * 4 + aa];
      float M1 = __builtin_fmaxf(m1, dpp<XOR1>(m1)); M1 = __builtin_fmaxf(M1, dpp<XOR2>(M1));
      float L1 = __builtin_amdgcn_exp2f(m1 - M1) * l1; L1 += dpp<XOR1>(L1); L1 += dpp<XOR2>(L1);
      const u32 e = safe(__float_as_uint(M1 + __builtin_amdgcn_logf(L1)));
      const v4u ev = {dppu<0x150>(e), dppu<0x154>(e), dppu<0x158>(e), dppu<0x15C>(e)};
      if (lane < 8) __builtin_amdgcn_raw_buffer_store_b128(ev, rs, WS_LSR + ((L * 32 + grp) * 8 + lane) * 128 + s * 16, 0, 16);
    }
    ST(9);
  }
  {
    const char* wsS = ws + L * 32768;
    u32 sp = 0;
    if (rep == 0) {
      if (TYPE == 0 && KB_AT == 3 && w >= 4) VMWAIT(8); else VMWAIT(0);
      transpose8<RO>();
    } else {
      if (DLY_A) __builtin_amdgcn_s_sleep(DLY_A);
    }
    ST(1);
    seg_p3<RO, 0>(A.vA, A.vL, wsL, wsS, A.lw, sp);
    cnt[2] += sp;
    ST(10);
    if (rep == 0 && TYPE == 0 && WQB_AT == 0) LD_WQ(R_WQB, wqB);
    BAR();
    if (w == FW) seg_final2<true>(A.r2, A.r3, A.so, box + BOX_X);
    ST(11);
    if (rep == 0 && TYPE == 0) {
      if (WQB_AT == 1) { STAGGER(); LD_WQ(R_WQB, wqB); }
      if (KB_AT == 2) LD_KB();
      if (VB_AT == 2) LD_VB();
    }
  }
  STFLUSH(stampv, L * 12);
}

__global__ void __launch_bounds__(512, 2) __attribute__((amdgpu_num_vgpr(24) SGPR_ATTR))
fused_kernel(const char* __restrict__ xin, const char* kA, const char* vA, const char* wqA, const char* woA,
             const char* kB, const char* vB, const char* wqB, const char* woB, unsigned short* out, char* ws) {
  extern __shared__ __attribute__((aligned(16))) char lds[];
  const int id = blockIdx.x, g = id & 7, b = id >> 6, s = (id >> 3) & 7;
  const int tid = threadIdx.x, lane = tid & 63, w = __builtin_amdgcn_readfirstlane(tid >> 6);
  const int c8 = lane & 7, kq = lane >> 4;
  const bool p = (lane >> 3) & 1;
  int stampv = 0;
  const __amdgpu_buffer_rsrc_t rs = __builtin_amdgcn_make_buffer_rsrc(ws, 0, WS_END, 0x00020000);
  const u32 cu = (u32)((b * 8 + g) * 8 + s);
  asm volatile("" ::: "v127", "v255");
  u32 ep; v4u xraw;
  asm volatile("s_nop 4\n\tglobal_load_dword v56, %0, %1 sc1\n\tglobal_load_dwordx4 v[52:55], %2, %3"
               :: "v"(WS_EPOCH + b * 256), "s"(ws), "v"((u32)((b * 512 + 64 * w + 16 * kq + (p ? 8 : 0)) * 2)), "s"(xin) : "memory");
  if (w == 5) {
    const v4u sv = {SENT, SENT, SENT, SENT};
#pragma unroll
    for (int i = 0; i < 5; ++i) __builtin_amdgcn_raw_buffer_store_b128(sv, rs, WS_BOX + cu * BOX + i * 1024 + lane * 16, 0, 16);
    const u32 o6 = lane < 16 ? WS_BOX + cu * BOX + 5120 + lane * 16
                             : WS_LSR + (u32)(((((lane - 16) >> 3) * 32 + b * 8 + g) * 8 + (lane & 7)) * 128 + s * 16);
    if (lane < 48) __builtin_amdgcn_raw_buffer_store_b128(sv, rs, o6, 0, 16);
  }
  if (w == 6) ((float*)(lds + L_XS))[lane] = 0.f;
  const size_t kvoff = ((size_t)(b * 8 + g) * SK + (SK - WIN) + s * 128) * 256;
  const size_t qoff = (size_t)(64 * w) * 8192 + (512 * g + 64 * s) * 2;
  if (STG) { const int r_ = (w - 2 * b) & 7; for (int i_ = 0; i_ < r_; ++i_) __builtin_amdgcn_s_sleep(STG); }
  bulk8<R_WQA, 8192>(wqA + qoff, (u32)(lane >> 3) * 65536 + c8 * 16);
  VMWAIT(8);
  asm volatile("v_mov_b32 %0, v52\n\tv_mov_b32 %1, v53\n\tv_mov_b32 %2, v54\n\tv_mov_b32 %3, v55\n\tv_mov_b32 %4, v56"
               : "=v"(xraw[0]), "=v"(xraw[1]), "=v"(xraw[2]), "=v"(xraw[3]), "=v"(ep));
  ST(6);
  const u32 tag = (u32)__builtin_amdgcn_readfirstlane((int)ep) + 1u;
  const u64 flag = (u64)tag | ((u64)(~tag) << 32);
  if (w == 5 && lane == 0) __hip_atomic_store((GAS u64*)(ws + WS_RDY + b * 512 + (s * 8 + g) * 8), flag, __ATOMIC_RELAXED, __HIP_MEMORY_SCOPE_AGENT);
  asm volatile("s_barrier" ::: "memory");
  kv_load(kA + kvoff, w, lane, L_K);
  kv_load(vA + kvoff, w, lane, L_V);
  Addr A; u32 qa0, qa1;
  {
    const u32 grp = (u32)(b * 8 + g);
    const u32 oct = kq * 32 + (p ? 16 : 0);
    A.vB = WS_BOX + (u32)((b * 8 + c8) * 8 + w) * BOX + BOX_X + oct;
    A.vA = WS_BOX + (grp * 8 + c8) * BOX + w * 128 + oct;
    A.vL = WS_LSR + (grp * 8 + s) * 128 + c8 * 16 + (w >> 1) * 4;
    A.lw = L_RED + (8 * c8 + (lane >> 3)) * 32 + w * 4;
    A.so = lane * 2;
    A.mr = L_ML + (w >> 1) * 16;
    A.orr = L_OM + (w & 1) * 1024 + lane * 16 + (w >> 1) * 4;
    const int r16 = lane & 15, q4 = r16 >> 2, p2 = lane & 3;
    qa0 = L_Q + (lane & 3) * 272 + kq * 16;
    qa1 = qa0;
    if (lane & 4) { qa0 += L_CSI - L_Q; qa1 += L_CSI + 1088 - L_Q; }
    if (w < 4) {
      const u32 kb = L_K + 2048 * (4 * w + (r16 >> 2)) + 64 * (r16 & 3);
      A.r0 = kb + 16 * (kq ^ ((2 * (r16 >> 2)) & 3));
      A.r1 = kb + 256 + 16 * (kq ^ ((2 * (r16 >> 2) + 1) & 3));
      const u32 vb = L_V + 2048 * (4 * w + kq) + 64 * q4 + 8 * (p2 & 1);
      A.r2 = vb + 16 * (((p2 >> 1)) ^ ((2 * kq) & 3));
      A.r3 = vb + 256 + 16 * (((p2 >> 1)) ^ ((2 * kq + 1) & 3));
      A.r4 = vb + 16 * ((2 + (p2 >> 1)) ^ ((2 * kq) & 3));
      A.r5 = vb + 256 + 16 * ((2 + (p2 >> 1)) ^ ((2 * kq + 1) & 3));
      A.r7 = L_OM + w * 2048 + lane * 16;
      A.r8 = L_ML + (lane * 4 + w) * 4;
    } else {
      A.r0 = WS_BOX + (grp * 8 + (lane >> 3)) * BOX + BOX_Q + c8 * 16;
      A.r1 = L_Q + kq * 272 + (lane & 15) * 16;
      A.r2 = L_RED + lane * 32;
      A.r3 = L_XS + lane * 4;
      A.r4 = A.r5 = A.r7 = A.r8 = 0;
    }
    A.r6 = L_ST + c8 * 8;
    PIN(A.r6);
    PIN(A.vB); PIN(A.vA); PIN(A.vL); PIN(A.lw); PIN(A.so); PIN(A.mr); PIN(A.orr); PIN(A.r0); PIN(A.r1); PIN(A.r2); PIN(A.r3); PIN(A.r4); PIN(A.r5); PIN(A.r7); PIN(A.r8);
    PIN(qa0); PIN(qa1);
  }
  u32 cnt[3] = {0, 0, 0};
  for (int rep = 0; rep < 2; ++rep) {
    layer<0>(rep, ws, lds, b, g, s, w, lane, xraw, A, qa0, kB + kvoff, vB + kvoff, wqB, woA, flag, stampv, cnt);
    layer<1>(rep, ws, lds, b, g, s, w, lane, xraw, A, qa1, kB + kvoff, vB + kvoff, wqB, woB, flag, stampv, cnt);
  }
  if (g == 0 && w == s) {
    const u32 voff = WS_BOX + (u32)((b * 8 + c8) * 8 + w) * BOX + 3u * BOXL + BOX_X + kq * 32 + (p ? 16 : 0);
    if (DLY_B) __builtin_amdgcn_s_sleep(DLY_B);
    const v4u x = poll128(ws, voff);
    float v[8];
    sum_partials(x, v);
    if (c8 == 0) *(v4u*)(out + b * 512 + 64 * w + 16 * kq + (p ? 8 : 0)) = v4u{pk(v[0], v[1]), pk(v[2], v[3]), pk(v[4], v[5]), pk(v[6], v[7])};
  }
}

extern "C" void kernel_launch(void* const* d_in, const int* in_sizes, int n_in, void* d_out, int out_size, void* d_ws, size_t ws_size,
                              hipStream_t stream) {
  (void)hipFuncSetAttribute((const void*)fused_kernel, hipFuncAttributeMaxDynamicSharedMemorySize, L_END);
  fused_kernel<<<256, 512, L_END, stream>>>((const char*)d_in[0], (const char*)d_in[1], (const char*)d_in[2], (const char*)d_in[3], (const char*)d_in[4],
                                            (const char*)d_in[6], (const char*)d_in[7], (const char*)d_in[8], (const char*)d_in[9],
                                            (unsigned short*)d_out, (char*)d_ws);
}
```

```cpp
#include <hip/hip_runtime.h>
#include <stdint.h>

typedef unsigned int u32;
typedef unsigned long long u64;
typedef u32 v4u __attribute__((ext_vector_type(4)));
typedef u32 v2u __attribute__((ext_vector_type(2)));
typedef float v4f __attribute__((ext_vector_type(4)));
typedef float v2f __attribute__((ext_vector_type(2)));
typedef __bf16 v8b __attribute__((ext_vector_type(8)));
typedef __bf16 v2b __attribute__((ext_vector_type(2)));
#define GAS __attribute__((address_space(1)))

#define DLY_C 2
#define DLY_A 8
#define DLY_B 12
#define STG 2
#define DLY_A0 4
#define DLY_B0 4
#define WQB_AT 1
#define KB_AT 9
#define VB_AT 1
#define HALF 1
#define NTKV " sc0 nt"
#define NTW ""

namespace {
constexpr int SK = 4096, WIN = 1024;
constexpr u32 WS_EPOCH = 0;
constexpr u32 WS_RDY = 4096;
constexpr u32 WS_LSR = 8192;
constexpr u32 WS_BOX = WS_LSR + 131072;
constexpr u32 BOXL = 1280, BOX_Q = 1024, BOX_X = 1152, BOX_CS = 5120, BOX = 5376;
constexpr u32 WS_END = WS_BOX + 256 * BOX;
constexpr u32 WS_DBG = 4u << 20;
constexpr u32 L_KV = 0;
constexpr u32 L_K = 0, L_V = 65536;
constexpr u32 L_OM = 131072;
constexpr u32 L_ML = L_OM + 8192;
constexpr u32 L_Q = L_ML + 128;
constexpr u32 L_CSI = L_Q + 1088;
constexpr u32 L_RED = L_CSI + 2176;
constexpr u32 L_REDC = L_RED + 2048;
constexpr u32 L_ST = L_REDC + 2048;
constexpr u32 L_RED3 = L_ST + 64;
constexpr u32 L_XS = L_RED3 + 2048;
constexpr u32 L_END = L_XS + 256;
static_assert(L_END <= 160 * 1024, "lds");
static_assert(L_Q % 16 == 0 && L_CSI % 16 == 0 && L_RED % 16 == 0 && L_ST % 16 == 0 && L_RED3 % 16 == 0 && L_XS % 16 == 0, "align");
constexpr u32 SENT = 0xFFFFFFFFu;
constexpr u32 SPIN_CAP = 1u << 15;
constexpr float C2 = 0.08838834764831845f * 1.4426950408889634f;
constexpr u32 ONES = 0x3F803F80u;
constexpr u64 PMASK = 0xFF00FF00FF00FF00ull;
constexpr u64 C8Z = 0x0101010101010101ull;

__device__ __forceinline__ float bflo(u32 w) { return __uint_as_float(w << 16); }
__device__ __forceinline__ float bfhi(u32 w) { return __uint_as_float(w & 0xffff0000u); }
__device__ __forceinline__ u32 pk(float lo, float hi) { v2f f = {lo, hi}; return __builtin_bit_cast(u32, __builtin_convertvector(f, v2b)); }
__device__ __forceinline__ u32 safe(u32 v) { return v < 0xFFFFFFFEu ? v : 0xFFFFFFFEu; }
template <int CTRL> __device__ __forceinline__ float dpp(float x) {
  return __builtin_bit_cast(float, __builtin_amdgcn_mov_dpp(__builtin_bit_cast(int, x), CTRL, 0xf, 0xf, true));
}
template <int CTRL> __device__ __forceinline__ u32 dppu(u32 x) { return (u32)__builtin_amdgcn_mov_dpp((int)x, CTRL, 0xf, 0xf, true); }
constexpr int XOR1 = 0xB1, XOR2 = 0x4E, HMIR = 0x141, ROR8 = 0x128;
__device__ __forceinline__ float x16_add(float x) { auto s = __builtin_amdgcn_permlane16_swap(__float_as_uint(x), __float_as_uint(x), false, false); return __uint_as_float(s[0]) + __uint_as_float(s[1]); }
__device__ __forceinline__ float x32_add(float x) { auto s = __builtin_amdgcn_permlane32_swap(__float_as_uint(x), __float_as_uint(x), false, false); return __uint_as_float(s[0]) + __uint_as_float(s[1]); }
__device__ __forceinline__ v4f mfma(v4u a, v4u b, v4f c) {
  return __builtin_amdgcn_mfma_f32_16x16x32_bf16(__builtin_bit_cast(v8b, a), __builtin_bit_cast(v8b, b), c, 0, 0, 0);
}
#define BAR() asm volatile("s_waitcnt lgkmcnt(0)\n\ts_barrier" ::: "memory")
#define VMWAIT(n) asm volatile("s_waitcnt vmcnt(" #n ")" ::: "memory")

constexpr int R_WQA = 128, R_WOA = 160, R_WQB = 192, R_WOB = 224;
template <int BASE, u32 STRIDE>
__device__ __forceinline__ void bulk8(const char* base, u32 voff) {
  asm volatile("" : "+s"(base));
  const __amdgpu_buffer_rsrc_t r = __builtin_amdgcn_make_buffer_rsrc((void*)base, 0, 0x7fffffff, 0x00020000);
  asm volatile("s_nop 4\n\t"
               "buffer_load_dwordx4 v[%c10:%c11], %0, %1, 0 offen" NTW "\n\tbuffer_load_dwordx4 v[%c12:%c13], %0, %1, %3 offen" NTW "\n\t"
               "buffer_load_dwordx4 v[%c14:%c15], %0, %1, %4 offen" NTW "\n\tbuffer_load_dwordx4 v[%c16:%c17], %0, %1, %5 offen" NTW "\n\t"
               "buffer_load_dwordx4 v[%c18:%c19], %0, %1, %6 offen" NTW "\n\tbuffer_load_dwordx4 v[%c20:%c21], %0, %1, %7 offen" NTW "\n\t"
               "buffer_load_dwordx4 v[%c22:%c23], %0, %1, %8 offen" NTW "\n\tbuffer_load_dwordx4 v[%c24:%c25], %0, %1, %9 offen" NTW
               :: "v"(voff), "s"(r), "s"(0), "s"(STRIDE), "s"(2 * STRIDE), "s"(3 * STRIDE), "s"(4 * STRIDE), "s"(5 * STRIDE), "s"(6 * STRIDE), "s"(7 * STRIDE),
                  "i"(BASE), "i"(BASE + 3), "i"(BASE + 4), "i"(BASE + 7), "i"(BASE + 8), "i"(BASE + 11), "i"(BASE + 12), "i"(BASE + 15),
                  "i"(BASE + 16), "i"(BASE + 19), "i"(BASE + 20), "i"(BASE + 23), "i"(BASE + 24), "i"(BASE + 27), "i"(BASE + 28), "i"(BASE + 31)
               : "memory");
}
template <int BASE, u32 STRIDE, int I0>
__device__ __forceinline__ void bulk2(const char* base, u32 voff) {
  asm volatile("" : "+s"(base));
  const __amdgpu_buffer_rsrc_t r = __builtin_amdgcn_make_buffer_rsrc((void*)base, 0, 0x7fffffff, 0x00020000);
  asm volatile("s_nop 4\n\tbuffer_load_dwordx4 v[%c4:%c4+3], %0, %1, %2 offen" NTW "\n\tbuffer_load_dwordx4 v[%c4+4:%c4+7], %0, %1, %3 offen" NTW
               :: "v"(voff), "s"(r), "s"(I0 * STRIDE), "s"((I0 + 1) * STRIDE), "i"(BASE + 4 * I0) : "memory");
}
template <int B, int M, int R>
__device__ __forceinline__ void tp_pair(u32 slo, u32 shi) {
  if constexpr (M == R) {
    asm volatile("v_perm_b32 v100, v%c3, v%c2, %0\n\tv_perm_b32 v%c3, v%c3, v%c2, %1\n\tv_mov_b32 v%c2, v100"
                 :: "s"(slo), "s"(shi), "i"(B + 8 * M + R), "i"(B + 8 * M + 4 + R));
  } else {
    asm volatile("v_perm_b32 v100, v%c3, v%c2, %0\n\tv_perm_b32 v101, v%c3, v%c2, %1\n\t"
                 "v_perm_b32 v%c2, v%c5, v%c4, %0\n\tv_perm_b32 v%c3, v%c5, v%c4, %1\n\t"
                 "v_mov_b32 v%c4, v100\n\tv_mov_b32 v%c5, v101"
                 :: "s"(slo), "s"(shi), "i"(B + 8 * M + R), "i"(B + 8 * M + 4 + R), "i"(B + 8 * R + M), "i"(B + 8 * R + 4 + M));
  }
}
template <int B>
__device__ __forceinline__ void transpose8() {
  const u32 slo = 0x05040100u, shi = 0x07060302u;
  tp_pair<B, 0, 0>(slo, shi); tp_pair<B, 1, 1>(slo, shi); tp_pair<B, 2, 2>(slo, shi); tp_pair<B, 3, 3>(slo, shi);
  tp_pair<B, 0, 1>(slo, shi); tp_pair<B, 0, 2>(slo, shi); tp_pair<B, 0, 3>(slo, shi);
  tp_pair<B, 1, 2>(slo, shi); tp_pair<B, 1, 3>(slo, shi); tp_pair<B, 2, 3>(slo, shi);
  asm volatile("s_nop 1");
}
#define SNOP
#define MF(d, b) "v_mfma_f32_16x16x32_bf16 v[" #d ":" #d "+3], v[48:51], v[%c[wb]+" #b ":%c[wb]+" #b "+3], 0\n\t"
#define FOLD_TAIL \
  "v_permlane32_swap_b32 v52, v68\n\t" \
  "v_permlane32_swap_b32 v56, v72\n\t" \
  "v_permlane32_swap_b32 v60, v76\n\t" \
  "v_permlane32_swap_b32 v64, v80\n\t" \
  "v_cndmask_b32_e64 v52, v52, v68, %[pm]\n\t" \
  "v_cndmask_b32_e64 v60, v60, v76, %[pm]\n\t" \
  "v_cndmask_b32_e64 v56, v56, v72, %[pm]\n\t" \
  "v_cndmask_b32_e64 v64, v64, v80, %[pm]\n\t" \
  "v_permlane16_swap_b32 v52, v60\n\t" \
  "s_nop 0\n\t" \
  "v_permlane16_swap_b32 v56, v64\n\t" \
  "v_add_f32 v52, v52, v60\n\t" \
  "v_add_f32 v56, v56, v64\n\t" \
  "s_nop 0\n\t" \
  "v_add_f32_dpp v52, v52, v52 row_ror:8 row_mask:0xf bank_mask:0xf\n\t" \
  "v_add_f32_dpp v56, v56, v56 row_ror:8 row_mask:0xf bank_mask:0xf\n\t" \
  "v_cndmask_b32_e64 v52, v52, v56, %[pm]\n\t"

__device__ __forceinline__ void poll_issue(u32 voff, const char* ws) {
  asm volatile("s_nop 4\n\tglobal_load_dwordx4 v[48:51], %0, %1 sc1" :: "v"(voff), "s"(ws) : "memory");
}
template <int WB, int POLL, int LOFF>
__device__ __forceinline__ void seg_p1(u32 voff, const char* ws, u32 ldsw, u32& spins) {
  if constexpr (POLL != 0) {
    asm volatile(
      SNOP
      ".if %c[pre]\n\ts_branch Lc%=\n\t.endif\n\t"
      "Lp%=:\n\t"
      "global_load_dwordx4 v[48:51], %[voff], %[ws] sc1\n\t"
      "s_add_u32 %[sp], %[sp], 1\n\t"
      "s_waitcnt vmcnt(0)\n\t"
      "Lc%=:\n\t"
      MF(52, 0) "v_max3_u32 v84, v48, v49, v50\n\t"
      MF(56, 4) "v_max_u32 v84, v84, v51\n\t"
      MF(60, 8) "v_cmp_eq_u32 vcc, -1, v84\n\t"
      MF(64, 12)
      "s_cbranch_vccz Lk%=\n\t"
      "s_cmp_lt_u32 %[sp], %[cap]\n\t"
      "s_cbranch_scc1 Lp%=\n\t"
      "Lk%=:\n\t"
      : [sp] "+s"(spins) : [voff] "v"(voff), [ws] "s"(ws), [wb] "i"(WB), [cap] "i"(SPIN_CAP), [pre] "i"(POLL == 2) : "memory", "vcc", "scc");
  } else {
    asm volatile("s_nop 1\n\t" MF(52, 0) "s_nop 0\n\t" MF(56, 4) "s_nop 0\n\t" MF(60, 8) "s_nop 0\n\t" MF(64, 12) "s_nop 0\n\t" :: [wb] "i"(WB));
  }
  asm volatile(
    MF(68, 16) "v_pk_add_f32 v[52:53], v[52:53], v[54:55]\n\t"
    MF(72, 20) "v_pk_add_f32 v[56:57], v[56:57], v[58:59]\n\t"
    MF(76, 24) "v_pk_add_f32 v[60:61], v[60:61], v[62:63]\n\t"
    MF(80, 28) "v_pk_add_f32 v[64:65], v[64:65], v[66:67]\n\t"
    "v_add_f32 v52, v52, v53\n\t"
    "v_pk_add_f32 v[68:69], v[68:69], v[70:71]\n\t"
    "v_add_f32 v56, v56, v57\n\t"
    "v_pk_add_f32 v[72:73], v[72:73], v[74:75]\n\t"
    "v_add_f32 v60, v60, v61\n\t"
    "v_pk_add_f32 v[76:77], v[76:77], v[78:79]\n\t"
    "v_add_f32 v64, v64, v65\n\t"
    "v_pk_add_f32 v[80:81], v[80:81], v[82:83]\n\t"
    "v_add_f32 v68, v68, v69\n\t"
    "v_add_f32 v72, v72, v73\n\t"
    "v_add_f32 v76, v76, v77\n\t"
    "v_add_f32 v80, v80, v81\n\t"
    "s_nop 0\n\t"
    FOLD_TAIL
    "ds_write_b32 %[lw], v52 offset:%c[lo]\n\t"
    :: [wb] "i"(WB), [pm] "s"(PMASK), [lw] "v"(ldsw), [lo] "i"(LOFF) : "memory");
}
__device__ __forceinline__ void seg_p3_issue(u32 voff, u32 voff2, const char* ws, const char* ws2) {
  asm volatile("s_nop 4\n\tglobal_load_dwordx4 v[48:51], %0, %2 sc1\n\tglobal_load_dword v84, %1, %3 sc1" :: "v"(voff), "v"(voff2), "s"(ws), "s"(ws2) : "memory");
}
template <int WB, int PRE>
__device__ __forceinline__ void seg_p3(u32 voff, u32 voff2, const char* ws, const char* ws2, u32 ldsw, u32& spins) {
  asm volatile(
    SNOP
    ".if %c[pre]\n\ts_branch Lc%=\n\t.endif\n\t"
    "Lp%=:\n\t"
    "global_load_dwordx4 v[48:51], %[voff], %[ws] sc1\n\t"
    "global_load_dword v84, %[voff2], %[ws2] sc1\n\t"
    "s_add_u32 %[sp], %[sp], 1\n\t"
    "s_waitcnt vmcnt(0)\n\t"
    "Lc%=:\n\t"
    MF(52, 0) "v_max3_u32 v85, v48, v49, v50\n\t"
    MF(56, 4) "v_max3_u32 v85, v85, v51, v84\n\t"
    MF(60, 8) "v_cmp_eq_u32 vcc, -1, v85\n\t"
    MF(64, 12)
    "s_cbranch_vccz Lk%=\n\t"
    "s_cmp_lt_u32 %[sp], %[cap]\n\t"
    "s_cbranch_scc1 Lp%=\n\t"
    "Lk%=:\n\t"
    "v_max_f32_dpp v85, v84, v84 quad_perm:[1,0,3,2] row_mask:0xf bank_mask:0xf\n\t"
    MF(68, 16)
    "s_nop 0\n\t"
    "v_max_f32_dpp v85, v85, v85 quad_perm:[2,3,0,1] row_mask:0xf bank_mask:0xf\n\t"
    MF(72, 20)
    "s_nop 0\n\t"
    "v_max_f32_dpp v85, v85, v85 row_half_mirror row_mask:0xf bank_mask:0xf\n\t"
    MF(76, 24)
    "v_sub_f32 v86, v84, v85\n\t"
    MF(80, 28)
    "v_exp_f32 v86, v86\n\t"
    "s_nop 1\n\t"
    "v_mov_b32_dpp v90, v86 row_newbcast:0 row_mask:0x5 bank_mask:0xf\n\t"
    "v_mov_b32_dpp v91, v86 row_newbcast:1 row_mask:0x5 bank_mask:0xf\n\t"
    "v_mov_b32_dpp v92, v86 row_newbcast:2 row_mask:0x5 bank_mask:0xf\n\t"
    "v_mov_b32_dpp v93, v86 row_newbcast:3 row_mask:0x5 bank_mask:0xf\n\t"
    "v_mov_b32_dpp v90, v86 row_newbcast:4 row_mask:0xa bank_mask:0xf\n\t"
    "v_mov_b32_dpp v91, v86 row_newbcast:5 row_mask:0xa bank_mask:0xf\n\t"
    "v_mov_b32_dpp v92, v86 row_newbcast:6 row_mask:0xa bank_mask:0xf\n\t"
    "v_mov_b32_dpp v93, v86 row_newbcast:7 row_mask:0xa bank_mask:0xf\n\t"
    "v_add_f32_dpp v87, v86, v86 quad_perm:[1,0,3,2] row_mask:0xf bank_mask:0xf\n\t"
    "v_pk_mul_f32 v[52:53], v[52:53], v[90:91]\n\t"
    "v_pk_mul_f32 v[56:57], v[56:57], v[90:91]\n\t"
    "v_add_f32_dpp v87, v87, v87 quad_perm:[2,3,0,1] row_mask:0xf bank_mask:0xf\n\t"
    "v_pk_mul_f32 v[60:61], v[60:61], v[90:91]\n\t"
    "v_pk_mul_f32 v[64:65], v[64:65], v[90:91]\n\t"
    "v_add_f32_dpp v87, v87, v87 row_half_mirror row_mask:0xf bank_mask:0xf\n\t"
    "v_pk_mul_f32 v[68:69], v[68:69], v[90:91]\n\t"
    "v_pk_mul_f32 v[72:73], v[72:73], v[90:91]\n\t"
    "v_rcp_f32 v87, v87\n\t"
    "v_pk_mul_f32 v[76:77], v[76:77], v[90:91]\n\t"
    "v_pk_mul_f32 v[80:81], v[80:81], v[90:91]\n\t"
    "v_pk_fma_f32 v[52:53], v[54:55], v[92:93], v[52:53]\n\t"
    "v_pk_fma_f32 v[68:69], v[70:71], v[92:93], v[68:69]\n\t"
    "v_pk_fma_f32 v[56:57], v[58:59], v[92:93], v[56:57]\n\t"
    "v_pk_fma_f32 v[72:73], v[74:75], v[92:93], v[72:73]\n\t"
    "v_pk_fma_f32 v[60:61], v[62:63], v[92:93], v[60:61]\n\t"
    "v_pk_fma_f32 v[76:77], v[78:79], v[92:93], v[76:77]\n\t"
    "v_pk_fma_f32 v[64:65], v[66:67], v[92:93], v[64:65]\n\t"
    "v_pk_fma_f32 v[80:81], v[82:83], v[92:93], v[80:81]\n\t"
    "v_add_f32 v52, v52, v53\n\t"
    "v_add_f32 v68, v68, v69\n\t"
    "v_add_f32 v56, v56, v57\n\t"
    "v_add_f32 v72, v72, v73\n\t"
    "v_add_f32 v60, v60, v61\n\t"
    "v_add_f32 v76, v76, v77\n\t"
    "v_add_f32 v64, v64, v65\n\t"
    "v_add_f32 v80, v80, v81\n\t"
    "s_nop 1\n\t"
    FOLD_TAIL
    "v_mul_f32 v52, v52, v87\n\t"
    "ds_write_b32 %[lw], v52 offset:%c[lo]\n\t"
    : [sp] "+s"(spins)
    : [voff] "v"(voff), [voff2] "v"(voff2), [ws] "s"(ws), [ws2] "s"(ws2), [wb] "i"(WB), [cap] "i"(SPIN_CAP), [pm] "s"(PMASK), [lw] "v"(ldsw), [lo] "i"(L_RED3 - L_RED), [pre] "i"(PRE) : "memory", "vcc", "scc");
}
#define PACK_STORE \
  "s_nop 1\n\t" \
  "v_mov_b32_dpp v53, v52 quad_perm:[1,0,3,2] row_mask:0xf bank_mask:0xf\n\t" \
  "v_cvt_pk_bf16_f32 v54, v52, v53\n\t" \
  "v_min_u32 v54, -2, v54\n\t" \
  "s_nop 1\n\t" \
  "v_mov_b32_dpp v55, v54 row_shl:2 row_mask:0xf bank_mask:0xf\n\t" \
  "v_mov_b32_dpp v56, v54 row_shl:4 row_mask:0xf bank_mask:0xf\n\t" \
  "v_mov_b32_dpp v57, v54 row_shl:6 row_mask:0xf bank_mask:0xf\n\t" \
  "s_mov_b64 exec, %[c8z]\n\t" \
  "global_store_dwordx4 %[so], v[54:57], %[ws] sc1\n\t" \
  "s_mov_b64 exec, -1\n\t" \
  "s_nop 1\n\t"
template <bool XS>
__device__ __forceinline__ void seg_final2(u32 ldsr, u32 xsr, u32 so, const char* ws) {
  constexpr u32 LO = XS ? L_RED3 - L_RED : 0;
  if constexpr (XS) {
    asm volatile(
      "ds_read_b128 v[52:55], %[lr] offset:%c[lo]\n\t"
      "ds_read_b128 v[56:59], %[lr] offset:%c[lo]+16\n\t"
      "ds_read_b32 v60, %[xr]\n\t"
      "s_waitcnt lgkmcnt(1)\n\t"
      "v_pk_add_f32 v[52:53], v[52:53], v[56:57]\n\t"
      "v_pk_add_f32 v[54:55], v[54:55], v[58:59]\n\t"
      "v_pk_add_f32 v[52:53], v[52:53], v[54:55]\n\t"
      "s_waitcnt lgkmcnt(0)\n\t"
      "v_add_f32 v52, v52, v53\n\t"
      "v_add_f32 v52, v52, v60\n\t"
      PACK_STORE
      :: [lr] "v"(ldsr), [xr] "v"(xsr), [so] "v"(so), [ws] "s"(ws), [c8z] "s"(C8Z), [lo] "i"(LO) : "memory");
  } else {
    asm volatile(
      "ds_read_b128 v[52:55], %[lr]\n\t"
      "ds_read_b128 v[56:59], %[lr] offset:16\n\t"
      "s_waitcnt lgkmcnt(0)\n\t"
      "v_pk_add_f32 v[52:53], v[52:53], v[56:57]\n\t"
      "v_pk_add_f32 v[54:55], v[54:55], v[58:59]\n\t"
      "v_pk_add_f32 v[52:53], v[52:53], v[54:55]\n\t"
      "v_add_f32 v52, v52, v53\n\t"
      PACK_STORE
      :: [lr] "v"(ldsr), [so] "v"(so), [ws] "s"(ws), [c8z] "s"(C8Z) : "memory");
  }
}
template <int RB>
__device__ __forceinline__ void qpoll_issue(u32 voff, const char* ws) {
  asm volatile("s_nop 4\n\tglobal_load_dwordx4 v[%c2:%c2+3], %0, %1 sc1" :: "v"(voff), "s"(ws), "i"(RB) : "memory");
}
template <int LOFF, int RB, int PRE>
__device__ __forceinline__ void seg_qpoll(u32 voff, const char* ws, u32 ldsw, u32& spins) {
  asm volatile(
    SNOP
    ".if %c[pre]\n\ts_branch Lc%=\n\t.endif\n\t"
    "Lp%=:\n\t"
    "global_load_dwordx4 v[%c[rb]:%c[rb]+3], %[voff], %[ws] sc1\n\t"
    "s_add_u32 %[sp], %[sp], 1\n\t"
    "s_waitcnt vmcnt(0)\n\t"
    "Lc%=:\n\t"
    "v_max3_u32 v70, v%c[rb], v[%c[rb]+1], v[%c[rb]+2]\n\t"
    "v_max_u32 v70, v70, v[%c[rb]+3]\n\t"
    "v_cmp_eq_u32 vcc, -1, v70\n\t"
    "ds_write_b128 %[lw], v[%c[rb]:%c[rb]+3] offset:%c[lo]\n\t"
    "s_cbranch_vccz Lk%=\n\t"
    "s_cmp_lt_u32 %[sp], %[cap]\n\t"
    "s_cbranch_scc1 Lp%=\n\t"
    "Lk%=:\n\t"
    : [sp] "+s"(spins) : [voff] "v"(voff), [ws] "s"(ws), [lw] "v"(ldsw), [cap] "i"(SPIN_CAP), [lo] "i"(LOFF), [rb] "i"(RB), [pre] "i"(PRE) : "memory", "vcc", "scc");
}
template <int TO>
__device__ __forceinline__ void seg_kpre(u32 ka0, u32 ka1) {
  asm volatile(
    "ds_read_b128 v[48:51], %0 offset:%c2\n\tds_read_b128 v[64:67], %1 offset:%c2\n\t"
    "ds_read_b128 v[52:55], %0 offset:%c2+512\n\tds_read_b128 v[68:71], %1 offset:%c2+512\n\t"
    "ds_read_b128 v[56:59], %0 offset:%c2+1024\n\tds_read_b128 v[72:75], %1 offset:%c2+1024\n\t"
    "ds_read_b128 v[60:63], %0 offset:%c2+1536\n\tds_read_b128 v[76:79], %1 offset:%c2+1536\n\t"
    "v_mov_b32 v96, 0\n\tv_mov_b32 v97, 0\n\tv_mov_b32 v98, 0\n\tv_mov_b32 v99, 0\n\t"
    "v_mov_b32 v100, 0\n\tv_mov_b32 v101, 0\n\tv_mov_b32 v102, 0\n\tv_mov_b32 v103, 0\n\t"
    "v_mov_b32 v104, 0\n\tv_mov_b32 v105, 0\n\tv_mov_b32 v106, 0\n\tv_mov_b32 v107, 0\n\t"
    "v_mov_b32 v108, 0\n\tv_mov_b32 v109, 0\n\tv_mov_b32 v110, 0\n\tv_mov_b32 v111, 0\n\t"
    :: "v"(ka0), "v"(ka1), "i"(TO) : "memory");
}
#define QK(s, k, q, c) "v_mfma_f32_16x16x32_bf16 v[" #s ":" #s "+3], v[" #k ":" #k "+3], v[" #q ":" #q "+3], " c "\n\t"
#define TR(d, a, off) "ds_read_b64_tr_b16 v[" #d ":" #d "+1], %[" #a "] offset:%c[to]+" #off "\n\t"
#define PV(r) "v_mfma_f32_16x16x32_bf16 v[" #r ":" #r "+3], v[124:127], v[" #r ":" #r "+3], 0\n\t"
#define SM(r) "v_fma_f32 v" #r ", v" #r ", v99, v121\n\tv_exp_f32 v" #r ", v" #r "\n\t"
#define PVA(acc, a, v, c) "v_mfma_f32_16x16x32_bf16 v[" #acc ":" #acc "+3], v[" #a ":" #a "+3], v[" #v ":" #v "+3], " c "\n\t"
#define PCP(d, s, ctl, bank) "v_mov_b32_dpp v" #d ", v" #s " " ctl " row_mask:0xf bank_mask:" bank "\n\t"
template <int TO>
__device__ __forceinline__ void seg_attn(u32 qa, u32 sta, u32 va0, u32 va1, u32 va2, u32 va3, u32 oa, u32 mla) {
  asm volatile(
    "ds_read_b128 v[80:83], %[qa]\n\t"
    "ds_read_b128 v[84:87], %[qa] offset:64\n\t"
    "ds_read_b128 v[88:91], %[qa] offset:128\n\t"
    "ds_read_b128 v[92:95], %[qa] offset:192\n\t"
    "ds_read_b64 v[120:121], %[sta]\n\t"
    "s_waitcnt lgkmcnt(4)\n\t"
    QK(112, 48, 80, "0") QK(116, 64, 80, "0")
    "s_waitcnt lgkmcnt(3)\n\t"
    QK(112, 52, 84, "v[112:115]") QK(116, 68, 84, "v[116:119]")
    "s_waitcnt lgkmcnt(2)\n\t"
    QK(112, 56, 88, "v[112:115]") QK(116, 72, 88, "v[116:119]")
    "s_waitcnt lgkmcnt(0)\n\t"
    QK(112, 60, 92, "v[112:115]")
    "v_add_f32_dpp v120, v120, v120 quad_perm:[1,0,3,2] row_mask:0xf bank_mask:0xf\n\t"
    "v_add_f32_dpp v121, v121, v121 quad_perm:[1,0,3,2] row_mask:0xf bank_mask:0xf\n\t"
    QK(116, 76, 92, "v[116:119]")
    TR(48, va0, 0)
    "v_add_f32_dpp v120, v120, v120 quad_perm:[2,3,0,1] row_mask:0xf bank_mask:0xf\n\t"
    "v_add_f32_dpp v121, v121, v121 quad_perm:[2,3,0,1] row_mask:0xf bank_mask:0xf\n\t"
    TR(50, va1, 0) TR(52, va2, 0)
    "v_add_f32_dpp v120, v120, v120 row_half_mirror row_mask:0xf bank_mask:0xf\n\t"
    "v_add_f32_dpp v121, v121, v121 row_half_mirror row_mask:0xf bank_mask:0xf\n\t"
    TR(54, va3, 0)
    "v_mul_f32 v122, 0xbb000000, v120\n\t"
    "v_mul_f32 v123, 0x3b000000, v121\n\t"
    "v_fma_f32 v123, -v122, v122, v123\n\t"
    "v_fmac_f32_dpp v112, v112, v122 row_shl:4 row_mask:0xf bank_mask:0xf bound_ctrl:1\n\t"
    "v_max_f32 v123, 0, v123\n\t"
    "v_fmac_f32_dpp v113, v113, v122 row_shl:4 row_mask:0xf bank_mask:0xf bound_ctrl:1\n\t"
    "v_add_f32 v123, 0x3727c5ac, v123\n\t"
    "v_fmac_f32_dpp v114, v114, v122 row_shl:4 row_mask:0xf bank_mask:0xf bound_ctrl:1\n\t"
    "v_rsq_f32 v123, v123\n\t"
    "v_fmac_f32_dpp v115, v115, v122 row_shl:4 row_mask:0xf bank_mask:0xf bound_ctrl:1\n\t"
    "v_fmac_f32_dpp v116, v116, v122 row_shl:4 row_mask:0xf bank_mask:0xf bound_ctrl:1\n\t"
    "v_max3_f32 v120, v112, v113, v114\n\t"
    "v_fmac_f32_dpp v117, v117, v122 row_shl:4 row_mask:0xf bank_mask:0xf bound_ctrl:1\n\t"
    "v_fmac_f32_dpp v118, v118, v122 row_shl:4 row_mask:0xf bank_mask:0xf bound_ctrl:1\n\t"
    "v_fmac_f32_dpp v119, v119, v122 row_shl:4 row_mask:0xf bank_mask:0xf bound_ctrl:1\n\t"
    "v_max3_f32 v120, v120, v115, v116\n\t"
    "v_mul_f32 v123, %[c2], v123\n\t"
    "v_max3_f32 v120, v120, v117, v118\n\t"
    "v_max_f32 v120, v120, v119\n\t"
    "v_mov_b32 v121, v120\n\t"
    TR(56, va0, 512) TR(58, va1, 512)
    "v_permlane16_swap_b32 v120, v121\n\t"
    "v_max_f32 v120, v120, v121\n\t"
    "v_mov_b32 v121, v120\n\t"
    TR(60, va2, 512) TR(62, va3, 512)
    "v_permlane32_swap_b32 v120, v121\n\t"
    "v_max_f32 v120, v120, v121\n\t"
    "v_mul_f32_e64 v121, -v120, v123\n\t"
    "v_mul_f32 v122, v120, v123\n\t"
    "v_fma_f32 v112, v112, v123, v121\n\t"
    "v_fma_f32 v113, v113, v123, v121\n\t"
    "v_exp_f32 v112, v112\n\t"
    TR(64, va0, 1024)
    "v_fma_f32 v114, v114, v123, v121\n\t"
    "v_exp_f32 v113, v113\n\t"
    TR(66, va1, 1024)
    "v_fma_f32 v115, v115, v123, v121\n\t"
    "v_exp_f32 v114, v114\n\t"
    TR(68, va2, 1024)
    "v_fma_f32 v116, v116, v123, v121\n\t"
    "v_exp_f32 v115, v115\n\t"
    TR(70, va3, 1024)
    "v_fma_f32 v117, v117, v123, v121\n\t"
    "v_exp_f32 v116, v116\n\t"
    "v_cvt_pk_bf16_f32 v124, v112, v113\n\t"
    "v_fma_f32 v118, v118, v123, v121\n\t"
    "v_exp_f32 v117, v117\n\t"
    TR(72, va0, 1536)
    "v_fma_f32 v119, v119, v123, v121\n\t"
    "v_exp_f32 v118, v118\n\t"
    "v_cvt_pk_bf16_f32 v125, v114, v115\n\t"
    "v_exp_f32 v119, v119\n\t"
    TR(74, va1, 1536) TR(76, va2, 1536) TR(78, va3, 1536)
    "v_cvt_pk_bf16_f32 v126, v116, v117\n\t"
    "v_cvt_pk_bf16_f32 v127, v118, v119\n\t"
    PCP(96, 124, "quad_perm:[0,1,2,3]", "0x1") PCP(100, 124, "row_shr:4", "0x2") PCP(104, 124, "quad_perm:[0,1,2,3]", "0x4") PCP(108, 124, "row_shr:4", "0x8")
    PCP(97, 125, "quad_perm:[0,1,2,3]", "0x1") PCP(101, 125, "row_shr:4", "0x2") PCP(105, 125, "quad_perm:[0,1,2,3]", "0x4") PCP(109, 125, "row_shr:4", "0x8")
    PCP(98, 126, "quad_perm:[0,1,2,3]", "0x1") PCP(102, 126, "row_shr:4", "0x2") PCP(106, 126, "quad_perm:[0,1,2,3]", "0x4") PCP(110, 126, "row_shr:4", "0x8")
    PCP(99, 127, "quad_perm:[0,1,2,3]", "0x1") PCP(103, 127, "row_shr:4", "0x2") PCP(107, 127, "quad_perm:[0,1,2,3]", "0x4") PCP(111, 127, "row_shr:4", "0x8")
    "v_mov_b32 v84, %[ones]\n\tv_mov_b32 v85, %[ones]\n\tv_mov_b32 v86, %[ones]\n\tv_mov_b32 v87, %[ones]\n\t"
    "s_waitcnt lgkmcnt(0)\n\t"
    PVA(112, 96, 48, "0") PVA(116, 96, 64, "0")
    PVA(112, 100, 52, "v[112:115]") PVA(116, 100, 68, "v[116:119]")
    PVA(112, 104, 56, "v[112:115]") PVA(116, 104, 72, "v[116:119]")
    PVA(112, 108, 60, "v[112:115]") PVA(116, 108, 76, "v[116:119]")
    "v_mfma_f32_16x16x32_bf16 v[80:83], v[84:87], v[124:127], 0\n\t"
    "s_nop 6\n\t"
    "ds_write_b128 %[oa], v[112:115]\n\t"
    "ds_write_b128 %[oa], v[116:119] offset:1024\n\t"
    "s_mov_b64 exec, 0xf\n\t"
    "ds_write2_b32 %[mla], v122, v80 offset0:0 offset1:16\n\t"
    "s_mov_b64 exec, -1\n\t"
    :: [qa] "v"(qa), [sta] "v"(sta), [va0] "v"(va0), [va1] "v"(va1), [va2] "v"(va2), [va3] "v"(va3), [oa] "v"(oa), [mla] "v"(mla),
       [c2] "s"(C2), [ones] "s"(ONES), [to] "i"(TO) : "memory");
}
__device__ __forceinline__ void seg_merge(u32 mla, u32 oa, u32 so, const char* ws) {
  asm volatile(
    "ds_read_b128 v[48:51], %[mla]\n\t"
    "ds_read_b128 v[60:63], %[mla] offset:64\n\t"
    "ds_read2st64_b32 v[56:57], %[oa] offset0:0 offset1:8\n\t"
    "ds_read2st64_b32 v[58:59], %[oa] offset0:16 offset1:24\n\t"
    "s_waitcnt lgkmcnt(3)\n\t"
    "v_max3_f32 v52, v48, v49, v50\n\t"
    "v_max_f32 v52, v52, v51\n\t"
    "v_sub_f32 v48, v48, v52\n\t"
    "v_sub_f32 v49, v49, v52\n\t"
    "v_exp_f32 v48, v48\n\t"
    "v_sub_f32 v50, v50, v52\n\t"
    "v_exp_f32 v49, v49\n\t"
    "v_sub_f32 v51, v51, v52\n\t"
    "v_exp_f32 v50, v50\n\t"
    "v_exp_f32 v51, v51\n\t"
    "s_waitcnt lgkmcnt(0)\n\t"
    "v_pk_mul_f32 v[60:61], v[60:61], v[48:49]\n\t"
    "v_pk_mul_f32 v[56:57], v[56:57], v[48:49]\n\t"
    "v_pk_fma_f32 v[60:61], v[62:63], v[50:51], v[60:61]\n\t"
    "v_pk_fma_f32 v[56:57], v[58:59], v[50:51], v[56:57]\n\t"
    "v_add_f32 v60, v60, v61\n\t"
    "v_add_f32 v56, v56, v57\n\t"
    "v_rcp_f32 v60, v60\n\t"
    "s_nop 0\n\t"
    "v_mul_f32 v52, v56, v60\n\t"
    PACK_STORE
    :: [mla] "v"(mla), [oa] "v"(oa), [so] "v"(so), [ws] "s"(ws), [c8z] "s"(C8Z) : "memory");
}

__device__ __forceinline__ void dma16(u32 voff, const char* sbase, u32 lds_dst) {
  u32 keep;
  asm volatile(SNOP "s_mov_b32 %0, m0\n\ts_mov_b32 m0, %3\n\ts_nop 0\n\tglobal_load_lds_dwordx4 %1, %2" NTKV "\n\ts_mov_b32 m0, %0"
               : "=&s"(keep) : "v"(voff), "s"(sbase), "s"(lds_dst) : "memory");
}
__device__ __forceinline__ void kv_load(const char* base, int w, int lane, u32 ldsb) {
  asm volatile("" : "+s"(base));
  const int r8 = (lane & 31) >> 2;
#pragma unroll
  for (int i = 0; i < 4; ++i) {
    const u32 ch = 8 * (i & 1) + 4 * (lane >> 5) + ((lane & 3) ^ ((2 * (i >> 1) + (r8 >> 2)) & 3));
    dma16(r8 * 256 + ch * 16, base + (size_t)(16 * w + 8 * (i >> 1)) * 256, ldsb + w * 4096 + i * 1024);
  }
}
__device__ __forceinline__ bool has_sent(v4u x) {
  const u32 m = __builtin_elementwise_max(__builtin_elementwise_max(x[0], x[1]), __builtin_elementwise_max(x[2], x[3]));
  return __builtin_amdgcn_ballot_w64(m == SENT) != 0ull;
}
__device__ __forceinline__ v4u poll128(const char* base, u32 voff) {
  v4u x;
  for (u32 spins = 0;; ++spins) {
    asm volatile("s_nop 4\n\tglobal_load_dwordx4 %0, %1, %2 sc1\n\ts_waitcnt vmcnt(0)" : "=&v"(x) : "v"(voff), "s"(base) : "memory");
    if (!has_sent(x) || spins > SPIN_CAP) break;
  }
  return x;
}
__device__ __forceinline__ v4u pack8(float v) {
  const u32 x = safe(pk(v, dpp<XOR1>(v)));
  return v4u{x, dppu<0x102>(x), dppu<0x104>(x), dppu<0x106>(x)};
}
__device__ __forceinline__ void sum_partials(v4u x, float (&v)[8]) {
#pragma unroll
  for (int m = 0; m < 4; ++m) { v[2 * m] = bflo(x[m]); v[2 * m + 1] = bfhi(x[m]); }
#pragma unroll
  for (int j = 0; j < 8; ++j) v[j] += dpp<XOR1>(v[j]);
#pragma unroll
  for (int j = 0; j < 8; ++j) v[j] += dpp<XOR2>(v[j]);
#pragma unroll
  for (int j = 0; j < 8; ++j) v[j] += dpp<HMIR>(v[j]);
}
__device__ __forceinline__ void x_stats(v4u xa, char* lds, int w, int lane) {
  const bool p = (lane >> 3) & 1; const int kq = lane >> 4;
  const v4f gm = mfma(xa, xa, v4f{0.f, 0.f, 0.f, 0.f});
  const v4f sm = mfma(xa, v4u{ONES, ONES, ONES, ONES}, v4f{0.f, 0.f, 0.f, 0.f});
  float s2 = (gm[0] + gm[1]) + (gm[2] + gm[3]);
  s2 = (p == (bool)(kq >> 1)) ? s2 : 0.f;
  s2 += dpp<XOR1>(s2); s2 += dpp<XOR2>(s2); s2 += dpp<HMIR>(s2); s2 += dpp<ROR8>(s2);
  s2 = x32_add(x16_add(s2));
  float s1 = (sm[0] + sm[1]) + (sm[2] + sm[3]);
  s1 = x32_add(x16_add(s1));
  if (lane == 0) *(v2f*)(lds + L_ST + w * 8) = v2f{s1, s2};
}
}

#define ST(i) do {} while (0)
#define STFLUSH(sv, base) do {} while (0)
#define SGPR_ATTR

__device__ __forceinline__ u32 lb_st() { return L_ST; }
#define OPAQUE_LANE() int lane = lane0; asm volatile("" : "+v"(lane)); const int c8 = lane & 7, kq = lane >> 4; const bool p = (lane >> 3) & 1; (void)c8; (void)p; (void)kq
#define OPAQUE_S(x) asm volatile("" : "+s"(x))

constexpr int FW = 4;

struct Addr {
  u32 vB, vA, vL, lw, so;
  u32 mr, orr;
  u32 r0, r1, r2, r3, r4, r5, r6, r7, r8;
};
#define PIN(x) asm volatile("" : "+v"(x))

template <int TYPE>
__device__ __forceinline__ void layer(const int rep, char* ws, char* lds, int b, int g, int s, int w, const int lane0, v4u xraw, Addr& A, u32 qaT,
                                      const char* kB, const char* vB, const char* wqB, const char* cwo, const u64 flag, int& stampv, u32 (&cnt)[3]) {
  constexpr int RQ = TYPE ? R_WQB : R_WQA, RO = TYPE ? R_WOB : R_WOA;
  const int L = rep * 2 + TYPE;
  OPAQUE_S(b); OPAQUE_S(g); OPAQUE_S(s); OPAQUE_S(w);
  const u32 grp = (u32)(b * 8 + g), cu = grp * 8 + s;
#define STAGGER() do { if (STG) { const int r_ = (w - 2 * b) & 7; for (int i_ = 0; i_ < r_; ++i_) __builtin_amdgcn_s_sleep(STG); } } while (0)
#define LD_WQ(R, ptr) do { OPAQUE_LANE(); const char* p_ = ptr; OPAQUE_S(p_); bulk8<R, 8192>(p_ + (size_t)(64 * w) * 8192 + (512 * g + 64 * s) * 2, (u32)(lane >> 3) * 65536 + c8 * 16); } while (0)
#define LD_WO(R, ptr) do { OPAQUE_LANE(); const char* p_ = ptr; OPAQUE_S(p_); bulk8<R, 1024>(p_ + (size_t)(512 * g + 64 * w) * 1024 + 128 * s, (u32)(lane >> 3) * 8192 + c8 * 16); } while (0)
#define LD_WQ2(R, ptr, I0) do { OPAQUE_LANE(); const char* p_ = ptr; OPAQUE_S(p_); bulk2<R, 8192, I0>(p_ + (size_t)(64 * w) * 8192 + (512 * g + 64 * s) * 2, (u32)(lane >> 3) * 65536 + c8 * 16); } while (0)
#define LD_KB() do { OPAQUE_LANE(); kv_load(kB, w, lane, L_K + 32768); } while (0)
#define LD_VB() do { OPAQUE_LANE(); kv_load(vB, w, lane, L_V + 32768); } while (0)
  const char* wsL = ws + L * BOXL;
  const char* box = ws + WS_BOX + cu * BOX + L * BOXL;
  {
    u32 sp = 0;
    if (rep == 0) {
      if (TYPE == 0) VMWAIT(8); else if (KB_AT == 2 && VB_AT == 2) VMWAIT(8); else if (KB_AT == 2 || VB_AT == 2) VMWAIT(4); else VMWAIT(0);
      if (w >= 4) ST(7);
      transpose8<RQ>();
    }
    if (L == 0) {
      const v4u xa = (lane0 & 7) == 0 ? xraw : v4u{0u, 0u, 0u, 0u};
      asm volatile("v_mov_b32 v48, %0\n\tv_mov_b32 v49, %1\n\tv_mov_b32 v50, %2\n\tv_mov_b32 v51, %3" :: "v"(xa[0]), "v"(xa[1]), "v"(xa[2]), "v"(xa[3]));
      seg_p1<RQ, 0, 0>(0, ws, A.lw, sp);
    } else {
      if (DLY_B && rep) __builtin_amdgcn_s_sleep(DLY_B);
      ST(0);
      if (HALF && rep == 0) {
        poll_issue(A.vB, wsL - BOXL);
        LD_KB();
        VMWAIT(4);
        seg_p1<RQ, 2, 0>(A.vB, wsL - BOXL, A.lw, sp);
      } else {
        seg_p1<RQ, 1, 0>(A.vB, wsL - BOXL, A.lw, sp);
      }
      cnt[0] += sp;
    }
    ST(2);
    if (rep == 0 && TYPE == 1) { if (KB_AT == 0) LD_KB(); if (VB_AT == 0) LD_VB(); }
    v4u xa;
    asm volatile("v_mov_b32 %0, v48\n\tv_mov_b32 %1, v49\n\tv_mov_b32 %2, v50\n\tv_mov_b32 %3, v51" : "=v"(xa[0]), "=v"(xa[1]), "=v"(xa[2]), "=v"(xa[3]));
    if (rep == 0) {
      asm volatile("v_mov_b32 v48, %0\n\tv_mov_b32 v49, %0\n\tv_mov_b32 v50, %0\n\tv_mov_b32 v51, %0" :: "s"(ONES));
      seg_p1<RQ, 0, L_REDC - L_RED>(0, ws, A.lw, sp);
    }
    BAR();
    ST(3);
    if (w == FW) {
      seg_final2<false>(A.r2, 0, A.so, box + BOX_Q);
      ST(4);
      OPAQUE_LANE();
      if (rep == 0) {
        const __amdgpu_buffer_rsrc_t rs = __builtin_amdgcn_make_buffer_rsrc(ws, 0, WS_END, 0x00020000);
        const v4f r0 = *(const v4f*)(lds + L_REDC + lane * 32), r1 = *(const v4f*)(lds + L_REDC + lane * 32 + 16);
        const float cs = 0.125f * (((r0[0] + r0[1]) + (r0[2] + r0[3])) + ((r1[0] + r1[1]) + (r1[2] + r1[3])));
        const v4u pc = pack8(cs);
        if (c8 == 0) __builtin_amdgcn_raw_buffer_store_b128(pc, rs, WS_BOX + cu * BOX + BOX_CS + TYPE * 128 + lane * 2, 0, 16);
        if (TYPE == 1) { if (KB_AT == 1) LD_KB(); if (VB_AT == 1) LD_VB(); }
      }
      x_stats(xa, lds, w, lane);
      if (L == 0) {
        for (u32 spins = 0;; ++spins) {
          const u64 f = __hip_atomic_load((GAS u64*)(ws + WS_RDY + b * 512 + lane * 8), __ATOMIC_RELAXED, __HIP_MEMORY_SCOPE_AGENT);
          if (__all(f == flag) || spins > SPIN_CAP) break;
        }
        if (g == 0 && s == 0 && lane == 0) __hip_atomic_store((GAS u32*)(ws + WS_EPOCH + b * 256), (u32)flag, __ATOMIC_RELAXED, __HIP_MEMORY_SCOPE_AGENT);
      } else {
        if (DLY_C && rep) __builtin_amdgcn_s_sleep(DLY_C);
      }
      u32 sq = 0;
      if (rep == 0) {
        const char* wsC = ws + (BOX_CS + TYPE * 128 - BOX_Q);
        qpoll_issue<52>(A.r0, wsL);
        qpoll_issue<60>(A.r0, wsC);
        VMWAIT(0);
        seg_qpoll<0, 52, 1>(A.r0, wsL, A.r1, sq);
        seg_qpoll<L_CSI + TYPE * 1088 - L_Q, 60, 1>(A.r0, wsC, A.r1, sq);
      } else {
        seg_qpoll<0, 52, 0>(A.r0, wsL, A.r1, sq);
      }
      cnt[1] += sq;
      ST(5);
    } else {
      OPAQUE_LANE();
      if (rep == 0 && TYPE == 1) { if (KB_AT == 1) LD_KB(); if (VB_AT == 1) LD_VB(); }
      x_stats(xa, lds, w, lane);
    }
    if (g == 0 && w == s) {
      OPAQUE_LANE();
      float v[8];
      sum_partials(xa, v);
      if (c8 == 0) {
        float* xs = (float*)(lds + L_XS) + 16 * kq + (p ? 8 : 0);
        *(v4f*)xs = v4f{v[0], v[1], v[2], v[3]};
        *(v4f*)(xs + 4) = v4f{v[4], v[5], v[6], v[7]};
      }
    }
    if (rep == 0) {
      VMWAIT(0);
      if (w >= 4) ST(8);
      BAR();
      if (w < 4) LD_WO(RO, cwo);
    }
  }
  if (w < 4) {
    seg_kpre<TYPE * 32768>(A.r0, A.r1);
    BAR();
    ST(7);
    seg_attn<TYPE * 32768>(qaT, A.r6, A.r2, A.r3, A.r4, A.r5, A.r7, A.r8);
    ST(8);
  } else {
    BAR();
    if (rep == 0) {
      STAGGER();
      LD_WO(RO, cwo);
      if (TYPE == 0 && KB_AT == 3) {
        OPAQUE_LANE();
        kv_load(kB, 2 * (w - 4), lane, L_K + 32768);
        kv_load(kB, 2 * (w - 4) + 1, lane, L_K + 32768);
      }
    }
  }
  {
    BAR();
    seg_merge(A.mr, A.orr, A.so, box + w * 128);
    if (w == 7) {
      OPAQUE_LANE();
      const __amdgpu_buffer_rsrc_t rs = __builtin_amdgcn_make_buffer_rsrc(ws, 0, WS_END, 0x00020000);
      const int hh = (lane >> 2) & 3, aa = lane & 3;
      const float m1 = ((const float*)(lds + L_ML))[hh * 4 + aa], l1 = ((const float*)(lds + L_ML))[16 + hh * 4 + aa];
      float M1 = __builtin_fmaxf(m1, dpp<XOR1>(m1)); M1 = __builtin_fmaxf(M1, dpp<XOR2>(M1));
      float L1 = __builtin_amdgcn_exp2f(m1 - M1) * l1; L1 += dpp<XOR1>(L1); L1 += dpp<XOR2>(L1);
      const u32 e = safe(__float_as_uint(M1 + __builtin_amdgcn_logf(L1)));
      const v4u ev = {dppu<0x150>(e), dppu<0x154>(e), dppu<0x158>(e), dppu<0x15C>(e)};
      if (lane < 8) __builtin_amdgcn_raw_buffer_store_b128(ev, rs, WS_LSR + ((L * 32 + grp) * 8 + lane) * 128 + s * 16, 0, 16);
    }
    ST(9);
  }
  {
    const char* wsS = ws + L * 32768;
    u32 sp = 0;
    if (rep == 0) {
      if (TYPE == 0 && KB_AT == 3 && w >= 4) VMWAIT(8); else VMWAIT(0);
      transpose8<RO>();
    } else {
      if (DLY_A) __builtin_amdgcn_s_sleep(DLY_A);
    }
    ST(1);
    if (HALF && rep == 0 && TYPE == 0) {
      seg_p3_issue(A.vA, A.vL, wsL, wsS);
      LD_WQ2(R_WQB, wqB, 0);
      VMWAIT(2);
      seg_p3<RO, 1>(A.vA, A.vL, wsL, wsS, A.lw, sp);
    } else {
      seg_p3<RO, 0>(A.vA, A.vL, wsL, wsS, A.lw, sp);
    }
    cnt[2] += sp;
    ST(10);
    if (rep == 0 && TYPE == 0 && WQB_AT == 0) LD_WQ(R_WQB, wqB);
    BAR();
    if (w == FW) seg_final2<true>(A.r2, A.r3, A.so, box + BOX_X);
    ST(11);
    if (rep == 0 && TYPE == 0) {
      if (HALF) { LD_WQ2(R_WQB, wqB, 2); LD_WQ2(R_WQB, wqB, 4); LD_WQ2(R_WQB, wqB, 6); }
      else if (WQB_AT == 1) { STAGGER(); LD_WQ(R_WQB, wqB); }
      if (KB_AT == 2) LD_KB();
      if (VB_AT == 2) LD_VB();
    }
  }
  STFLUSH(stampv, L * 12);
}

__global__ void __launch_bounds__(512, 2) __attribute__((amdgpu_num_vgpr(24) SGPR_ATTR))
fused_kernel(const char* __restrict__ xin, const char* kA, const char* vA, const char* wqA, const char* woA,
             const char* kB, const char* vB, const char* wqB, const char* woB, unsigned short* out, char* ws) {
  extern __shared__ __attribute__((aligned(16))) char lds[];
  const int id = blockIdx.x, g = id & 7, b = id >> 6, s = (id >> 3) & 7;
  const int tid = threadIdx.x, lane = tid & 63, w = __builtin_amdgcn_readfirstlane(tid >> 6);
  const int c8 = lane & 7, kq = lane >> 4;
  const bool p = (lane >> 3) & 1;
  int stampv = 0;
  const __amdgpu_buffer_rsrc_t rs = __builtin_amdgcn_make_buffer_rsrc(ws, 0, WS_END, 0x00020000);
  const u32 cu = (u32)((b * 8 + g) * 8 + s);
  asm volatile("" ::: "v127", "v255");
  u32 ep; v4u xraw;
  asm volatile("s_nop 4\n\tglobal_load_dword v56, %0, %1 sc1\n\tglobal_load_dwordx4 v[52:55], %2, %3"
               :: "v"(WS_EPOCH + b * 256), "s"(ws), "v"((u32)((b * 512 + 64 * w + 16 * kq + (p ? 8 : 0)) * 2)), "s"(xin) : "memory");
  if (w == 5) {
    const v4u sv = {SENT, SENT, SENT, SENT};
#pragma unroll
    for (int i = 0; i < 5; ++i) __builtin_amdgcn_raw_buffer_store_b128(sv, rs, WS_BOX + cu * BOX + i * 1024 + lane * 16, 0, 16);
    const u32 o6 = lane < 16 ? WS_BOX + cu * BOX + 5120 + lane * 16
                             : WS_LSR + (u32)(((((lane - 16) >> 3) * 32 + b * 8 + g) * 8 + (lane & 7)) * 128 + s * 16);
    if (lane < 48) __builtin_amdgcn_raw_buffer_store_b128(sv, rs, o6, 0, 16);
  }
  if (w == 6) ((float*)(lds + L_XS))[lane] = 0.f;
  const size_t kvoff = ((size_t)(b * 8 + g) * SK + (SK - WIN) + s * 128) * 256;
  const size_t qoff = (size_t)(64 * w) * 8192 + (512 * g + 64 * s) * 2;
  if (STG) { const int r_ = (w - 2 * b) & 7; for (int i_ = 0; i_ < r_; ++i_) __builtin_amdgcn_s_sleep(STG); }
  bulk8<R_WQA, 8192>(wqA + qoff, (u32)(lane >> 3) * 65536 + c8 * 16);
  VMWAIT(8);
  asm volatile("v_mov_b32 %0, v52\n\tv_mov_b32 %1, v53\n\tv_mov_b32 %2, v54\n\tv_mov_b32 %3, v55\n\tv_mov_b32 %4, v56"
               : "=v"(xraw[0]), "=v"(xraw[1]), "=v"(xraw[2]), "=v"(xraw[3]), "=v"(ep));
  ST(6);
  const u32 tag = (u32)__builtin_amdgcn_readfirstlane((int)ep) + 1u;
  const u64 flag = (u64)tag | ((u64)(~tag) << 32);
  if (w == 5 && lane == 0) __hip_atomic_store((GAS u64*)(ws + WS_RDY + b * 512 + (s * 8 + g) * 8), flag, __ATOMIC_RELAXED, __HIP_MEMORY_SCOPE_AGENT);
  asm volatile("s_barrier" ::: "memory");
  kv_load(kA + kvoff, w, lane, L_K);
  kv_load(vA + kvoff, w, lane, L_V);
  Addr A; u32 qa0, qa1;
  {
    const u32 grp = (u32)(b * 8 + g);
    const u32 oct = kq * 32 + (p ? 16 : 0);
    A.vB = WS_BOX + (u32)((b * 8 + c8) * 8 + w) * BOX + BOX_X + oct;
    A.vA = WS_BOX + (grp * 8 + c8) * BOX + w * 128 + oct;
    A.vL = WS_LSR + (grp * 8 + s) * 128 + c8 * 16 + (w >> 1) * 4;
    A.lw = L_RED + (8 * c8 + (lane >> 3)) * 32 + w * 4;
    A.so = lane * 2;
    A.mr = L_ML + (w >> 1) * 16;
    A.orr = L_OM + (w & 1) * 1024 + lane * 16 + (w >> 1) * 4;
    const int r16 = lane & 15, q4 = r16 >> 2, p2 = lane & 3;
    qa0 = L_Q + (lane & 3) * 272 + kq * 16;
    qa1 = qa0;
    if (lane & 4) { qa0 += L_CSI - L_Q; qa1 += L_CSI + 1088 - L_Q; }
    if (w < 4) {
      const u32 kb = L_K + 2048 * (4 * w + (r16 >> 2)) + 64 * (r16 & 3);
      A.r0 = kb + 16 * (kq ^ ((2 * (r16 >> 2)) & 3));
      A.r1 = kb + 256 + 16 * (kq ^ ((2 * (r16 >> 2) + 1) & 3));
      const u32 vb = L_V + 2048 * (4 * w + kq) + 64 * q4 + 8 * (p2 & 1);
      A.r2 = vb + 16 * (((p2 >> 1)) ^ ((2 * kq) & 3));
      A.r3 = vb + 256 + 16 * (((p2 >> 1)) ^ ((2 * kq + 1) & 3));
      A.r4 = vb + 16 * ((2 + (p2 >> 1)) ^ ((2 * kq) & 3));
      A.r5 = vb + 256 + 16 * ((2 + (p2 >> 1)) ^ ((2 * kq + 1) & 3));
      A.r7 = L_OM + w * 2048 + lane * 16;
      A.r8 = L_ML + (lane * 4 + w) * 4;
    } else {
      A.r0 = WS_BOX + (grp * 8 + (lane >> 3)) * BOX + BOX_Q + c8 * 16;
      A.r1 = L_Q + kq * 272 + (lane & 15) * 16;
      A.r2 = L_RED + lane * 32;
      A.r3 = L_XS + lane * 4;
      A.r4 = A.r5 = A.r7 = A.r8 = 0;
    }
    A.r6 = L_ST + c8 * 8;
    PIN(A.r6);
    PIN(A.vB); PIN(A.vA); PIN(A.vL); PIN(A.lw); PIN(A.so); PIN(A.mr); PIN(A.orr); PIN(A.r0); PIN(A.r1); PIN(A.r2); PIN(A.r3); PIN(A.r4); PIN(A.r5); PIN(A.r7); PIN(A.r8);
    PIN(qa0); PIN(qa1);
  }
  u32 cnt[3] = {0, 0, 0};
  for (int rep = 0; rep < 2; ++rep) {
    layer<0>(rep, ws, lds, b, g, s, w, lane, xraw, A, qa0, kB + kvoff, vB + kvoff, wqB, woA, flag, stampv, cnt);
    layer<1>(rep, ws, lds, b, g, s, w, lane, xraw, A, qa1, kB + kvoff, vB + kvoff, wqB, woB, flag, stampv, cnt);
  }
  if (g == 0 && w == s) {
    const u32 voff = WS_BOX + (u32)((b * 8 + c8) * 8 + w) * BOX + 3u * BOXL + BOX_X + kq * 32 + (p ? 16 : 0);
    if (DLY_B) __builtin_amdgcn_s_sleep(DLY_B);
    const v4u x = poll128(ws, voff);
    float v[8];
    sum_partials(x, v);
    if (c8 == 0) *(v4u*)(out + b * 512 + 64 * w + 16 * kq + (p ? 8 : 0)) = v4u{pk(v[0], v[1]), pk(v[2], v[3]), pk(v[4], v[5]), pk(v[6], v[7])};
  }
}

extern "C" void kernel_launch(void* const* d_in, const int* in_sizes, int n_in, void* d_out, int out_size, void* d_ws, size_t ws_size,
                              hipStream_t stream) {
  (void)hipFuncSetAttribute((const void*)fused_kernel, hipFuncAttributeMaxDynamicSharedMemorySize, L_END);
  fused_kernel<<<256, 512, L_END, stream>>>((const char*)d_in[0], (const char*)d_in[1], (const char*)d_in[2], (const char*)d_in[3], (const char*)d_in[4],
                                            (const char*)d_in[6], (const char*)d_in[7], (const char*)d_in[8], (const char*)d_in[9],
                                            (unsigned short*)d_out, (char*)d_ws);
}
```

```cpp
#include <hip/hip_runtime.h>
#include <stdint.h>

typedef unsigned int u32;
typedef unsigned long long u64;
typedef u32 v4u __attribute__((ext_vector_type(4)));
typedef u32 v2u __attribute__((ext_vector_type(2)));
typedef float v4f __attribute__((ext_vector_type(4)));
typedef float v2f __attribute__((ext_vector_type(2)));
typedef __bf16 v8b __attribute__((ext_vector_type(8)));
typedef __bf16 v2b __attribute__((ext_vector_type(2)));
#define GAS __attribute__((address_space(1)))

#define DLY_C 2
#define DLY_A 8
#define DLY_B 12
#define STG 2
#define NTKV " nt"
#define NTW ""

namespace {
constexpr int SK = 4096, WIN = 1024;
constexpr u32 WS_EPOCH = 0;
constexpr u32 WS_RDY = 4096;
constexpr u32 WS_LSR = 8192;
constexpr u32 WS_BOX = WS_LSR + 131072;
constexpr u32 BOXL = 1280, BOX_Q = 1024, BOX_X = 1152, BOX_CS = 5120, BOX = 5376;
constexpr u32 WS_END = WS_BOX + 256 * BOX;
constexpr u32 L_K = 0, L_V = 65536;
constexpr u32 L_OM = 131072;
constexpr u32 L_ML = L_OM + 8192;
constexpr u32 L_Q = L_ML + 128;
constexpr u32 L_CSI = L_Q + 1088;
constexpr u32 L_RED = L_CSI + 2176;
constexpr u32 L_REDC = L_RED + 2048;
constexpr u32 L_ST = L_REDC + 2048;
constexpr u32 L_RED3 = L_ST + 64;
constexpr u32 L_XS = L_RED3 + 2048;
constexpr u32 L_END = L_XS + 256;
static_assert(L_END <= 160 * 1024, "lds");
static_assert(L_Q % 16 == 0 && L_CSI % 16 == 0 && L_RED % 16 == 0 && L_ST % 16 == 0 && L_RED3 % 16 == 0 && L_XS % 16 == 0, "align");
constexpr u32 SENT = 0xFFFFFFFFu;
constexpr u32 SPIN_CAP = 1u << 15;
constexpr float C2 = 0.08838834764831845f * 1.4426950408889634f;
constexpr u32 ONES = 0x3F803F80u;
constexpr u64 PMASK = 0xFF00FF00FF00FF00ull;
constexpr u64 C8Z = 0x0101010101010101ull;

__device__ __forceinline__ float bflo(u32 w) { return __uint_as_float(w << 16); }
__device__ __forceinline__ float bfhi(u32 w) { return __uint_as_float(w & 0xffff0000u); }
__device__ __forceinline__ u32 pk(float lo, float hi) { v2f f = {lo, hi}; return __builtin_bit_cast(u32, __builtin_convertvector(f, v2b)); }
__device__ __forceinline__ u32 safe(u32 v) { return v < 0xFFFFFFFEu ? v : 0xFFFFFFFEu; }
template <int CTRL> __device__ __forceinline__ float dpp(float x) {
  return __builtin_bit_cast(float, __builtin_amdgcn_mov_dpp(__builtin_bit_cast(int, x), CTRL, 0xf, 0xf, true));
}
template <int CTRL> __device__ __forceinline__ u32 dppu(u32 x) { return (u32)__builtin_amdgcn_mov_dpp((int)x, CTRL, 0xf, 0xf, true); }
constexpr int XOR1 = 0xB1, XOR2 = 0x4E, HMIR = 0x141, ROR8 = 0x128;
__device__ __forceinline__ float x16_add(float x) { auto s = __builtin_amdgcn_permlane16_swap(__float_as_uint(x), __float_as_uint(x), false, false); return __uint_as_float(s[0]) + __uint_as_float(s[1]); }
__device__ __forceinline__ float x32_add(float x) { auto s = __builtin_amdgcn_permlane32_swap(__float_as_uint(x), __float_as_uint(x), false, false); return __uint_as_float(s[0]) + __uint_as_float(s[1]); }
__device__ __forceinline__ v4f mfma(v4u a, v4u b, v4f c) {
  return __builtin_amdgcn_mfma_f32_16x16x32_bf16(__builtin_bit_cast(v8b, a), __builtin_bit_cast(v8b, b), c, 0, 0, 0);
}
#define BAR() asm volatile("s_waitcnt lgkmcnt(0)\n\ts_barrier" ::: "memory")
#define VMWAIT(n) asm volatile("s_waitcnt vmcnt(" #n ")" ::: "memory")

constexpr int R_WQA = 128, R_WOA = 160, R_WQB = 192, R_WOB = 224;
template <int BASE, u32 STRIDE>
__device__ __forceinline__ void bulk8(const char* base, u32 voff) {
  asm volatile("" : "+s"(base));
  const __amdgpu_buffer_rsrc_t r = __builtin_amdgcn_make_buffer_rsrc((void*)base, 0, 0x7fffffff, 0x00020000);
  asm volatile("s_nop 4\n\t"
               "buffer_load_dwordx4 v[%c10:%c11], %0, %1, 0 offen" NTW "\n\tbuffer_load_dwordx4 v[%c12:%c13], %0, %1, %3 offen" NTW "\n\t"
               "buffer_load_dwordx4 v[%c14:%c15], %0, %1, %4 offen" NTW "\n\tbuffer_load_dwordx4 v[%c16:%c17], %0, %1, %5 offen" NTW "\n\t"
               "buffer_load_dwordx4 v[%c18:%c19], %0, %1, %6 offen" NTW "\n\tbuffer_load_dwordx4 v[%c20:%c21], %0, %1, %7 offen" NTW "\n\t"
               "buffer_load_dwordx4 v[%c22:%c23], %0, %1, %8 offen" NTW "\n\tbuffer_load_dwordx4 v[%c24:%c25], %0, %1, %9 offen" NTW
               :: "v"(voff), "s"(r), "s"(0), "s"(STRIDE), "s"(2 * STRIDE), "s"(3 * STRIDE), "s"(4 * STRIDE), "s"(5 * STRIDE), "s"(6 * STRIDE), "s"(7 * STRIDE),
                  "i"(BASE), "i"(BASE + 3), "i"(BASE + 4), "i"(BASE + 7), "i"(BASE + 8), "i"(BASE + 11), "i"(BASE + 12), "i"(BASE + 15),
                  "i"(BASE + 16), "i"(BASE + 19), "i"(BASE + 20), "i"(BASE + 23), "i"(BASE + 24), "i"(BASE + 27), "i"(BASE + 28), "i"(BASE + 31)
               : "memory");
}
template <int BASE, u32 STRIDE, int I0>
__device__ __forceinline__ void bulk2(const char* base, u32 voff) {
  asm volatile("" : "+s"(base));
  const __amdgpu_buffer_rsrc_t r = __builtin_amdgcn_make_buffer_rsrc((void*)base, 0, 0x7fffffff, 0x00020000);
  asm volatile("s_nop 4\n\tbuffer_load_dwordx4 v[%c4:%c4+3], %0, %1, %2 offen" NTW "\n\tbuffer_load_dwordx4 v[%c4+4:%c4+7], %0, %1, %3 offen" NTW
               :: "v"(voff), "s"(r), "s"(I0 * STRIDE), "s"((I0 + 1) * STRIDE), "i"(BASE + 4 * I0) : "memory");
}
template <int B, int M, int R>
__device__ __forceinline__ void tp_pair(u32 slo, u32 shi) {
  if constexpr (M == R) {
    asm volatile("v_perm_b32 v100, v%c3, v%c2, %0\n\tv_perm_b32 v%c3, v%c3, v%c2, %1\n\tv_mov_b32 v%c2, v100"
                 :: "s"(slo), "s"(shi), "i"(B + 8 * M + R), "i"(B + 8 * M + 4 + R));
  } else {
    asm volatile("v_perm_b32 v100, v%c3, v%c2, %0\n\tv_perm_b32 v101, v%c3, v%c2, %1\n\t"
                 "v_perm_b32 v%c2, v%c5, v%c4, %0\n\tv_perm_b32 v%c3, v%c5, v%c4, %1\n\t"
                 "v_mov_b32 v%c4, v100\n\tv_mov_b32 v%c5, v101"
                 :: "s"(slo), "s"(shi), "i"(B + 8 * M + R), "i"(B + 8 * M + 4 + R), "i"(B + 8 * R + M), "i"(B + 8 * R + 4 + M));
  }
}
template <int B>
__device__ __forceinline__ void transpose8() {
  const u32 slo = 0x05040100u, shi = 0x07060302u;
  tp_pair<B, 0, 0>(slo, shi); tp_pair<B, 1, 1>(slo, shi); tp_pair<B, 2, 2>(slo, shi); tp_pair<B, 3, 3>(slo, shi);
  tp_pair<B, 0, 1>(slo, shi); tp_pair<B, 0, 2>(slo, shi); tp_pair<B, 0, 3>(slo, shi);
  tp_pair<B, 1, 2>(slo, shi); tp_pair<B, 1, 3>(slo, shi); tp_pair<B, 2, 3>(slo, shi);
  asm volatile("s_nop 1");
}
#define MF(d, b) "v_mfma_f32_16x16x32_bf16 v[" #d ":" #d "+3], v[48:51], v[%c[wb]+" #b ":%c[wb]+" #b "+3], 0\n\t"
#define FOLD_TAIL \
  "v_permlane32_swap_b32 v52, v68\n\t" \
  "v_permlane32_swap_b32 v56, v72\n\t" \
  "v_permlane32_swap_b32 v60, v76\n\t" \
  "v_permlane32_swap_b32 v64, v80\n\t" \
  "v_cndmask_b32_e64 v52, v52, v68, %[pm]\n\t" \
  "v_cndmask_b32_e64 v60, v60, v76, %[pm]\n\t" \
  "v_cndmask_b32_e64 v56, v56, v72, %[pm]\n\t" \
  "v_cndmask_b32_e64 v64, v64, v80, %[pm]\n\t" \
  "v_permlane16_swap_b32 v52, v60\n\t" \
  "s_nop 0\n\t" \
  "v_permlane16_swap_b32 v56, v64\n\t" \
  "v_add_f32 v52, v52, v60\n\t" \
  "v_add_f32 v56, v56, v64\n\t" \
  "s_nop 0\n\t" \
  "v_add_f32_dpp v52, v52, v52 row_ror:8 row_mask:0xf bank_mask:0xf\n\t" \
  "v_add_f32_dpp v56, v56, v56 row_ror:8 row_mask:0xf bank_mask:0xf\n\t" \
  "v_cndmask_b32_e64 v52, v52, v56, %[pm]\n\t"

__device__ __forceinline__ void poll_issue(u32 voff, const char* ws) {
  asm volatile("s_nop 4\n\tglobal_load_dwordx4 v[48:51], %0, %1 sc1" :: "v"(voff), "s"(ws) : "memory");
}
template <int WB, int POLL, int LOFF>
__device__ __forceinline__ void seg_p1(u32 voff, const char* ws, u32 ldsw, u32& spins) {
  if constexpr (POLL != 0) {
    asm volatile(
      ".if %c[pre]\n\ts_branch Lc%=\n\t.endif\n\t"
      "Lp%=:\n\t"
      "global_load_dwordx4 v[48:51], %[voff], %[ws] sc1\n\t"
      "s_add_u32 %[sp], %[sp], 1\n\t"
      "s_waitcnt vmcnt(0)\n\t"
      "Lc%=:\n\t"
      MF(52, 0) "v_max3_u32 v84, v48, v49, v50\n\t"
      MF(56, 4) "v_max_u32 v84, v84, v51\n\t"
      MF(60, 8) "v_cmp_eq_u32 vcc, -1, v84\n\t"
      MF(64, 12)
      "s_cbranch_vccz Lk%=\n\t"
      "s_cmp_lt_u32 %[sp], %[cap]\n\t"
      "s_cbranch_scc1 Lp%=\n\t"
      "Lk%=:\n\t"
      : [sp] "+s"(spins) : [voff] "v"(voff), [ws] "s"(ws), [wb] "i"(WB), [cap] "i"(SPIN_CAP), [pre] "i"(POLL == 2) : "memory", "vcc", "scc");
  } else {
    asm volatile("s_nop 1\n\t" MF(52, 0) "s_nop 0\n\t" MF(56, 4) "s_nop 0\n\t" MF(60, 8) "s_nop 0\n\t" MF(64, 12) "s_nop 0\n\t" :: [wb] "i"(WB));
  }
  asm volatile(
    MF(68, 16) "v_pk_add_f32 v[52:53], v[52:53], v[54:55]\n\t"
    MF(72, 20) "v_pk_add_f32 v[56:57], v[56:57], v[58:59]\n\t"
    MF(76, 24) "v_pk_add_f32 v[60:61], v[60:61], v[62:63]\n\t"
    MF(80, 28) "v_pk_add_f32 v[64:65], v[64:65], v[66:67]\n\t"
    "v_add_f32 v52, v52, v53\n\t"
    "v_pk_add_f32 v[68:69], v[68:69], v[70:71]\n\t"
    "v_add_f32 v56, v56, v57\n\t"
    "v_pk_add_f32 v[72:73], v[72:73], v[74:75]\n\t"
    "v_add_f32 v60, v60, v61\n\t"
    "v_pk_add_f32 v[76:77], v[76:77], v[78:79]\n\t"
    "v_add_f32 v64, v64, v65\n\t"
    "v_pk_add_f32 v[80:81], v[80:81], v[82:83]\n\t"
    "v_add_f32 v68, v68, v69\n\t"
    "v_add_f32 v72, v72, v73\n\t"
    "v_add_f32 v76, v76, v77\n\t"
    "v_add_f32 v80, v80, v81\n\t"
    "s_nop 0\n\t"
    FOLD_TAIL
    "ds_write_b32 %[lw], v52 offset:%c[lo]\n\t"
    :: [wb] "i"(WB), [pm] "s"(PMASK), [lw] "v"(ldsw), [lo] "i"(LOFF) : "memory");
}
__device__ __forceinline__ void seg_p3_issue(u32 voff, u32 voff2, const char* ws, const char* ws2) {
  asm volatile("s_nop 4\n\tglobal_load_dwordx4 v[48:51], %0, %2 sc1\n\tglobal_load_dword v84, %1, %3 sc1" :: "v"(voff), "v"(voff2), "s"(ws), "s"(ws2) : "memory");
}
template <int WB, int PRE>
__device__ __forceinline__ void seg_p3(u32 voff, u32 voff2, const char* ws, const char* ws2, u32 ldsw, u32& spins) {
  asm volatile(
    ".if %c[pre]\n\ts_branch Lc%=\n\t.endif\n\t"
    "Lp%=:\n\t"
    "global_load_dwordx4 v[48:51], %[voff], %[ws] sc1\n\t"
    "global_load_dword v84, %[voff2], %[ws2] sc1\n\t"
    "s_add_u32 %[sp], %[sp], 1\n\t"
    "s_waitcnt vmcnt(0)\n\t"
    "Lc%=:\n\t"
    MF(52, 0) "v_max3_u32 v85, v48, v49, v50\n\t"
    MF(56, 4) "v_max3_u32 v85, v85, v51, v84\n\t"
    MF(60, 8) "v_cmp_eq_u32 vcc, -1, v85\n\t"
    MF(64, 12)
    "s_cbranch_vccz Lk%=\n\t"
    "s_cmp_lt_u32 %[sp], %[cap]\n\t"
    "s_cbranch_scc1 Lp%=\n\t"
    "Lk%=:\n\t"
    "v_max_f32_dpp v85, v84, v84 quad_perm:[1,0,3,2] row_mask:0xf bank_mask:0xf\n\t"
    MF(68, 16)
    "s_nop 0\n\t"
    "v_max_f32_dpp v85, v85, v85 quad_perm:[2,3,0,1] row_mask:0xf bank_mask:0xf\n\t"
    MF(72, 20)
    "s_nop 0\n\t"
    "v_max_f32_dpp v85, v85, v85 row_half_mirror row_mask:0xf bank_mask:0xf\n\t"
    MF(76, 24)
    "v_sub_f32 v86, v84, v85\n\t"
    MF(80, 28)
    "v_exp_f32 v86, v86\n\t"
    "s_nop 1\n\t"
    "v_mov_b32_dpp v90, v86 row_newbcast:0 row_mask:0x5 bank_mask:0xf\n\t"
    "v_mov_b32_dpp v91, v86 row_newbcast:1 row_mask:0x5 bank_mask:0xf\n\t"
    "v_mov_b32_dpp v92, v86 row_newbcast:2 row_mask:0x5 bank_mask:0xf\n\t"
    "v_mov_b32_dpp v93, v86 row_newbcast:3 row_mask:0x5 bank_mask:0xf\n\t"
    "v_mov_b32_dpp v90, v86 row_newbcast:4 row_mask:0xa bank_mask:0xf\n\t"
    "v_mov_b32_dpp v91, v86 row_newbcast:5 row_mask:0xa bank_mask:0xf\n\t"
    "v_mov_b32_dpp v92, v86 row_newbcast:6 row_mask:0xa bank_mask:0xf\n\t"
    "v_mov_b32_dpp v93, v86 row_newbcast:7 row_mask:0xa bank_mask:0xf\n\t"
    "v_add_f32_dpp v87, v86, v86 quad_perm:[1,0,3,2] row_mask:0xf bank_mask:0xf\n\t"
    "v_pk_mul_f32 v[52:53], v[52:53], v[90:91]\n\t"
    "v_pk_mul_f32 v[56:57], v[56:57], v[90:91]\n\t"
    "v_add_f32_dpp v87, v87, v87 quad_perm:[2,3,0,1] row_mask:0xf bank_mask:0xf\n\t"
    "v_pk_mul_f32 v[60:61], v[60:61], v[90:91]\n\t"
    "v_pk_mul_f32 v[64:65], v[64:65], v[90:91]\n\t"
    "v_add_f32_dpp v87, v87, v87 row_half_mirror row_mask:0xf bank_mask:0xf\n\t"
    "v_pk_mul_f32 v[68:69], v[68:69], v[90:91]\n\t"
    "v_pk_mul_f32 v[72:73], v[72:73], v[90:91]\n\t"
    "v_rcp_f32 v87, v87\n\t"
    "v_pk_mul_f32 v[76:77], v[76:77], v[90:91]\n\t"
    "v_pk_mul_f32 v[80:81], v[80:81], v[90:91]\n\t"
    "v_pk_fma_f32 v[52:53], v[54:55], v[92:93], v[52:53]\n\t"
    "v_pk_fma_f32 v[68:69], v[70:71], v[92:93], v[68:69]\n\t"
    "v_pk_fma_f32 v[56:57], v[58:59], v[92:93], v[56:57]\n\t"
    "v_pk_fma_f32 v[72:73], v[74:75], v[92:93], v[72:73]\n\t"
    "v_pk_fma_f32 v[60:61], v[62:63], v[92:93], v[60:61]\n\t"
    "v_pk_fma_f32 v[76:77], v[78:79], v[92:93], v[76:77]\n\t"
    "v_pk_fma_f32 v[64:65], v[66:67], v[92:93], v[64:65]\n\t"
    "v_pk_fma_f32 v[80:81], v[82:83], v[92:93], v[80:81]\n\t"
    "v_add_f32 v52, v52, v53\n\t"
    "v_add_f32 v68, v68, v69\n\t"
    "v_add_f32 v56, v56, v57\n\t"
    "v_add_f32 v72, v72, v73\n\t"
    "v_add_f32 v60, v60, v61\n\t"
    "v_add_f32 v76, v76, v77\n\t"
    "v_add_f32 v64, v64, v65\n\t"
    "v_add_f32 v80, v80, v81\n\t"
    "s_nop 1\n\t"
    FOLD_TAIL
    "v_mul_f32 v52, v52, v87\n\t"
    "ds_write_b32 %[lw], v52 offset:%c[lo]\n\t"
    : [sp] "+s"(spins)
    : [voff] "v"(voff), [voff2] "v"(voff2), [ws] "s"(ws), [ws2] "s"(ws2), [wb] "i"(WB), [cap] "i"(SPIN_CAP), [pm] "s"(PMASK), [lw] "v"(ldsw), [lo] "i"(L_RED3 - L_RED), [pre] "i"(PRE) : "memory", "vcc", "scc");
}
#define PACK_STORE \
  "s_nop 1\n\t" \
  "v_mov_b32_dpp v53, v52 quad_perm:[1,0,3,2] row_mask:0xf bank_mask:0xf\n\t" \
  "v_cvt_pk_bf16_f32 v54, v52, v53\n\t" \
  "v_min_u32 v54, -2, v54\n\t" \
  "s_nop 1\n\t" \
  "v_mov_b32_dpp v55, v54 row_shl:2 row_mask:0xf bank_mask:0xf\n\t" \
  "v_mov_b32_dpp v56, v54 row_shl:4 row_mask:0xf bank_mask:0xf\n\t" \
  "v_mov_b32_dpp v57, v54 row_shl:6 row_mask:0xf bank_mask:0xf\n\t" \
  "s_mov_b64 exec, %[c8z]\n\t" \
  "global_store_dwordx4 %[so], v[54:57], %[ws] sc1\n\t" \
  "s_mov_b64 exec, -1\n\t" \
  "s_nop 1\n\t"
template <bool XS>
__device__ __forceinline__ void seg_final2(u32 ldsr, u32 xsr, u32 so, const char* ws) {
  constexpr u32 LO = XS ? L_RED3 - L_RED : 0;
  if constexpr (XS) {
    asm volatile(
      "ds_read_b128 v[52:55], %[lr] offset:%c[lo]\n\t"
      "ds_read_b128 v[56:59], %[lr] offset:%c[lo]+16\n\t"
      "ds_read_b32 v60, %[xr]\n\t"
      "s_waitcnt lgkmcnt(1)\n\t"
      "v_pk_add_f32 v[52:53], v[52:53], v[56:57]\n\t"
      "v_pk_add_f32 v[54:55], v[54:55], v[58:59]\n\t"
      "v_pk_add_f32 v[52:53], v[52:53], v[54:55]\n\t"
      "s_waitcnt lgkmcnt(0)\n\t"
      "v_add_f32 v52, v52, v53\n\t"
      "v_add_f32 v52, v52, v60\n\t"
      PACK_STORE
      :: [lr] "v"(ldsr), [xr] "v"(xsr), [so] "v"(so), [ws] "s"(ws), [c8z] "s"(C8Z), [lo] "i"(LO) : "memory");
  } else {
    asm volatile(
      "ds_read_b128 v[52:55], %[lr]\n\t"
      "ds_read_b128 v[56:59], %[lr] offset:16\n\t"
      "s_waitcnt lgkmcnt(0)\n\t"
      "v_pk_add_f32 v[52:53], v[52:53], v[56:57]\n\t"
      "v_pk_add_f32 v[54:55], v[54:55], v[58:59]\n\t"
      "v_pk_add_f32 v[52:53], v[52:53], v[54:55]\n\t"
      "v_add_f32 v52, v52, v53\n\t"
      PACK_STORE
      :: [lr] "v"(ldsr), [so] "v"(so), [ws] "s"(ws), [c8z] "s"(C8Z) : "memory");
  }
}
template <int RB>
__device__ __forceinline__ void qpoll_issue(u32 voff, const char* ws) {
  asm volatile("s_nop 4\n\tglobal_load_dwordx4 v[%c2:%c2+3], %0, %1 sc1" :: "v"(voff), "s"(ws), "i"(RB) : "memory");
}
template <int LOFF, int RB, int PRE>
__device__ __forceinline__ void seg_qpoll(u32 voff, const char* ws, u32 ldsw, u32& spins) {
  asm volatile(
    ".if %c[pre]\n\ts_branch Lc%=\n\t.endif\n\t"
    "Lp%=:\n\t"
    "global_load_dwordx4 v[%c[rb]:%c[rb]+3], %[voff], %[ws] sc1\n\t"
    "s_add_u32 %[sp], %[sp], 1\n\t"
    "s_waitcnt vmcnt(0)\n\t"
    "Lc%=:\n\t"
    "v_max3_u32 v70, v%c[rb], v[%c[rb]+1], v[%c[rb]+2]\n\t"
    "v_max_u32 v70, v70, v[%c[rb]+3]\n\t"
    "v_cmp_eq_u32 vcc, -1, v70\n\t"
    "ds_write_b128 %[lw], v[%c[rb]:%c[rb]+3] offset:%c[lo]\n\t"
    "s_cbranch_vccz Lk%=\n\t"
    "s_cmp_lt_u32 %[sp], %[cap]\n\t"
    "s_cbranch_scc1 Lp%=\n\t"
    "Lk%=:\n\t"
    : [sp] "+s"(spins) : [voff] "v"(voff), [ws] "s"(ws), [lw] "v"(ldsw), [cap] "i"(SPIN_CAP), [lo] "i"(LOFF), [rb] "i"(RB), [pre] "i"(PRE) : "memory", "vcc", "scc");
}
template <int TO>
__device__ __forceinline__ void seg_kpre(u32 ka0, u32 ka1) {
  asm volatile(
    "ds_read_b128 v[48:51], %0 offset:%c2\n\tds_read_b128 v[64:67], %1 offset:%c2\n\t"
    "ds_read_b128 v[52:55], %0 offset:%c2+512\n\tds_read_b128 v[68:71], %1 offset:%c2+512\n\t"
    "ds_read_b128 v[56:59], %0 offset:%c2+1024\n\tds_read_b128 v[72:75], %1 offset:%c2+1024\n\t"
    "ds_read_b128 v[60:63], %0 offset:%c2+1536\n\tds_read_b128 v[76:79], %1 offset:%c2+1536\n\t"
    "v_mov_b32 v96, 0\n\tv_mov_b32 v97, 0\n\tv_mov_b32 v98, 0\n\tv_mov_b32 v99, 0\n\t"
    "v_mov_b32 v100, 0\n\tv_mov_b32 v101, 0\n\tv_mov_b32 v102, 0\n\tv_mov_b32 v103, 0\n\t"
    "v_mov_b32 v104, 0\n\tv_mov_b32 v105, 0\n\tv_mov_b32 v106, 0\n\tv_mov_b32 v107, 0\n\t"
    "v_mov_b32 v108, 0\n\tv_mov_b32 v109, 0\n\tv_mov_b32 v110, 0\n\tv_mov_b32 v111, 0\n\t"
    :: "v"(ka0), "v"(ka1), "i"(TO) : "memory");
}
#define QK(s, k, q, c) "v_mfma_f32_16x16x32_bf16 v[" #s ":" #s "+3], v[" #k ":" #k "+3], v[" #q ":" #q "+3], " c "\n\t"
#define TR(d, a, off) "ds_read_b64_tr_b16 v[" #d ":" #d "+1], %[" #a "] offset:%c[to]+" #off "\n\t"
#define PV(r) "v_mfma_f32_16x16x32_bf16 v[" #r ":" #r "+3], v[124:127], v[" #r ":" #r "+3], 0\n\t"
#define SM(r) "v_fma_f32 v" #r ", v" #r ", v99, v121\n\tv_exp_f32 v" #r ", v" #r "\n\t"
#define PVA(acc, a, v, c) "v_mfma_f32_16x16x32_bf16 v[" #acc ":" #acc "+3], v[" #a ":" #a "+3], v[" #v ":" #v "+3], " c "\n\t"
#define PCP(d, s, ctl, bank) "v_mov_b32_dpp v" #d ", v" #s " " ctl " row_mask:0xf bank_mask:" bank "\n\t"
template <int TO>
__device__ __forceinline__ void seg_attn(u32 qa, u32 sta, u32 va0, u32 va1, u32 va2, u32 va3, u32 oa, u32 mla) {
  asm volatile(
    "ds_read_b128 v[80:83], %[qa]\n\t"
    "ds_read_b128 v[84:87], %[qa] offset:64\n\t"
    "ds_read_b128 v[88:91], %[qa] offset:128\n\t"
    "ds_read_b128 v[92:95], %[qa] offset:192\n\t"
    "ds_read_b64 v[120:121], %[sta]\n\t"
    "s_waitcnt lgkmcnt(4)\n\t"
    QK(112, 48, 80, "0") QK(116, 64, 80, "0")
    "s_waitcnt lgkmcnt(3)\n\t"
    QK(112, 52, 84, "v[112:115]") QK(116, 68, 84, "v[116:119]")
    "s_waitcnt lgkmcnt(2)\n\t"
    QK(112, 56, 88, "v[112:115]") QK(116, 72, 88, "v[116:119]")
    "s_waitcnt lgkmcnt(0)\n\t"
    QK(112, 60, 92, "v[112:115]")
    "v_add_f32_dpp v120, v120, v120 quad_perm:[1,0,3,2] row_mask:0xf bank_mask:0xf\n\t"
    "v_add_f32_dpp v121, v121, v121 quad_perm:[1,0,3,2] row_mask:0xf bank_mask:0xf\n\t"
    QK(116, 76, 92, "v[116:119]")
    TR(48, va0, 0)
    "v_add_f32_dpp v120, v120, v120 quad_perm:[2,3,0,1] row_mask:0xf bank_mask:0xf\n\t"
    "v_add_f32_dpp v121, v121, v121 quad_perm:[2,3,0,1] row_mask:0xf bank_mask:0xf\n\t"
    TR(50, va1, 0) TR(52, va2, 0)
    "v_add_f32_dpp v120, v120, v120 row_half_mirror row_mask:0xf bank_mask:0xf\n\t"
    "v_add_f32_dpp v121, v121, v121 row_half_mirror row_mask:0xf bank_mask:0xf\n\t"
    TR(54, va3, 0)
    "v_mul_f32 v122, 0xbb000000, v120\n\t"
    "v_mul_f32 v123, 0x3b000000, v121\n\t"
    "v_fma_f32 v123, -v122, v122, v123\n\t"
    "v_fmac_f32_dpp v112, v112, v122 row_shl:4 row_mask:0xf bank_mask:0xf bound_ctrl:1\n\t"
    "v_max_f32 v123, 0, v123\n\t"
    "v_fmac_f32_dpp v113, v113, v122 row_shl:4 row_mask:0xf bank_mask:0xf bound_ctrl:1\n\t"
    "v_add_f32 v123, 0x3727c5ac, v123\n\t"
    "v_fmac_f32_dpp v114, v114, v122 row_shl:4 row_mask:0xf bank_mask:0xf bound_ctrl:1\n\t"
    "v_rsq_f32 v123, v123\n\t"
    "v_fmac_f32_dpp v115, v115, v122 row_shl:4 row_mask:0xf bank_mask:0xf bound_ctrl:1\n\t"
    "v_fmac_f32_dpp v116, v116, v122 row_shl:4 row_mask:0xf bank_mask:0xf bound_ctrl:1\n\t"
    "v_max3_f32 v120, v112, v113, v114\n\t"
    "v_fmac_f32_dpp v117, v117, v122 row_shl:4 row_mask:0xf bank_mask:0xf bound_ctrl:1\n\t"
    "v_fmac_f32_dpp v118, v118, v122 row_shl:4 row_mask:0xf bank_mask:0xf bound_ctrl:1\n\t"
    "v_fmac_f32_dpp v119, v119, v122 row_shl:4 row_mask:0xf bank_mask:0xf bound_ctrl:1\n\t"
    "v_max3_f32 v120, v120, v115, v116\n\t"
    "v_mul_f32 v123, %[c2], v123\n\t"
    "v_max3_f32 v120, v120, v117, v118\n\t"
    "v_max_f32 v120, v120, v119\n\t"
    "v_mov_b32 v121, v120\n\t"
    TR(56, va0, 512) TR(58, va1, 512)
    "v_permlane16_swap_b32 v120, v121\n\t"
    "v_max_f32 v120, v120, v121\n\t"
    "v_mov_b32 v121, v120\n\t"
    TR(60, va2, 512) TR(62, va3, 512)
    "v_permlane32_swap_b32 v120, v121\n\t"
    "v_max_f32 v120, v120, v121\n\t"
    "v_mul_f32_e64 v121, -v120, v123\n\t"
    "v_mul_f32 v122, v120, v123\n\t"
    "v_fma_f32 v112, v112, v123, v121\n\t"
    "v_fma_f32 v113, v113, v123, v121\n\t"
    "v_exp_f32 v112, v112\n\t"
    TR(64, va0, 1024)
    "v_fma_f32 v114, v114, v123, v121\n\t"
    "v_exp_f32 v113, v113\n\t"
    TR(66, va1, 1024)
    "v_fma_f32 v115, v115, v123, v121\n\t"
    "v_exp_f32 v114, v114\n\t"
    TR(68, va2, 1024)
    "v_fma_f32 v116, v116, v123, v121\n\t"
    "v_exp_f32 v115, v115\n\t"
    TR(70, va3, 1024)
    "v_fma_f32 v117, v117, v123, v121\n\t"
    "v_exp_f32 v116, v116\n\t"
    "v_cvt_pk_bf16_f32 v124, v112, v113\n\t"
    "v_fma_f32 v118, v118, v123, v121\n\t"
    "v_exp_f32 v117, v117\n\t"
    TR(72, va0, 1536)
    "v_fma_f32 v119, v119, v123, v121\n\t"
    "v_exp_f32 v118, v118\n\t"
    "v_cvt_pk_bf16_f32 v125, v114, v115\n\t"
    "v_exp_f32 v119, v119\n\t"
    TR(74, va1, 1536) TR(76, va2, 1536) TR(78, va3, 1536)
    "v_cvt_pk_bf16_f32 v126, v116, v117\n\t"
    "v_cvt_pk_bf16_f32 v127, v118, v119\n\t"
    PCP(96, 124, "quad_perm:[0,1,2,3]", "0x1") PCP(100, 124, "row_shr:4", "0x2") PCP(104, 124, "quad_perm:[0,1,2,3]", "0x4") PCP(108, 124, "row_shr:4", "0x8")
    PCP(97, 125, "quad_perm:[0,1,2,3]", "0x1") PCP(101, 125, "row_shr:4", "0x2") PCP(105, 125, "quad_perm:[0,1,2,3]", "0x4") PCP(109, 125, "row_shr:4", "0x8")
    PCP(98, 126, "quad_perm:[0,1,2,3]", "0x1") PCP(102, 126, "row_shr:4", "0x2") PCP(106, 126, "quad_perm:[0,1,2,3]", "0x4") PCP(110, 126, "row_shr:4", "0x8")
    PCP(99, 127, "quad_perm:[0,1,2,3]", "0x1") PCP(103, 127, "row_shr:4", "0x2") PCP(107, 127, "quad_perm:[0,1,2,3]", "0x4") PCP(111, 127, "row_shr:4", "0x8")
    "v_mov_b32 v84, %[ones]\n\tv_mov_b32 v85, %[ones]\n\tv_mov_b32 v86, %[ones]\n\tv_mov_b32 v87, %[ones]\n\t"
    "s_waitcnt lgkmcnt(0)\n\t"
    PVA(112, 96, 48, "0") PVA(116, 96, 64, "0")
    PVA(112, 100, 52, "v[112:115]") PVA(116, 100, 68, "v[116:119]")
    PVA(112, 104, 56, "v[112:115]") PVA(116, 104, 72, "v[116:119]")
    PVA(112, 108, 60, "v[112:115]") PVA(116, 108, 76, "v[116:119]")
    "v_mfma_f32_16x16x32_bf16 v[80:83], v[84:87], v[124:127], 0\n\t"
    "s_nop 6\n\t"
    "ds_write_b128 %[oa], v[112:115]\n\t"
    "ds_write_b128 %[oa], v[116:119] offset:1024\n\t"
    "s_mov_b64 exec, 0xf\n\t"
    "ds_write2_b32 %[mla], v122, v80 offset0:0 offset1:16\n\t"
    "s_mov_b64 exec, -1\n\t"
    :: [qa] "v"(qa), [sta] "v"(sta), [va0] "v"(va0), [va1] "v"(va1), [va2] "v"(va2), [va3] "v"(va3), [oa] "v"(oa), [mla] "v"(mla),
       [c2] "s"(C2), [ones] "s"(ONES), [to] "i"(TO) : "memory");
}
__device__ __forceinline__ void seg_merge(u32 mla, u32 oa, u32 so, const char* ws) {
  asm volatile(
    "ds_read_b128 v[48:51], %[mla]\n\t"
    "ds_read_b128 v[60:63], %[mla] offset:64\n\t"
    "ds_read2st64_b32 v[56:57], %[oa] offset0:0 offset1:8\n\t"
    "ds_read2st64_b32 v[58:59], %[oa] offset0:16 offset1:24\n\t"
    "s_waitcnt lgkmcnt(3)\n\t"
    "v_max3_f32 v52, v48, v49, v50\n\t"
    "v_max_f32 v52, v52, v51\n\t"
    "v_sub_f32 v48, v48, v52\n\t"
    "v_sub_f32 v49, v49, v52\n\t"
    "v_exp_f32 v48, v48\n\t"
    "v_sub_f32 v50, v50, v52\n\t"
    "v_exp_f32 v49, v49\n\t"
    "v_sub_f32 v51, v51, v52\n\t"
    "v_exp_f32 v50, v50\n\t"
    "v_exp_f32 v51, v51\n\t"
    "s_waitcnt lgkmcnt(0)\n\t"
    "v_pk_mul_f32 v[60:61], v[60:61], v[48:49]\n\t"
    "v_pk_mul_f32 v[56:57], v[56:57], v[48:49]\n\t"
    "v_pk_fma_f32 v[60:61], v[62:63], v[50:51], v[60:61]\n\t"
    "v_pk_fma_f32 v[56:57], v[58:59], v[50:51], v[56:57]\n\t"
    "v_add_f32 v60, v60, v61\n\t"
    "v_add_f32 v56, v56, v57\n\t"
    "v_rcp_f32 v60, v60\n\t"
    "s_nop 0\n\t"
    "v_mul_f32 v52, v56, v60\n\t"
    PACK_STORE
    :: [mla] "v"(mla), [oa] "v"(oa), [so] "v"(so), [ws] "s"(ws), [c8z] "s"(C8Z) : "memory");
}

__device__ __forceinline__ void dma16(u32 voff, const char* sbase, u32 lds_dst) {
  u32 keep;
  asm volatile("s_mov_b32 %0, m0\n\ts_mov_b32 m0, %3\n\ts_nop 0\n\tglobal_load_lds_dwordx4 %1, %2" NTKV "\n\ts_mov_b32 m0, %0"
               : "=&s"(keep) : "v"(voff), "s"(sbase), "s"(lds_dst) : "memory");
}
__device__ __forceinline__ void kv_load(const char* base, int w, int lane, u32 ldsb) {
  asm volatile("" : "+s"(base));
  const int r8 = (lane & 31) >> 2;
#pragma unroll
  for (int i = 0; i < 4; ++i) {
    const u32 ch = 8 * (i & 1) + 4 * (lane >> 5) + ((lane & 3) ^ ((2 * (i >> 1) + (r8 >> 2)) & 3));
    dma16(r8 * 256 + ch * 16, base + (size_t)(16 * w + 8 * (i >> 1)) * 256, ldsb + w * 4096 + i * 1024);
  }
}
__device__ __forceinline__ bool has_sent(v4u x) {
  const u32 m = __builtin_elementwise_max(__builtin_elementwise_max(x[0], x[1]), __builtin_elementwise_max(x[2], x[3]));
  return __builtin_amdgcn_ballot_w64(m == SENT) != 0ull;
}
__device__ __forceinline__ v4u poll128(const char* base, u32 voff) {
  v4u x;
  for (u32 spins = 0;; ++spins) {
    asm volatile("s_nop 4\n\tglobal_load_dwordx4 %0, %1, %2 sc1\n\ts_waitcnt vmcnt(0)" : "=&v"(x) : "v"(voff), "s"(base) : "memory");
    if (!has_sent(x) || spins > SPIN_CAP) break;
  }
  return x;
}
__device__ __forceinline__ v4u pack8(float v) {
  const u32 x = safe(pk(v, dpp<XOR1>(v)));
  return v4u{x, dppu<0x102>(x), dppu<0x104>(x), dppu<0x106>(x)};
}
__device__ __forceinline__ void sum_partials(v4u x, float (&v)[8]) {
#pragma unroll
  for (int m = 0; m < 4; ++m) { v[2 * m] = bflo(x[m]); v[2 * m + 1] = bfhi(x[m]); }
#pragma unroll
  for (int j = 0; j < 8; ++j) v[j] += dpp<XOR1>(v[j]);
#pragma unroll
  for (int j = 0; j < 8; ++j) v[j] += dpp<XOR2>(v[j]);
#pragma unroll
  for (int j = 0; j < 8; ++j) v[j] += dpp<HMIR>(v[j]);
}
__device__ __forceinline__ void x_stats(v4u xa, char* lds, int w, int lane) {
  const bool p = (lane >> 3) & 1; const int kq = lane >> 4;
  const v4f gm = mfma(xa, xa, v4f{0.f, 0.f, 0.f, 0.f});
  const v4f sm = mfma(xa, v4u{ONES, ONES, ONES, ONES}, v4f{0.f, 0.f, 0.f, 0.f});
  float s2 = (gm[0] + gm[1]) + (gm[2] + gm[3]);
  s2 = (p == (bool)(kq >> 1)) ? s2 : 0.f;
  s2 += dpp<XOR1>(s2); s2 += dpp<XOR2>(s2); s2 += dpp<HMIR>(s2); s2 += dpp<ROR8>(s2);
  s2 = x32_add(x16_add(s2));
  float s1 = (sm[0] + sm[1]) + (sm[2] + sm[3]);
  s1 = x32_add(x16_add(s1));
  if (lane == 0) *(v2f*)(lds + L_ST + w * 8) = v2f{s1, s2};
}
}

#define OPAQUE_LANE() int lane = lane0; asm volatile("" : "+v"(lane)); const int c8 = lane & 7, kq = lane >> 4; const bool p = (lane >> 3) & 1; (void)c8; (void)p; (void)kq
#define OPAQUE_S(x) asm volatile("" : "+s"(x))

constexpr int FW = 4;

struct Addr {
  u32 vB, vA, vL, lw, so;
  u32 mr, orr;
  u32 r0, r1, r2, r3, r4, r5, r6, r7, r8;
};
#define PIN(x) asm volatile("" : "+v"(x))

template <int TYPE>
__device__ __forceinline__ void layer(const int rep, char* ws, char* lds, int b, int g, int s, int w, const int lane0, v4u xraw, Addr& A, u32 qaT,
                                      const char* vA0, const char* kB, const char* vB, const char* wqB, const char* cwo, const u64 flag) {
  constexpr int RQ = TYPE ? R_WQB : R_WQA, RO = TYPE ? R_WOB : R_WOA;
  const int L = rep * 2 + TYPE;
  OPAQUE_S(b); OPAQUE_S(g); OPAQUE_S(s); OPAQUE_S(w);
  const u32 grp = (u32)(b * 8 + g), cu = grp * 8 + s;
#define STAGGER() do { if (STG) { const int r_ = (w - 2 * b) & 7; for (int i_ = 0; i_ < r_; ++i_) __builtin_amdgcn_s_sleep(STG); } } while (0)
#define LD_WO(R, ptr) do { OPAQUE_LANE(); const char* p_ = ptr; OPAQUE_S(p_); bulk8<R, 1024>(p_ + (size_t)(512 * g + 64 * w) * 1024 + 128 * s, (u32)(lane >> 3) * 8192 + c8 * 16); } while (0)
#define LD_WQ2(R, ptr, I0) do { OPAQUE_LANE(); const char* p_ = ptr; OPAQUE_S(p_); bulk2<R, 8192, I0>(p_ + (size_t)(64 * w) * 8192 + (512 * g + 64 * s) * 2, (u32)(lane >> 3) * 65536 + c8 * 16); } while (0)
#define LD_KB() do { OPAQUE_LANE(); kv_load(kB, w, lane, L_K + 32768); } while (0)
#define LD_VB() do { OPAQUE_LANE(); kv_load(vB, w, lane, L_V + 32768); } while (0)
  const char* wsL = ws + L * BOXL;
  const char* box = ws + WS_BOX + cu * BOX + L * BOXL;
  {
    u32 sp = 0;
    if (rep == 0) {
      if (TYPE == 0) VMWAIT(4); else VMWAIT(0);
      transpose8<RQ>();
    }
    if (L == 0) {
      const v4u xa = (lane0 & 7) == 0 ? xraw : v4u{0u, 0u, 0u, 0u};
      asm volatile("v_mov_b32 v48, %0\n\tv_mov_b32 v49, %1\n\tv_mov_b32 v50, %2\n\tv_mov_b32 v51, %3" :: "v"(xa[0]), "v"(xa[1]), "v"(xa[2]), "v"(xa[3]));
      seg_p1<RQ, 0, 0>(0, ws, A.lw, sp);
    } else {
      if (DLY_B && rep) __builtin_amdgcn_s_sleep(DLY_B);
      if (rep == 0) {
        poll_issue(A.vB, wsL - BOXL);
        LD_KB();
        VMWAIT(4);
        seg_p1<RQ, 2, 0>(A.vB, wsL - BOXL, A.lw, sp);
      } else {
        seg_p1<RQ, 1, 0>(A.vB, wsL - BOXL, A.lw, sp);
      }
    }
    if (L == 0) { OPAQUE_LANE(); kv_load(vA0, w, lane, L_V); }
    v4u xa;
    asm volatile("v_mov_b32 %0, v48\n\tv_mov_b32 %1, v49\n\tv_mov_b32 %2, v50\n\tv_mov_b32 %3, v51" : "=v"(xa[0]), "=v"(xa[1]), "=v"(xa[2]), "=v"(xa[3]));
    if (rep == 0) {
      asm volatile("v_mov_b32 v48, %0\n\tv_mov_b32 v49, %0\n\tv_mov_b32 v50, %0\n\tv_mov_b32 v51, %0" :: "s"(ONES));
      seg_p1<RQ, 0, L_REDC - L_RED>(0, ws, A.lw, sp);
    }
    BAR();
    if (w == FW) {
      seg_final2<false>(A.r2, 0, A.so, box + BOX_Q);
      OPAQUE_LANE();
      if (rep == 0) {
        const __amdgpu_buffer_rsrc_t rs = __builtin_amdgcn_make_buffer_rsrc(ws, 0, WS_END, 0x00020000);
        const v4f r0 = *(const v4f*)(lds + L_REDC + lane * 32), r1 = *(const v4f*)(lds + L_REDC + lane * 32 + 16);
        const float cs = 0.125f * (((r0[0] + r0[1]) + (r0[2] + r0[3])) + ((r1[0] + r1[1]) + (r1[2] + r1[3])));
        const v4u pc = pack8(cs);
        if (c8 == 0) __builtin_amdgcn_raw_buffer_store_b128(pc, rs, WS_BOX + cu * BOX + BOX_CS + TYPE * 128 + lane * 2, 0, 16);
        if (TYPE == 1) LD_VB();
      }
      x_stats(xa, lds, w, lane);
      if (L == 0) {
        for (u32 spins = 0;; ++spins) {
          const u64 f = __hip_atomic_load((GAS u64*)(ws + WS_RDY + b * 512 + lane * 8), __ATOMIC_RELAXED, __HIP_MEMORY_SCOPE_AGENT);
          if (__all(f == flag) || spins > SPIN_CAP) break;
        }
        if (g == 0 && s == 0 && lane == 0) __hip_atomic_store((GAS u32*)(ws + WS_EPOCH + b * 256), (u32)flag, __ATOMIC_RELAXED, __HIP_MEMORY_SCOPE_AGENT);
      } else {
        if (DLY_C && rep) __builtin_amdgcn_s_sleep(DLY_C);
      }
      u32 sq = 0;
      if (rep == 0) {
        const char* wsC = ws + (BOX_CS + TYPE * 128 - BOX_Q);
        qpoll_issue<52>(A.r0, wsL);
        qpoll_issue<60>(A.r0, wsC);
        VMWAIT(0);
        seg_qpoll<0, 52, 1>(A.r0, wsL, A.r1, sq);
        seg_qpoll<L_CSI + TYPE * 1088 - L_Q, 60, 1>(A.r0, wsC, A.r1, sq);
      } else {
        seg_qpoll<0, 52, 0>(A.r0, wsL, A.r1, sq);
      }
    } else {
      OPAQUE_LANE();
      if (rep == 0 && TYPE == 1) LD_VB();
      x_stats(xa, lds, w, lane);
    }
    if (g == 0 && w == s) {
      OPAQUE_LANE();
      float v[8];
      sum_partials(xa, v);
      if (c8 == 0) {
        float* xs = (float*)(lds + L_XS) + 16 * kq + (p ? 8 : 0);
        *(v4f*)xs = v4f{v[0], v[1], v[2], v[3]};
        *(v4f*)(xs + 4) = v4f{v[4], v[5], v[6], v[7]};
      }
    }
    if (rep == 0) {
      VMWAIT(0);
      BAR();
      if (w < 4) LD_WO(RO, cwo);
    }
  }
  if (w < 4) {
    seg_kpre<TYPE * 32768>(A.r0, A.r1);
    BAR();
    seg_attn<TYPE * 32768>(qaT, A.r6, A.r2, A.r3, A.r4, A.r5, A.r7, A.r8);
  } else {
    BAR();
    if (rep == 0) {
      STAGGER();
      LD_WO(RO, cwo);
    }
  }
  {
    BAR();
    seg_merge(A.mr, A.orr, A.so, box + w * 128);
    if (w == 7) {
      OPAQUE_LANE();
      const __amdgpu_buffer_rsrc_t rs = __builtin_amdgcn_make_buffer_rsrc(ws, 0, WS_END, 0x00020000);
      const int hh = (lane >> 2) & 3, aa = lane & 3;
      const float m1 = ((const float*)(lds + L_ML))[hh * 4 + aa], l1 = ((const float*)(lds + L_ML))[16 + hh * 4 + aa];
      float M1 = __builtin_fmaxf(m1, dpp<XOR1>(m1)); M1 = __builtin_fmaxf(M1, dpp<XOR2>(M1));
      float L1 = __builtin_amdgcn_exp2f(m1 - M1) * l1; L1 += dpp<XOR1>(L1); L1 += dpp<XOR2>(L1);
      const u32 e = safe(__float_as_uint(M1 + __builtin_amdgcn_logf(L1)));
      const v4u ev = {dppu<0x150>(e), dppu<0x154>(e), dppu<0x158>(e), dppu<0x15C>(e)};
      if (lane < 8) __builtin_amdgcn_raw_buffer_store_b128(ev, rs, WS_LSR + ((L * 32 + grp) * 8 + lane) * 128 + s * 16, 0, 16);
    }
  }
  {
    const char* wsS = ws + L * 32768;
    u32 sp = 0;
    if (rep == 0) {
      VMWAIT(0);
      transpose8<RO>();
    } else {
      if (DLY_A) __builtin_amdgcn_s_sleep(DLY_A);
    }
    if (rep == 0 && TYPE == 0) {
      seg_p3_issue(A.vA, A.vL, wsL, wsS);
      LD_WQ2(R_WQB, wqB, 0);
      VMWAIT(2);
      seg_p3<RO, 1>(A.vA, A.vL, wsL, wsS, A.lw, sp);
    } else {
      seg_p3<RO, 0>(A.vA, A.vL, wsL, wsS, A.lw, sp);
    }
    BAR();
    if (w == FW) seg_final2<true>(A.r2, A.r3, A.so, box + BOX_X);
    if (rep == 0 && TYPE == 0) { LD_WQ2(R_WQB, wqB, 2); LD_WQ2(R_WQB, wqB, 4); LD_WQ2(R_WQB, wqB, 6); }
  }
}

__global__ void __launch_bounds__(512, 2) __attribute__((amdgpu_num_vgpr(24)))
fused_kernel(const char* __restrict__ xin, const char* kA, const char* vA, const char* wqA, const char* woA,
             const char* kB, const char* vB, const char* wqB, const char* woB, unsigned short* out, char* ws) {
  extern __shared__ __attribute__((aligned(16))) char lds[];
  const int id = blockIdx.x, g = id & 7, b = id >> 6, s = (id >> 3) & 7;
  const int tid = threadIdx.x, lane = tid & 63, w = __builtin_amdgcn_readfirstlane(tid >> 6);
  const int c8 = lane & 7, kq = lane >> 4;
  const bool p = (lane >> 3) & 1;
  const __amdgpu_buffer_rsrc_t rs = __builtin_amdgcn_make_buffer_rsrc(ws, 0, WS_END, 0x00020000);
  const u32 cu = (u32)((b * 8 + g) * 8 + s);
  asm volatile("" ::: "v127", "v255");
  u32 ep; v4u xraw;
  asm volatile("s_nop 4\n\tglobal_load_dword v56, %0, %1 sc1\n\tglobal_load_dwordx4 v[52:55], %2, %3"
               :: "v"(WS_EPOCH + b * 256), "s"(ws), "v"((u32)((b * 512 + 64 * w + 16 * kq + (p ? 8 : 0)) * 2)), "s"(xin) : "memory");
  if (w == 5) {
    const v4u sv = {SENT, SENT, SENT, SENT};
#pragma unroll
    for (int i = 0; i < 5; ++i) __builtin_amdgcn_raw_buffer_store_b128(sv, rs, WS_BOX + cu * BOX + i * 1024 + lane * 16, 0, 16);
    const u32 o6 = lane < 16 ? WS_BOX + cu * BOX + 5120 + lane * 16
                             : WS_LSR + (u32)(((((lane - 16) >> 3) * 32 + b * 8 + g) * 8 + (lane & 7)) * 128 + s * 16);
    if (lane < 48) __builtin_amdgcn_raw_buffer_store_b128(sv, rs, o6, 0, 16);
  }
  if (w == 6) ((float*)(lds + L_XS))[lane] = 0.f;
  const size_t kvoff = ((size_t)(b * 8 + g) * SK + (SK - WIN) + s * 128) * 256;
  const size_t qoff = (size_t)(64 * w) * 8192 + (512 * g + 64 * s) * 2;
  if (STG) { const int r_ = (w - 2 * b) & 7; for (int i_ = 0; i_ < r_; ++i_) __builtin_amdgcn_s_sleep(STG); }
  bulk8<R_WQA, 8192>(wqA + qoff, (u32)(lane >> 3) * 65536 + c8 * 16);
  VMWAIT(8);
  asm volatile("v_mov_b32 %0, v52\n\tv_mov_b32 %1, v53\n\tv_mov_b32 %2, v54\n\tv_mov_b32 %3, v55\n\tv_mov_b32 %4, v56"
               : "=v"(xraw[0]), "=v"(xraw[1]), "=v"(xraw[2]), "=v"(xraw[3]), "=v"(ep));
  const u32 tag = (u32)__builtin_amdgcn_readfirstlane((int)ep) + 1u;
  const u64 flag = (u64)tag | ((u64)(~tag) << 32);
  if (w == 5 && lane == 0) __hip_atomic_store((GAS u64*)(ws + WS_RDY + b * 512 + (s * 8 + g) * 8), flag, __ATOMIC_RELAXED, __HIP_MEMORY_SCOPE_AGENT);
  asm volatile("s_barrier" ::: "memory");
  kv_load(kA + kvoff, w, lane, L_K);
  Addr A; u32 qa0, qa1;
  {
    const u32 grp = (u32)(b * 8 + g);
    const u32 oct = kq * 32 + (p ? 16 : 0);
    A.vB = WS_BOX + (u32)((b * 8 + c8) * 8 + w) * BOX + BOX_X + oct;
    A.vA = WS_BOX + (grp * 8 + c8) * BOX + w * 128 + oct;
    A.vL = WS_LSR + (grp * 8 + s) * 128 + c8 * 16 + (w >> 1) * 4;
    A.lw = L_RED + (8 * c8 + (lane >> 3)) * 32 + w * 4;
    A.so = lane * 2;
    A.mr = L_ML + (w >> 1) * 16;
    A.orr = L_OM + (w & 1) * 1024 + lane * 16 + (w >> 1) * 4;
    const int r16 = lane & 15, q4 = r16 >> 2, p2 = lane & 3;
    qa0 = L_Q + (lane & 3) * 272 + kq * 16;
    qa1 = qa0;
    if (lane & 4) { qa0 += L_CSI - L_Q; qa1 += L_CSI + 1088 - L_Q; }
    if (w < 4) {
      const u32 kb = L_K + 2048 * (4 * w + (r16 >> 2)) + 64 * (r16 & 3);
      A.r0 = kb + 16 * (kq ^ ((2 * (r16 >> 2)) & 3));
      A.r1 = kb + 256 + 16 * (kq ^ ((2 * (r16 >> 2) + 1) & 3));
      const u32 vb = L_V + 2048 * (4 * w + kq) + 64 * q4 + 8 * (p2 & 1);
      A.r2 = vb + 16 * (((p2 >> 1)) ^ ((2 * kq) & 3));
      A.r3 = vb + 256 + 16 * (((p2 >> 1)) ^ ((2 * kq + 1) & 3));
      A.r4 = vb + 16 * ((2 + (p2 >> 1)) ^ ((2 * kq) & 3));
      A.r5 = vb + 256 + 16 * ((2 + (p2 >> 1)) ^ ((2 * kq + 1) & 3));
      A.r7 = L_OM + w * 2048 + lane * 16;
      A.r8 = L_ML + (lane * 4 + w) * 4;
    } else {
      A.r0 = WS_BOX + (grp * 8 + (lane >> 3)) * BOX + BOX_Q + c8 * 16;
      A.r1 = L_Q + kq * 272 + (lane & 15) * 16;
      A.r2 = L_RED + lane * 32;
      A.r3 = L_XS + lane * 4;
      A.r4 = A.r5 = A.r7 = A.r8 = 0;
    }
    A.r6 = L_ST + c8 * 8;
    PIN(A.r6);
    PIN(A.vB); PIN(A.vA); PIN(A.vL); PIN(A.lw); PIN(A.so); PIN(A.mr); PIN(A.orr); PIN(A.r0); PIN(A.r1); PIN(A.r2); PIN(A.r3); PIN(A.r4); PIN(A.r5); PIN(A.r7); PIN(A.r8);
    PIN(qa0); PIN(qa1);
  }
  for (int rep = 0; rep < 2; ++rep) {
    layer<0>(rep, ws, lds, b, g, s, w, lane, xraw, A, qa0, vA + kvoff, kB + kvoff, vB + kvoff, wqB, woA, flag);
    layer<1>(rep, ws, lds, b, g, s, w, lane, xraw, A, qa1, nullptr, kB + kvoff, vB + kvoff, wqB, woB, flag);
  }
  if (g == 0 && w == s) {
    const u32 voff = WS_BOX + (u32)((b * 8 + c8) * 8 + w) * BOX + 3u * BOXL + BOX_X + kq * 32 + (p ? 16 : 0);
    if (DLY_B) __builtin_amdgcn_s_sleep(DLY_B);
    const v4u x = poll128(ws, voff);
    float v[8];
    sum_partials(x, v);
    if (c8 == 0) *(v4u*)(out + b * 512 + 64 * w + 16 * kq + (p ? 8 : 0)) = v4u{pk(v[0], v[1]), pk(v[2], v[3]), pk(v[4], v[5]), pk(v[6], v[7])};
  }
}

extern "C" void kernel_launch(void* const* d_in, const int* in_sizes, int n_in, void* d_out, int out_size, void* d_ws, size_t ws_size,
                              hipStream_t stream) {
  (void)hipFuncSetAttribute((const void*)fused_kernel, hipFuncAttributeMaxDynamicSharedMemorySize, L_END);
  fused_kernel<<<256, 512, L_END, stream>>>((const char*)d_in[0], (const char*)d_in[1], (const char*)d_in[2], (const char*)d_in[3], (const char*)d_in[4],
                                            (const char*)d_in[6], (const char*)d_in[7], (const char*)d_in[8], (const char*)d_in[9],
                                            (unsigned short*)d_out, (char*)d_ws);
}
```

```cpp
#include <hip/hip_runtime.h>
#include <stdint.h>

typedef unsigned int u32;
typedef unsigned long long u64;
typedef u32 v4u __attribute__((ext_vector_type(4)));
typedef u32 v2u __attribute__((ext_vector_type(2)));
typedef float v4f __attribute__((ext_vector_type(4)));
typedef float v2f __attribute__((ext_vector_type(2)));
typedef __bf16 v8b __attribute__((ext_vector_type(8)));
typedef __bf16 v2b __attribute__((ext_vector_type(2)));
#define GAS __attribute__((address_space(1)))

#define DLY_C 2
#define DLY_A 8
#define DLY_B 10
#define SCHED 2
#define EXTRA 0
#define TOUCHBOX 0
#define KVDELAY 0
#define PRETOUCH 0
#define NTKV ""
#define NTW ""

namespace {
constexpr int SK = 4096, WIN = 1024;
constexpr u32 WS_EPOCH = 0;
constexpr u32 WS_RDY = 4096;
constexpr u32 WS_LSR = 8192;
constexpr u32 WS_BOX = WS_LSR + 131072;
constexpr u32 BOXL = 1280, BOX_Q = 1024, BOX_X = 1152, BOX_CS = 5120, BOX = 5376;
constexpr u32 WS_END = WS_BOX + 256 * BOX;
constexpr u32 WS_DBG = 4u << 20;
constexpr u32 L_KV = 0;
constexpr u32 L_K = 0, L_V = 65536;
constexpr u32 L_OM = 131072;
constexpr u32 L_ML = L_OM + 8192;
constexpr u32 L_Q = L_ML + 128;
constexpr u32 L_CSI = L_Q + 1088;
constexpr u32 L_RED = L_CSI + 2176;
constexpr u32 L_REDC = L_RED + 2048;
constexpr u32 L_ST = L_REDC + 2048;
constexpr u32 L_RED3 = L_ST + 64;
constexpr u32 L_XS = L_RED3 + 2048;
constexpr u32 L_END = L_XS + 256;
static_assert(L_END <= 160 * 1024, "lds");
static_assert(L_Q % 16 == 0 && L_CSI % 16 == 0 && L_RED % 16 == 0 && L_ST % 16 == 0 && L_RED3 % 16 == 0 && L_XS % 16 == 0, "align");
constexpr u32 SENT = 0xFFFFFFFFu;
constexpr u32 SPIN_CAP = 1u << 15;
constexpr float C2 = 0.08838834764831845f * 1.4426950408889634f;
constexpr u32 ONES = 0x3F803F80u;
constexpr u64 PMASK = 0xFF00FF00FF00FF00ull;
constexpr u64 C8Z = 0x0101010101010101ull;

__device__ __forceinline__ float bflo(u32 w) { return __uint_as_float(w << 16); }
__device__ __forceinline__ float bfhi(u32 w) { return __uint_as_float(w & 0xffff0000u); }
__device__ __forceinline__ u32 pk(float lo, float hi) { v2f f = {lo, hi}; return __builtin_bit_cast(u32, __builtin_convertvector(f, v2b)); }
__device__ __forceinline__ u32 safe(u32 v) { return v < 0xFFFFFFFEu ? v : 0xFFFFFFFEu; }
template <int CTRL> __device__ __forceinline__ float dpp(float x) {
  return __builtin_bit_cast(float, __builtin_amdgcn_mov_dpp(__builtin_bit_cast(int, x), CTRL, 0xf, 0xf, true));
}
template <int CTRL> __device__ __forceinline__ u32 dppu(u32 x) { return (u32)__builtin_amdgcn_mov_dpp((int)x, CTRL, 0xf, 0xf, true); }
constexpr int XOR1 = 0xB1, XOR2 = 0x4E, HMIR = 0x141, ROR8 = 0x128;
__device__ __forceinline__ float x16_add(float x) { auto s = __builtin_amdgcn_permlane16_swap(__float_as_uint(x), __float_as_uint(x), false, false); return __uint_as_float(s[0]) + __uint_as_float(s[1]); }
__device__ __forceinline__ float x32_add(float x) { auto s = __builtin_amdgcn_permlane32_swap(__float_as_uint(x), __float_as_uint(x), false, false); return __uint_as_float(s[0]) + __uint_as_float(s[1]); }
__device__ __forceinline__ v4f mfma(v4u a, v4u b, v4f c) {
  return __builtin_amdgcn_mfma_f32_16x16x32_bf16(__builtin_bit_cast(v8b, a), __builtin_bit_cast(v8b, b), c, 0, 0, 0);
}
#define BAR() asm volatile("s_waitcnt lgkmcnt(0)\n\ts_barrier" ::: "memory")
#define VMWAIT(n) asm volatile("s_waitcnt vmcnt(" #n ")" ::: "memory")

constexpr int R_WQA = 128, R_WOA = 160, R_WQB = 192, R_WOB = 224;
template <int BASE, u32 STRIDE>
__device__ __forceinline__ void bulk8(const char* base, u32 voff) {
  asm volatile("" : "+s"(base));
  const __amdgpu_buffer_rsrc_t r = __builtin_amdgcn_make_buffer_rsrc((void*)base, 0, 0x7fffffff, 0x00020000);
  asm volatile("s_nop 4\n\t"
               "buffer_load_dwordx4 v[%c10:%c11], %0, %1, 0 offen" NTW "\n\tbuffer_load_dwordx4 v[%c12:%c13], %0, %1, %3 offen" NTW "\n\t"
               "buffer_load_dwordx4 v[%c14:%c15], %0, %1, %4 offen" NTW "\n\tbuffer_load_dwordx4 v[%c16:%c17], %0, %1, %5 offen" NTW "\n\t"
               "buffer_load_dwordx4 v[%c18:%c19], %0, %1, %6 offen" NTW "\n\tbuffer_load_dwordx4 v[%c20:%c21], %0, %1, %7 offen" NTW "\n\t"
               "buffer_load_dwordx4 v[%c22:%c23], %0, %1, %8 offen" NTW "\n\tbuffer_load_dwordx4 v[%c24:%c25], %0, %1, %9 offen" NTW
               :: "v"(voff), "s"(r), "s"(0), "s"(STRIDE), "s"(2 * STRIDE), "s"(3 * STRIDE), "s"(4 * STRIDE), "s"(5 * STRIDE), "s"(6 * STRIDE), "s"(7 * STRIDE),
                  "i"(BASE), "i"(BASE + 3), "i"(BASE + 4), "i"(BASE + 7), "i"(BASE + 8), "i"(BASE + 11), "i"(BASE + 12), "i"(BASE + 15),
                  "i"(BASE + 16), "i"(BASE + 19), "i"(BASE + 20), "i"(BASE + 23), "i"(BASE + 24), "i"(BASE + 27), "i"(BASE + 28), "i"(BASE + 31)
               : "memory");
}
template <int B, int M, int R>
__device__ __forceinline__ void tp_pair(u32 slo, u32 shi) {
  if constexpr (M == R) {
    asm volatile("v_perm_b32 v48, v%c3, v%c2, %0\n\tv_perm_b32 v%c3, v%c3, v%c2, %1\n\tv_mov_b32 v%c2, v48"
                 :: "s"(slo), "s"(shi), "i"(B + 8 * M + R), "i"(B + 8 * M + 4 + R));
  } else {
    asm volatile("v_perm_b32 v48, v%c3, v%c2, %0\n\tv_perm_b32 v49, v%c3, v%c2, %1\n\t"
                 "v_perm_b32 v%c2, v%c5, v%c4, %0\n\tv_perm_b32 v%c3, v%c5, v%c4, %1\n\t"
                 "v_mov_b32 v%c4, v48\n\tv_mov_b32 v%c5, v49"
                 :: "s"(slo), "s"(shi), "i"(B + 8 * M + R), "i"(B + 8 * M + 4 + R), "i"(B + 8 * R + M), "i"(B + 8 * R + 4 + M));
  }
}
template <int B>
__device__ __forceinline__ void transpose8() {
  const u32 slo = 0x05040100u, shi = 0x07060302u;
  tp_pair<B, 0, 0>(slo, shi); tp_pair<B, 1, 1>(slo, shi); tp_pair<B, 2, 2>(slo, shi); tp_pair<B, 3, 3>(slo, shi);
  tp_pair<B, 0, 1>(slo, shi); tp_pair<B, 0, 2>(slo, shi); tp_pair<B, 0, 3>(slo, shi);
  tp_pair<B, 1, 2>(slo, shi); tp_pair<B, 1, 3>(slo, shi); tp_pair<B, 2, 3>(slo, shi);
  asm volatile("s_nop 1");
}
#define SNOP
#define MF(d, b) "v_mfma_f32_16x16x32_bf16 v[" #d ":" #d "+3], v[48:51], v[%c[wb]+" #b ":%c[wb]+" #b "+3], 0\n\t"
#define FOLD_TAIL \
  "v_permlane32_swap_b32 v52, v68\n\t" \
  "v_permlane32_swap_b32 v56, v72\n\t" \
  "v_permlane32_swap_b32 v60, v76\n\t" \
  "v_permlane32_swap_b32 v64, v80\n\t" \
  "v_cndmask_b32_e64 v52, v52, v68, %[pm]\n\t" \
  "v_cndmask_b32_e64 v60, v60, v76, %[pm]\n\t" \
  "v_cndmask_b32_e64 v56, v56, v72, %[pm]\n\t" \
  "v_cndmask_b32_e64 v64, v64, v80, %[pm]\n\t" \
  "v_permlane16_swap_b32 v52, v60\n\t" \
  "s_nop 0\n\t" \
  "v_permlane16_swap_b32 v56, v64\n\t" \
  "v_add_f32 v52, v52, v60\n\t" \
  "v_add_f32 v56, v56, v64\n\t" \
  "s_nop 0\n\t" \
  "v_add_f32_dpp v52, v52, v52 row_ror:8 row_mask:0xf bank_mask:0xf\n\t" \
  "v_add_f32_dpp v56, v56, v56 row_ror:8 row_mask:0xf bank_mask:0xf\n\t" \
  "v_cndmask_b32_e64 v52, v52, v56, %[pm]\n\t"

template <int WB, bool POLL, int LOFF>
__device__ __forceinline__ void seg_p1(u32 voff, const char* ws, u32 ldsw, u32& spins) {
  if constexpr (POLL) {
    asm volatile(
      SNOP "Lp%=:\n\t"
      "global_load_dwordx4 v[48:51], %[voff], %[ws] sc1\n\t"
      "s_add_u32 %[sp], %[sp], 1\n\t"
      "s_waitcnt vmcnt(0)\n\t"
      MF(52, 0) "v_max3_u32 v84, v48, v49, v50\n\t"
      MF(56, 4) "v_max_u32 v84, v84, v51\n\t"
      MF(60, 8) "v_cmp_eq_u32 vcc, -1, v84\n\t"
      MF(64, 12)
      "s_cbranch_vccz Lk%=\n\t"
      "s_cmp_lt_u32 %[sp], %[cap]\n\t"
      "s_cbranch_scc1 Lp%=\n\t"
      "Lk%=:\n\t"
      : [sp] "+s"(spins) : [voff] "v"(voff), [ws] "s"(ws), [wb] "i"(WB), [cap] "i"(SPIN_CAP) : "memory", "vcc", "scc");
  } else {
    asm volatile("s_nop 1\n\t" MF(52, 0) "s_nop 0\n\t" MF(56, 4) "s_nop 0\n\t" MF(60, 8) "s_nop 0\n\t" MF(64, 12) "s_nop 0\n\t" :: [wb] "i"(WB));
  }
  asm volatile(
    MF(68, 16) "v_pk_add_f32 v[52:53], v[52:53], v[54:55]\n\t"
    MF(72, 20) "v_pk_add_f32 v[56:57], v[56:57], v[58:59]\n\t"
    MF(76, 24) "v_pk_add_f32 v[60:61], v[60:61], v[62:63]\n\t"
    MF(80, 28) "v_pk_add_f32 v[64:65], v[64:65], v[66:67]\n\t"
    "v_add_f32 v52, v52, v53\n\t"
    "v_pk_add_f32 v[68:69], v[68:69], v[70:71]\n\t"
    "v_add_f32 v56, v56, v57\n\t"
    "v_pk_add_f32 v[72:73], v[72:73], v[74:75]\n\t"
    "v_add_f32 v60, v60, v61\n\t"
    "v_pk_add_f32 v[76:77], v[76:77], v[78:79]\n\t"
    "v_add_f32 v64, v64, v65\n\t"
    "v_pk_add_f32 v[80:81], v[80:81], v[82:83]\n\t"
    "v_add_f32 v68, v68, v69\n\t"
    "v_add_f32 v72, v72, v73\n\t"
    "v_add_f32 v76, v76, v77\n\t"
    "v_add_f32 v80, v80, v81\n\t"
    "s_nop 0\n\t"
    FOLD_TAIL
    "ds_write_b32 %[lw], v52 offset:%c[lo]\n\t"
    :: [wb] "i"(WB), [pm] "s"(PMASK), [lw] "v"(ldsw), [lo] "i"(LOFF) : "memory");
}
__device__ __forceinline__ void seg_p3_issue(u32 voff, u32 voff2, const char* ws, const char* ws2) {
  asm volatile("s_nop 4\n\tglobal_load_dwordx4 v[48:51], %0, %2 sc1\n\tglobal_load_dword v84, %1, %3 sc1" :: "v"(voff), "v"(voff2), "s"(ws), "s"(ws2) : "memory");
}
template <int WB, int PRE>
__device__ __forceinline__ void seg_p3(u32 voff, u32 voff2, const char* ws, const char* ws2, u32 ldsw, u32& spins) {
  asm volatile(
    SNOP
    ".if %c[pre]\n\ts_waitcnt vmcnt(%c[pre])\n\ts_branch Lc%=\n\t.endif\n\t"
    "Lp%=:\n\t"
    "global_load_dwordx4 v[48:51], %[voff], %[ws] sc1\n\t"
    "global_load_dword v84, %[voff2], %[ws2] sc1\n\t"
    "s_add_u32 %[sp], %[sp], 1\n\t"
    "s_waitcnt vmcnt(0)\n\t"
    "Lc%=:\n\t"
    MF(52, 0) "v_max3_u32 v85, v48, v49, v50\n\t"
    MF(56, 4) "v_max3_u32 v85, v85, v51, v84\n\t"
    MF(60, 8) "v_cmp_eq_u32 vcc, -1, v85\n\t"
    MF(64, 12)
    "s_cbranch_vccz Lk%=\n\t"
    "s_cmp_lt_u32 %[sp], %[cap]\n\t"
    "s_cbranch_scc1 Lp%=\n\t"
    "Lk%=:\n\t"
    "v_max_f32_dpp v85, v84, v84 quad_perm:[1,0,3,2] row_mask:0xf bank_mask:0xf\n\t"
    MF(68, 16)
    "s_nop 0\n\t"
    "v_max_f32_dpp v85, v85, v85 quad_perm:[2,3,0,1] row_mask:0xf bank_mask:0xf\n\t"
    MF(72, 20)
    "s_nop 0\n\t"
    "v_max_f32_dpp v85, v85, v85 row_half_mirror row_mask:0xf bank_mask:0xf\n\t"
    MF(76, 24)
    "v_sub_f32 v86, v84, v85\n\t"
    MF(80, 28)
    "v_exp_f32 v86, v86\n\t"
    "s_nop 1\n\t"
    "v_mov_b32_dpp v90, v86 row_newbcast:0 row_mask:0x5 bank_mask:0xf\n\t"
    "v_mov_b32_dpp v91, v86 row_newbcast:1 row_mask:0x5 bank_mask:0xf\n\t"
    "v_mov_b32_dpp v92, v86 row_newbcast:2 row_mask:0x5 bank_mask:0xf\n\t"
    "v_mov_b32_dpp v93, v86 row_newbcast:3 row_mask:0x5 bank_mask:0xf\n\t"
    "v_mov_b32_dpp v90, v86 row_newbcast:4 row_mask:0xa bank_mask:0xf\n\t"
    "v_mov_b32_dpp v91, v86 row_newbcast:5 row_mask:0xa bank_mask:0xf\n\t"
    "v_mov_b32_dpp v92, v86 row_newbcast:6 row_mask:0xa bank_mask:0xf\n\t"
    "v_mov_b32_dpp v93, v86 row_newbcast:7 row_mask:0xa bank_mask:0xf\n\t"
    "v_add_f32_dpp v87, v86, v86 quad_perm:[1,0,3,2] row_mask:0xf bank_mask:0xf\n\t"
    "v_pk_mul_f32 v[52:53], v[52:53], v[90:91]\n\t"
    "v_pk_mul_f32 v[56:57], v[56:57], v[90:91]\n\t"
    "v_add_f32_dpp v87, v87, v87 quad_perm:[2,3,0,1] row_mask:0xf bank_mask:0xf\n\t"
    "v_pk_mul_f32 v[60:61], v[60:61], v[90:91]\n\t"
    "v_pk_mul_f32 v[64:65], v[64:65], v[90:91]\n\t"
    "v_add_f32_dpp v87, v87, v87 row_half_mirror row_mask:0xf bank_mask:0xf\n\t"
    "v_pk_mul_f32 v[68:69], v[68:69], v[90:91]\n\t"
    "v_pk_mul_f32 v[72:73], v[72:73], v[90:91]\n\t"
    "v_rcp_f32 v87, v87\n\t"
    "v_pk_mul_f32 v[76:77], v[76:77], v[90:91]\n\t"
    "v_pk_mul_f32 v[80:81], v[80:81], v[90:91]\n\t"
    "v_pk_fma_f32 v[52:53], v[54:55], v[92:93], v[52:53]\n\t"
    "v_pk_fma_f32 v[68:69], v[70:71], v[92:93], v[68:69]\n\t"
    "v_pk_fma_f32 v[56:57], v[58:59], v[92:93], v[56:57]\n\t"
    "v_pk_fma_f32 v[72:73], v[74:75], v[92:93], v[72:73]\n\t"
    "v_pk_fma_f32 v[60:61], v[62:63], v[92:93], v[60:61]\n\t"
    "v_pk_fma_f32 v[76:77], v[78:79], v[92:93], v[76:77]\n\t"
    "v_pk_fma_f32 v[64:65], v[66:67], v[92:93], v[64:65]\n\t"
    "v_pk_fma_f32 v[80:81], v[82:83], v[92:93], v[80:81]\n\t"
    "v_add_f32 v52, v52, v53\n\t"
    "v_add_f32 v68, v68, v69\n\t"
    "v_add_f32 v56, v56, v57\n\t"
    "v_add_f32 v72, v72, v73\n\t"
    "v_add_f32 v60, v60, v61\n\t"
    "v_add_f32 v76, v76, v77\n\t"
    "v_add_f32 v64, v64, v65\n\t"
    "v_add_f32 v80, v80, v81\n\t"
    "s_nop 1\n\t"
    FOLD_TAIL
    "v_mul_f32 v52, v52, v87\n\t"
    "ds_write_b32 %[lw], v52 offset:%c[lo]\n\t"
    : [sp] "+s"(spins)
    : [voff] "v"(voff), [voff2] "v"(voff2), [ws] "s"(ws), [ws2] "s"(ws2), [wb] "i"(WB), [cap] "i"(SPIN_CAP), [pm] "s"(PMASK), [lw] "v"(ldsw), [lo] "i"(L_RED3 - L_RED), [pre] "i"(PRE) : "memory", "vcc", "scc");
}
#define PACK_STORE \
  "s_nop 1\n\t" \
  "v_mov_b32_dpp v53, v52 quad_perm:[1,0,3,2] row_mask:0xf bank_mask:0xf\n\t" \
  "v_cvt_pk_bf16_f32 v54, v52, v53\n\t" \
  "v_min_u32 v54, -2, v54\n\t" \
  "s_nop 1\n\t" \
  "v_mov_b32_dpp v55, v54 row_shl:2 row_mask:0xf bank_mask:0xf\n\t" \
  "v_mov_b32_dpp v56, v54 row_shl:4 row_mask:0xf bank_mask:0xf\n\t" \
  "v_mov_b32_dpp v57, v54 row_shl:6 row_mask:0xf bank_mask:0xf\n\t" \
  "s_mov_b64 exec, %[c8z]\n\t" \
  "global_store_dwordx4 %[so], v[54:57], %[ws] sc1\n\t" \
  "s_mov_b64 exec, -1\n\t" \
  "s_nop 1\n\t"
template <bool XS>
__device__ __forceinline__ void seg_final2(u32 ldsr, u32 xsr, u32 so, const char* ws) {
  constexpr u32 LO = XS ? L_RED3 - L_RED : 0;
  if constexpr (XS) {
    asm volatile(
      "ds_read_b128 v[52:55], %[lr] offset:%c[lo]\n\t"
      "ds_read_b128 v[56:59], %[lr] offset:%c[lo]+16\n\t"
      "ds_read_b32 v60, %[xr]\n\t"
      "s_waitcnt lgkmcnt(1)\n\t"
      "v_pk_add_f32 v[52:53], v[52:53], v[56:57]\n\t"
      "v_pk_add_f32 v[54:55], v[54:55], v[58:59]\n\t"
      "v_pk_add_f32 v[52:53], v[52:53], v[54:55]\n\t"
      "s_waitcnt lgkmcnt(0)\n\t"
      "v_add_f32 v52, v52, v53\n\t"
      "v_add_f32 v52, v52, v60\n\t"
      PACK_STORE
      :: [lr] "v"(ldsr), [xr] "v"(xsr), [so] "v"(so), [ws] "s"(ws), [c8z] "s"(C8Z), [lo] "i"(LO) : "memory");
  } else {
    asm volatile(
      "ds_read_b128 v[52:55], %[lr]\n\t"
      "ds_read_b128 v[56:59], %[lr] offset:16\n\t"
      "s_waitcnt lgkmcnt(0)\n\t"
      "v_pk_add_f32 v[52:53], v[52:53], v[56:57]\n\t"
      "v_pk_add_f32 v[54:55], v[54:55], v[58:59]\n\t"
      "v_pk_add_f32 v[52:53], v[52:53], v[54:55]\n\t"
      "v_add_f32 v52, v52, v53\n\t"
      PACK_STORE
      :: [lr] "v"(ldsr), [so] "v"(so), [ws] "s"(ws), [c8z] "s"(C8Z) : "memory");
  }
}
template <int LOFF>
__device__ __forceinline__ void seg_qpoll(u32 voff, const char* ws, u32 ldsw, u32& spins) {
  asm volatile(
    SNOP "Lp%=:\n\t"
    "global_load_dwordx4 v[52:55], %[voff], %[ws] sc1\n\t"
    "s_add_u32 %[sp], %[sp], 1\n\t"
    "s_waitcnt vmcnt(0)\n\t"
    "v_max3_u32 v56, v52, v53, v54\n\t"
    "v_max_u32 v56, v56, v55\n\t"
    "v_cmp_eq_u32 vcc, -1, v56\n\t"
    "ds_write_b128 %[lw], v[52:55] offset:%c[lo]\n\t"
    "s_cbranch_vccz Lk%=\n\t"
    "s_cmp_lt_u32 %[sp], %[cap]\n\t"
    "s_cbranch_scc1 Lp%=\n\t"
    "Lk%=:\n\t"
    : [sp] "+s"(spins) : [voff] "v"(voff), [ws] "s"(ws), [lw] "v"(ldsw), [cap] "i"(SPIN_CAP), [lo] "i"(LOFF) : "memory", "vcc", "scc");
}
template <int TO>
__device__ __forceinline__ void seg_kpre(u32 ka0, u32 ka1) {
  asm volatile(
    "ds_read_b128 v[48:51], %0 offset:%c2\n\tds_read_b128 v[64:67], %1 offset:%c2\n\t"
    "ds_read_b128 v[52:55], %0 offset:%c2+512\n\tds_read_b128 v[68:71], %1 offset:%c2+512\n\t"
    "ds_read_b128 v[56:59], %0 offset:%c2+1024\n\tds_read_b128 v[72:75], %1 offset:%c2+1024\n\t"
    "ds_read_b128 v[60:63], %0 offset:%c2+1536\n\tds_read_b128 v[76:79], %1 offset:%c2+1536\n\t"
    "v_mov_b32 v96, 0\n\tv_mov_b32 v97, 0\n\tv_mov_b32 v98, 0\n\tv_mov_b32 v99, 0\n\t"
    "v_mov_b32 v100, 0\n\tv_mov_b32 v101, 0\n\tv_mov_b32 v102, 0\n\tv_mov_b32 v103, 0\n\t"
    "v_mov_b32 v104, 0\n\tv_mov_b32 v105, 0\n\tv_mov_b32 v106, 0\n\tv_mov_b32 v107, 0\n\t"
    "v_mov_b32 v108, 0\n\tv_mov_b32 v109, 0\n\tv_mov_b32 v110, 0\n\tv_mov_b32 v111, 0\n\t"
    :: "v"(ka0), "v"(ka1), "i"(TO) : "memory");
}
#define QK(s, k, q, c) "v_mfma_f32_16x16x32_bf16 v[" #s ":" #s "+3], v[" #k ":" #k "+3], v[" #q ":" #q "+3], " c "\n\t"
#define TR(d, a, off) "ds_read_b64_tr_b16 v[" #d ":" #d "+1], %[" #a "] offset:%c[to]+" #off "\n\t"
#define PV(r) "v_mfma_f32_16x16x32_bf16 v[" #r ":" #r "+3], v[124:127], v[" #r ":" #r "+3], 0\n\t"
#define SM(r) "v_fma_f32 v" #r ", v" #r ", v99, v121\n\tv_exp_f32 v" #r ", v" #r "\n\t"
#define PVA(acc, a, v, c) "v_mfma_f32_16x16x32_bf16 v[" #acc ":" #acc "+3], v[" #a ":" #a "+3], v[" #v ":" #v "+3], " c "\n\t"
#define PCP(d, s, ctl, bank) "v_mov_b32_dpp v" #d ", v" #s " " ctl " row_mask:0xf bank_mask:" bank "\n\t"
template <int TO>
__device__ __forceinline__ void seg_attn(u32 qa, u32 sta, u32 va0, u32 va1, u32 va2, u32 va3, u32 oa, u32 mla) {
  asm volatile(
    "ds_read_b128 v[80:83], %[qa]\n\t"
    "ds_read_b128 v[84:87], %[qa] offset:64\n\t"
    "ds_read_b128 v[88:91], %[qa] offset:128\n\t"
    "ds_read_b128 v[92:95], %[qa] offset:192\n\t"
    "ds_read_b64 v[120:121], %[sta]\n\t"
    "s_waitcnt lgkmcnt(4)\n\t"
    QK(112, 48, 80, "0") QK(116, 64, 80, "0")
    "s_waitcnt lgkmcnt(3)\n\t"
    QK(112, 52, 84, "v[112:115]") QK(116, 68, 84, "v[116:119]")
    "s_waitcnt lgkmcnt(2)\n\t"
    QK(112, 56, 88, "v[112:115]") QK(116, 72, 88, "v[116:119]")
    "s_waitcnt lgkmcnt(0)\n\t"
    QK(112, 60, 92, "v[112:115]")
    "v_add_f32_dpp v120, v120, v120 quad_perm:[1,0,3,2] row_mask:0xf bank_mask:0xf\n\t"
    "v_add_f32_dpp v121, v121, v121 quad_perm:[1,0,3,2] row_mask:0xf bank_mask:0xf\n\t"
    QK(116, 76, 92, "v[116:119]")
    TR(48, va0, 0)
    "v_add_f32_dpp v120, v120, v120 quad_perm:[2,3,0,1] row_mask:0xf bank_mask:0xf\n\t"
    "v_add_f32_dpp v121, v121, v121 quad_perm:[2,3,0,1] row_mask:0xf bank_mask:0xf\n\t"
    TR(50, va1, 0) TR(52, va2, 0)
    "v_add_f32_dpp v120, v120, v120 row_half_mirror row_mask:0xf bank_mask:0xf\n\t"
    "v_add_f32_dpp v121, v121, v121 row_half_mirror row_mask:0xf bank_mask:0xf\n\t"
    TR(54, va3, 0)
    "v_mul_f32 v122, 0xbb000000, v120\n\t"
    "v_mul_f32 v123, 0x3b000000, v121\n\t"
    "v_fma_f32 v123, -v122, v122, v123\n\t"
    "v_fmac_f32_dpp v112, v112, v122 row_shl:4 row_mask:0xf bank_mask:0xf bound_ctrl:1\n\t"
    "v_max_f32 v123, 0, v123\n\t"
    "v_fmac_f32_dpp v113, v113, v122 row_shl:4 row_mask:0xf bank_mask:0xf bound_ctrl:1\n\t"
    "v_add_f32 v123, 0x3727c5ac, v123\n\t"
    "v_fmac_f32_dpp v114, v114, v122 row_shl:4 row_mask:0xf bank_mask:0xf bound_ctrl:1\n\t"
    "v_rsq_f32 v123, v123\n\t"
    "v_fmac_f32_dpp v115, v115, v122 row_shl:4 row_mask:0xf bank_mask:0xf bound_ctrl:1\n\t"
    "v_fmac_f32_dpp v116, v116, v122 row_shl:4 row_mask:0xf bank_mask:0xf bound_ctrl:1\n\t"
    "v_max3_f32 v120, v112, v113, v114\n\t"
    "v_fmac_f32_dpp v117, v117, v122 row_shl:4 row_mask:0xf bank_mask:0xf bound_ctrl:1\n\t"
    "v_fmac_f32_dpp v118, v118, v122 row_shl:4 row_mask:0xf bank_mask:0xf bound_ctrl:1\n\t"
    "v_fmac_f32_dpp v119, v119, v122 row_shl:4 row_mask:0xf bank_mask:0xf bound_ctrl:1\n\t"
    "v_max3_f32 v120, v120, v115, v116\n\t"
    "v_mul_f32 v123, %[c2], v123\n\t"
    "v_max3_f32 v120, v120, v117, v118\n\t"
    "v_max_f32 v120, v120, v119\n\t"
    "v_mov_b32 v121, v120\n\t"
    TR(56, va0, 512) TR(58, va1, 512)
    "v_permlane16_swap_b32 v120, v121\n\t"
    "v_max_f32 v120, v120, v121\n\t"
    "v_mov_b32 v121, v120\n\t"
    TR(60, va2, 512) TR(62, va3, 512)
    "v_permlane32_swap_b32 v120, v121\n\t"
    "v_max_f32 v120, v120, v121\n\t"
    "v_mul_f32_e64 v121, -v120, v123\n\t"
    "v_mul_f32 v122, v120, v123\n\t"
    "v_fma_f32 v112, v112, v123, v121\n\t"
    "v_fma_f32 v113, v113, v123, v121\n\t"
    "v_exp_f32 v112, v112\n\t"
    TR(64, va0, 1024)
    "v_fma_f32 v114, v114, v123, v121\n\t"
    "v_exp_f32 v113, v113\n\t"
    TR(66, va1, 1024)
    "v_fma_f32 v115, v115, v123, v121\n\t"
    "v_exp_f32 v114, v114\n\t"
    TR(68, va2, 1024)
    "v_fma_f32 v116, v116, v123, v121\n\t"
    "v_exp_f32 v115, v115\n\t"
    TR(70, va3, 1024)
    "v_fma_f32 v117, v117, v123, v121\n\t"
    "v_exp_f32 v116, v116\n\t"
    "v_cvt_pk_bf16_f32 v124, v112, v113\n\t"
    "v_fma_f32 v118, v118, v123, v121\n\t"
    "v_exp_f32 v117, v117\n\t"
    TR(72, va0, 1536)
    "v_fma_f32 v119, v119, v123, v121\n\t"
    "v_exp_f32 v118, v118\n\t"
    "v_cvt_pk_bf16_f32 v125, v114, v115\n\t"
    "v_exp_f32 v119, v119\n\t"
    TR(74, va1, 1536) TR(76, va2, 1536) TR(78, va3, 1536)
    "v_cvt_pk_bf16_f32 v126, v116, v117\n\t"
    "v_cvt_pk_bf16_f32 v127, v118, v119\n\t"
    PCP(96, 124, "quad_perm:[0,1,2,3]", "0x1") PCP(100, 124, "row_shr:4", "0x2") PCP(104, 124, "quad_perm:[0,1,2,3]", "0x4") PCP(108, 124, "row_shr:4", "0x8")
    PCP(97, 125, "quad_perm:[0,1,2,3]", "0x1") PCP(101, 125, "row_shr:4", "0x2") PCP(105, 125, "quad_perm:[0,1,2,3]", "0x4") PCP(109, 125, "row_shr:4", "0x8")
    PCP(98, 126, "quad_perm:[0,1,2,3]", "0x1") PCP(102, 126, "row_shr:4", "0x2") PCP(106, 126, "quad_perm:[0,1,2,3]", "0x4") PCP(110, 126, "row_shr:4", "0x8")
    PCP(99, 127, "quad_perm:[0,1,2,3]", "0x1") PCP(103, 127, "row_shr:4", "0x2") PCP(107, 127, "quad_perm:[0,1,2,3]", "0x4") PCP(111, 127, "row_shr:4", "0x8")
    "v_mov_b32 v84, %[ones]\n\tv_mov_b32 v85, %[ones]\n\tv_mov_b32 v86, %[ones]\n\tv_mov_b32 v87, %[ones]\n\t"
    "s_waitcnt lgkmcnt(0)\n\t"
    PVA(112, 96, 48, "0") PVA(116, 96, 64, "0")
    PVA(112, 100, 52, "v[112:115]") PVA(116, 100, 68, "v[116:119]")
    PVA(112, 104, 56, "v[112:115]") PVA(116, 104, 72, "v[116:119]")
    PVA(112, 108, 60, "v[112:115]") PVA(116, 108, 76, "v[116:119]")
    "v_mfma_f32_16x16x32_bf16 v[80:83], v[84:87], v[124:127], 0\n\t"
    "s_nop 6\n\t"
    "ds_write_b128 %[oa], v[112:115]\n\t"
    "ds_write_b128 %[oa], v[116:119] offset:1024\n\t"
    "s_mov_b64 exec, 0xf\n\t"
    "ds_write2_b32 %[mla], v122, v80 offset0:0 offset1:16\n\t"
    "s_mov_b64 exec, -1\n\t"
    :: [qa] "v"(qa), [sta] "v"(sta), [va0] "v"(va0), [va1] "v"(va1), [va2] "v"(va2), [va3] "v"(va3), [oa] "v"(oa), [mla] "v"(mla),
       [c2] "s"(C2), [ones] "s"(ONES), [to] "i"(TO) : "memory");
}
__device__ __forceinline__ void seg_merge(u32 mla, u32 oa, u32 so, const char* ws) {
  asm volatile(
    "ds_read_b128 v[48:51], %[mla]\n\t"
    "ds_read_b128 v[60:63], %[mla] offset:64\n\t"
    "ds_read2st64_b32 v[56:57], %[oa] offset0:0 offset1:8\n\t"
    "ds_read2st64_b32 v[58:59], %[oa] offset0:16 offset1:24\n\t"
    "s_waitcnt lgkmcnt(3)\n\t"
    "v_max3_f32 v52, v48, v49, v50\n\t"
    "v_max_f32 v52, v52, v51\n\t"
    "v_sub_f32 v48, v48, v52\n\t"
    "v_sub_f32 v49, v49, v52\n\t"
    "v_exp_f32 v48, v48\n\t"
    "v_sub_f32 v50, v50, v52\n\t"
    "v_exp_f32 v49, v49\n\t"
    "v_sub_f32 v51, v51, v52\n\t"
    "v_exp_f32 v50, v50\n\t"
    "v_exp_f32 v51, v51\n\t"
    "s_waitcnt lgkmcnt(0)\n\t"
    "v_pk_mul_f32 v[60:61], v[60:61], v[48:49]\n\t"
    "v_pk_mul_f32 v[56:57], v[56:57], v[48:49]\n\t"
    "v_pk_fma_f32 v[60:61], v[62:63], v[50:51], v[60:61]\n\t"
    "v_pk_fma_f32 v[56:57], v[58:59], v[50:51], v[56:57]\n\t"
    "v_add_f32 v60, v60, v61\n\t"
    "v_add_f32 v56, v56, v57\n\t"
    "v_rcp_f32 v60, v60\n\t"
    "s_nop 0\n\t"
    "v_mul_f32 v52, v56, v60\n\t"
    PACK_STORE
    :: [mla] "v"(mla), [oa] "v"(oa), [so] "v"(so), [ws] "s"(ws), [c8z] "s"(C8Z) : "memory");
}

__device__ __forceinline__ void dma16(u32 voff, const char* sbase, u32 lds_dst) {
  u32 keep;
  asm volatile(SNOP "s_mov_b32 %0, m0\n\ts_mov_b32 m0, %3\n\ts_nop 0\n\tglobal_load_lds_dwordx4 %1, %2" NTKV "\n\ts_mov_b32 m0, %0"
               : "=&s"(keep) : "v"(voff), "s"(sbase), "s"(lds_dst) : "memory");
}
__device__ __forceinline__ void kv_load(const char* base, int w, int lane, u32 ldsb) {
  asm volatile("" : "+s"(base));
  const int r8 = (lane & 31) >> 2;
#pragma unroll
  for (int i = 0; i < 4; ++i) {
    const u32 ch = 8 * (i & 1) + 4 * (lane >> 5) + ((lane & 3) ^ ((2 * (i >> 1) + (r8 >> 2)) & 3));
    dma16(r8 * 256 + ch * 16, base + (size_t)(16 * w + 8 * (i >> 1)) * 256, ldsb + w * 4096 + i * 1024);
  }
}
__device__ __forceinline__ bool has_sent(v4u x) {
  const u32 m = __builtin_elementwise_max(__builtin_elementwise_max(x[0], x[1]), __builtin_elementwise_max(x[2], x[3]));
  return __builtin_amdgcn_ballot_w64(m == SENT) != 0ull;
}
__device__ __forceinline__ v4u poll128(const char* base, u32 voff) {
  v4u x;
  for (u32 spins = 0;; ++spins) {
    asm volatile("s_nop 4\n\tglobal_load_dwordx4 %0, %1, %2 sc1\n\ts_waitcnt vmcnt(0)" : "=&v"(x) : "v"(voff), "s"(base) : "memory");
    if (!has_sent(x) || spins > SPIN_CAP) break;
  }
  return x;
}
__device__ __forceinline__ v4u pack8(float v) {
  const u32 x = safe(pk(v, dpp<XOR1>(v)));
  return v4u{x, dppu<0x102>(x), dppu<0x104>(x), dppu<0x106>(x)};
}
__device__ __forceinline__ void sum_partials(v4u x, float (&v)[8]) {
#pragma unroll
  for (int m = 0; m < 4; ++m) { v[2 * m] = bflo(x[m]); v[2 * m + 1] = bfhi(x[m]); }
#pragma unroll
  for (int j = 0; j < 8; ++j) v[j] += dpp<XOR1>(v[j]);
#pragma unroll
  for (int j = 0; j < 8; ++j) v[j] += dpp<XOR2>(v[j]);
#pragma unroll
  for (int j = 0; j < 8; ++j) v[j] += dpp<HMIR>(v[j]);
}
__device__ __forceinline__ void x_stats(v4u xa, char* lds, int w, int lane) {
  const bool p = (lane >> 3) & 1; const int kq = lane >> 4;
  const v4f gm = mfma(xa, xa, v4f{0.f, 0.f, 0.f, 0.f});
  const v4f sm = mfma(xa, v4u{ONES, ONES, ONES, ONES}, v4f{0.f, 0.f, 0.f, 0.f});
  float s2 = (gm[0] + gm[1]) + (gm[2] + gm[3]);
  s2 = (p == (bool)(kq >> 1)) ? s2 : 0.f;
  s2 += dpp<XOR1>(s2); s2 += dpp<XOR2>(s2); s2 += dpp<HMIR>(s2); s2 += dpp<ROR8>(s2);
  s2 = x32_add(x16_add(s2));
  float s1 = (sm[0] + sm[1]) + (sm[2] + sm[3]);
  s1 = x32_add(x16_add(s1));
  if (lane == 0) *(v2f*)(lds + L_ST + w * 8) = v2f{s1, s2};
}
}

#define ST(i) do {} while (0)
#define STFLUSH(sv, base) do {} while (0)
#define SGPR_ATTR

__device__ __forceinline__ u32 lb_st() { return L_ST; }
#define OPAQUE_LANE() int lane = lane0; asm volatile("" : "+v"(lane)); const int c8 = lane & 7, kq = lane >> 4; const bool p = (lane >> 3) & 1; (void)c8; (void)p; (void)kq
#define OPAQUE_S(x) asm volatile("" : "+s"(x))

constexpr int FW = 4;

struct Addr {
  u32 vB, vA, vL, lw, so;
  u32 mr, orr;
  u32 r0, r1, r2, r3, r4, r5, r6, r7, r8;
};
#define PIN(x) asm volatile("" : "+v"(x))

template <int TYPE>
__device__ __forceinline__ void layer(const int rep, char* ws, char* lds, int b, int g, int s, int w, const int lane0, v4u xraw, Addr& A, u32 qaT,
                                      const char* nkc, const char* nvc, const char* nwq, const char* cwo, const char* nwo, const char* nkc0, const char* nvc0, const bool lead,
                                      const u32 tag, int& stampv, u32 (&cnt)[3]) {
  constexpr int RQ = TYPE ? R_WQB : R_WQA, RO = TYPE ? R_WOB : R_WOA;
  const int L = rep * 2 + TYPE;
  OPAQUE_S(b); OPAQUE_S(g); OPAQUE_S(s); OPAQUE_S(w);
  const u32 grp = (u32)(b * 8 + g), cu = grp * 8 + s;
#define LD_WQ(R, ptr) do { OPAQUE_LANE(); OPAQUE_S(ptr); bulk8<R, 8192>(ptr + (size_t)(64 * w) * 8192 + (512 * g + 64 * s) * 2, (u32)(lane >> 3) * 65536 + c8 * 16); } while (0)
#define LD_WO(R, ptr) do { OPAQUE_LANE(); OPAQUE_S(ptr); bulk8<R, 1024>(ptr + (size_t)(512 * g + 64 * w) * 1024 + 128 * s, (u32)(lane >> 3) * 8192 + c8 * 16); } while (0)
  const char* wsL = ws + L * BOXL;
  const char* box = ws + WS_BOX + cu * BOX + L * BOXL;
  {
    if (rep == 0) {
      if (SCHED == 4) { if (TYPE == 0) VMWAIT(8); else VMWAIT(8); }
      else if (SCHED == 3) { if (TYPE == 0) { if (KVDELAY) VMWAIT(0); else VMWAIT(8); } else VMWAIT(4); }
      else if (SCHED == 2) VMWAIT(0);
      else if (SCHED == 0) { if (TYPE == 0) { if (PRETOUCH && w == 7) VMWAIT(13); else VMWAIT(8); } else VMWAIT(0); }
      else { if (TYPE == 0) { if (!lead) VMWAIT(4); } else { VMWAIT(4); } }
      if (w >= 4) ST(7);
      if (SCHED == 3 && KVDELAY && TYPE == 0) { OPAQUE_LANE(); kv_load(nkc0, w, lane, L_K); kv_load(nvc0, w, lane, L_V); }
      transpose8<RQ>();
    }
    if (L > 0) {
      const char* wsP = wsL - BOXL;
      if (DLY_B && rep) __builtin_amdgcn_s_sleep(DLY_B);
      ST(0);
      u32 sp = 0;
      seg_p1<RQ, true, 0>(A.vB, wsP, A.lw, sp);
      cnt[0] += sp;
    } else {
      const v4u xa = (lane0 & 7) == 0 ? xraw : v4u{0u, 0u, 0u, 0u};
      asm volatile("v_mov_b32 v48, %0\n\tv_mov_b32 v49, %1\n\tv_mov_b32 v50, %2\n\tv_mov_b32 v51, %3" :: "v"(xa[0]), "v"(xa[1]), "v"(xa[2]), "v"(xa[3]));
      u32 sp = 0;
      seg_p1<RQ, false, 0>(0, ws, A.lw, sp);
    }
    ST(2);
    if (SCHED != 2 && SCHED != 3 && SCHED != 4 && rep == 0 && TYPE == 1) {
      OPAQUE_LANE();
      if (SCHED == 0) kv_load(nkc, w, lane, L_K + 32768);
      kv_load(nvc, w, lane, L_V + 32768);
    }
    v4u xa;
    asm volatile("v_mov_b32 %0, v48\n\tv_mov_b32 %1, v49\n\tv_mov_b32 %2, v50\n\tv_mov_b32 %3, v51" : "=v"(xa[0]), "=v"(xa[1]), "=v"(xa[2]), "=v"(xa[3]));
    if (rep == 0) {
      asm volatile("v_mov_b32 v48, %0\n\tv_mov_b32 v49, %0\n\tv_mov_b32 v50, %0\n\tv_mov_b32 v51, %0" :: "s"(ONES));
      u32 sp = 0;
      seg_p1<RQ, false, L_REDC - L_RED>(0, ws, A.lw, sp);
    }
    BAR();
    ST(3);
    if (w == FW) {
      seg_final2<false>(A.r2, 0, A.so, box + BOX_Q);
      ST(4);
      OPAQUE_LANE();
      if (SCHED == 3 && rep == 0 && TYPE == 1) kv_load(nvc, w, lane, L_V + 32768);
      if (rep == 0) {
        const __amdgpu_buffer_rsrc_t rs = __builtin_amdgcn_make_buffer_rsrc(ws, 0, WS_END, 0x00020000);
        const v4f r0 = *(const v4f*)(lds + L_REDC + lane * 32), r1 = *(const v4f*)(lds + L_REDC + lane * 32 + 16);
        const float cs = 0.125f * (((r0[0] + r0[1]) + (r0[2] + r0[3])) + ((r1[0] + r1[1]) + (r1[2] + r1[3])));
        const v4u pc = pack8(cs);
        if (c8 == 0) __builtin_amdgcn_raw_buffer_store_b128(pc, rs, WS_BOX + cu * BOX + BOX_CS + TYPE * 128 + lane * 2, 0, 16);
      }
      x_stats(xa, lds, w, lane);
      if (L == 0) {
        const u64 flag = (u64)tag | ((u64)(~tag) << 32);
        for (u32 spins = 0;; ++spins) {
          const u64 f = __hip_atomic_load((GAS u64*)(ws + WS_RDY + b * 512 + lane * 8), __ATOMIC_RELAXED, __HIP_MEMORY_SCOPE_AGENT);
          if (__all(f == flag) || spins > SPIN_CAP) break;
        }
        if (g == 0 && s == 0 && lane == 0) __hip_atomic_store((GAS u32*)(ws + WS_EPOCH + b * 256), tag, __ATOMIC_RELAXED, __HIP_MEMORY_SCOPE_AGENT);
      }
      if (DLY_C) __builtin_amdgcn_s_sleep(DLY_C);
      u32 sp = 0;
      seg_qpoll<0>(A.r0, wsL, A.r1, sp);
      cnt[1] += sp;
      if (rep == 0) { sp = 0; seg_qpoll<L_CSI + TYPE * 1088 - L_Q>(A.r0, ws + (BOX_CS + TYPE * 128 - BOX_Q), A.r1, sp); }
      ST(5);
    } else {
      OPAQUE_LANE();
      if (SCHED == 3 && rep == 0 && TYPE == 1) kv_load(nvc, w, lane, L_V + 32768);
      if (SCHED == 4 && rep == 0 && TYPE == 1) LD_WO(RO, cwo);
      x_stats(xa, lds, w, lane);
    }
    if (g == 0 && w == s) {
      OPAQUE_LANE();
      float v[8];
      sum_partials(xa, v);
      if (c8 == 0) {
        float* xs = (float*)(lds + L_XS) + 16 * kq + (p ? 8 : 0);
        *(v4f*)xs = v4f{v[0], v[1], v[2], v[3]};
        *(v4f*)(xs + 4) = v4f{v[4], v[5], v[6], v[7]};
      }
    }
    if (SCHED == 4 && rep == 0 && TYPE == 1 && w == FW) LD_WO(RO, cwo);
    if (rep == 0) {
      if (SCHED == 4 && TYPE == 1) {   }
      else if (SCHED == 1 && TYPE == 0 && lead) VMWAIT(16); else VMWAIT(0);
      if (w >= 4) ST(8);
      BAR();
      if (SCHED == 2) {}
      else if (SCHED == 4) { if (TYPE == 0) LD_WO(RO, cwo); }
      else if (SCHED == 0 || SCHED == 3) LD_WO(RO, cwo);
      else {
        if (!lead) LD_WO(RO, cwo);
        else if (TYPE == 0) LD_WO(R_WOB, nwo);
      }
    }
  }
  if (w < 4) {
    seg_kpre<TYPE * 32768>(A.r0, A.r1);
    ST(6);
    BAR();
    ST(7);
    seg_attn<TYPE * 32768>(qaT, A.r6, A.r2, A.r3, A.r4, A.r5, A.r7, A.r8);
    ST(8);
  } else {
    BAR();
  }
  {
    BAR();
    seg_merge(A.mr, A.orr, A.so, box + w * 128);
    if (w == 7) {
      OPAQUE_LANE();
      const __amdgpu_buffer_rsrc_t rs = __builtin_amdgcn_make_buffer_rsrc(ws, 0, WS_END, 0x00020000);
      const int hh = (lane >> 2) & 3, aa = lane & 3;
      const float m1 = ((const float*)(lds + L_ML))[hh * 4 + aa], l1 = ((const float*)(lds + L_ML))[16 + hh * 4 + aa];
      float M1 = __builtin_fmaxf(m1, dpp<XOR1>(m1)); M1 = __builtin_fmaxf(M1, dpp<XOR2>(M1));
      float L1 = __builtin_amdgcn_exp2f(m1 - M1) * l1; L1 += dpp<XOR1>(L1); L1 += dpp<XOR2>(L1);
      const u32 e = safe(__float_as_uint(M1 + __builtin_amdgcn_logf(L1)));
      const v4u ev = {dppu<0x150>(e), dppu<0x154>(e), dppu<0x158>(e), dppu<0x15C>(e)};
      if (lane < 8) __builtin_amdgcn_raw_buffer_store_b128(ev, rs, WS_LSR + ((L * 32 + grp) * 8 + lane) * 128 + s * 16, 0, 16);
    }
    ST(9);
  }
  {
    const char* wsS = ws + L * 32768;
    if (rep == 0) {
      if (SCHED == 1 && TYPE == 0 && lead) VMWAIT(8); else VMWAIT(0);
      transpose8<RO>();
    } else {
      if (DLY_A) __builtin_amdgcn_s_sleep(DLY_A);
    }
    ST(1);
    u32 sp = 0;
    if (SCHED == 4 && rep == 0 && TYPE == 0) {
      seg_p3_issue(A.vA, A.vL, wsL, wsS);
      LD_WQ(R_WQB, nwq);
      { OPAQUE_LANE(); kv_load(nkc, w, lane, L_K + 32768); kv_load(nvc, w, lane, L_V + 32768); }
      seg_p3<RO, 16>(A.vA, A.vL, wsL, wsS, A.lw, sp);
    } else {
      seg_p3<RO, 0>(A.vA, A.vL, wsL, wsS, A.lw, sp);
    }
    cnt[2] += sp;
    ST(10);
    if (SCHED != 2 && SCHED != 3 && SCHED != 4 && rep == 0 && TYPE == 0) {
      if (SCHED == 0) LD_WQ(R_WQB, nwq);
      else {
        if (!lead) LD_WQ(R_WQB, nwq);
        OPAQUE_LANE();
        kv_load(nkc, w, lane, L_K + 32768);
      }
    }
    BAR();
    if (w == FW) seg_final2<true>(A.r2, A.r3, A.so, box + BOX_X);
    ST(11);
    if (SCHED == 3 && rep == 0 && TYPE == 0) {
      LD_WQ(R_WQB, nwq);
      OPAQUE_LANE();
      kv_load(nkc, w, lane, L_K + 32768);
    }
  }
  STFLUSH(stampv, L * 12);
}

__global__ void __launch_bounds__(512, 2) __attribute__((amdgpu_num_vgpr(24) SGPR_ATTR))
fused_kernel(const char* __restrict__ xin, const char* kA, const char* vA, const char* wqA, const char* woA,
             const char* kB, const char* vB, const char* wqB, const char* woB, unsigned short* out, char* ws) {
  extern __shared__ __attribute__((aligned(16))) char lds[];
  const int id = blockIdx.x, g = id & 7, b = id >> 6, s = (id >> 3) & 7;
  const int tid = threadIdx.x, lane = tid & 63, w = __builtin_amdgcn_readfirstlane(tid >> 6);
  const int c8 = lane & 7, kq = lane >> 4;
  const bool p = (lane >> 3) & 1;
  int stampv = 0;
  const __amdgpu_buffer_rsrc_t rs = __builtin_amdgcn_make_buffer_rsrc(ws, 0, WS_END, 0x00020000);
  const u32 cu = (u32)((b * 8 + g) * 8 + s);
  asm volatile("" ::: "v127", "v255");
  u32 ep; v4u xraw;
  asm volatile("s_nop 4\n\tglobal_load_dword v56, %0, %1 sc1\n\tglobal_load_dwordx4 v[52:55], %2, %3"
               :: "v"(WS_EPOCH + b * 256), "s"(ws), "v"((u32)((b * 512 + 64 * w + 16 * kq + (p ? 8 : 0)) * 2)), "s"(xin) : "memory");
  if (w == 5) {
    const v4u sv = {SENT, SENT, SENT, SENT};
#pragma unroll
    for (int i = 0; i < 5; ++i) __builtin_amdgcn_raw_buffer_store_b128(sv, rs, WS_BOX + cu * BOX + i * 1024 + lane * 16, 0, 16);
    const u32 o6 = lane < 16 ? WS_BOX + cu * BOX + 5120 + lane * 16
                             : WS_LSR + (u32)(((((lane - 16) >> 3) * 32 + b * 8 + g) * 8 + (lane & 7)) * 128 + s * 16);
    if (lane < 48) __builtin_amdgcn_raw_buffer_store_b128(sv, rs, o6, 0, 16);
  }
  if (w == 6) ((float*)(lds + L_XS))[lane] = 0.f;
  const size_t kvoff = ((size_t)(b * 8 + g) * SK + (SK - WIN) + s * 128) * 256;
  const bool lead = (w >> 1) == b;
  const size_t qoff = (size_t)(64 * w) * 8192 + (512 * g + 64 * s) * 2, ooff = (size_t)(512 * g + 64 * w) * 1024 + 128 * s;
  const u32 vq = (u32)(lane >> 3) * 65536 + c8 * 16, vo = (u32)(lane >> 3) * 8192 + c8 * 16;
  if (SCHED == 0 || SCHED == 2 || SCHED == 3 || SCHED == 4 || lead) {
    bulk8<R_WQA, 8192>(wqA + qoff, vq);
    VMWAIT(8);
  } else {
    VMWAIT(0);
  }
  asm volatile("v_mov_b32 %0, v52\n\tv_mov_b32 %1, v53\n\tv_mov_b32 %2, v54\n\tv_mov_b32 %3, v55\n\tv_mov_b32 %4, v56"
               : "=v"(xraw[0]), "=v"(xraw[1]), "=v"(xraw[2]), "=v"(xraw[3]), "=v"(ep));
  ST(6);
  const u32 tag = (u32)__builtin_amdgcn_readfirstlane((int)ep) + 1u;
  if (w == 5 && lane == 0) {
    const u64 flag = (u64)tag | ((u64)(~tag) << 32);
    __hip_atomic_store((GAS u64*)(ws + WS_RDY + b * 512 + (s * 8 + g) * 8), flag, __ATOMIC_RELAXED, __HIP_MEMORY_SCOPE_AGENT);
  }
  if (TOUCHBOX && w == 5) {
    const u32 o6 = lane < 16 ? WS_BOX + cu * BOX + 5120 + lane * 16
                             : WS_LSR + (u32)((((((lane - 16) & 31) >> 3) * 32 + b * 8 + g) * 8 + (lane & 7)) * 128 + s * 16);
    asm volatile("s_nop 4\n\tglobal_load_dwordx4 v[100:103], %0, %2 sc1\n\tglobal_load_dwordx4 v[100:103], %0, %2 offset:1024 sc1\n\t"
                 "global_load_dwordx4 v[100:103], %0, %2 offset:2048 sc1\n\tglobal_load_dwordx4 v[100:103], %0, %2 offset:3072 sc1\n\t"
                 "global_load_dwordx4 v[100:103], %3, %2 sc1\n\tglobal_load_dwordx4 v[100:103], %1, %2 sc1"
                 :: "v"(WS_BOX + cu * BOX + lane * 16), "v"(o6), "s"(ws), "v"(WS_BOX + cu * BOX + 4096 + lane * 16) : "memory");
  }
  if (SCHED == 0 || SCHED == 3 || SCHED == 4) {
    if (!(SCHED == 3 && KVDELAY)) {
      asm volatile("s_barrier" ::: "memory");
      kv_load(kA + kvoff, w, lane, L_K);
      kv_load(vA + kvoff, w, lane, L_V);
    }
  } else if (SCHED == 2) {
    kv_load(kA + kvoff, w, lane, L_K);
    kv_load(vA + kvoff, w, lane, L_V);
    bulk8<R_WOA, 1024>(woA + ooff, vo);
    bulk8<R_WQB, 8192>(wqB + qoff, vq);
    kv_load(kB + kvoff, w, lane, L_K + 32768);
    kv_load(vB + kvoff, w, lane, L_V + 32768);
    bulk8<R_WOB, 1024>(woB + ooff, vo);
    if (EXTRA == 1) {
      const char* e = kA + ((size_t)(b * 8 + g) * SK + s * 64 * 4 + w * 32) * 256;
      bulk8<64, 1024>(e, (u32)lane * 16);
      const char* e2 = vA + ((size_t)(b * 8 + g) * SK + s * 64 * 4 + w * 32) * 256;
      bulk8<64, 1024>(e2, (u32)lane * 16);
    } else if (EXTRA == 3) {
      VMWAIT(0);
      const size_t o = kvoff + (size_t)w * 4096;
      asm volatile("s_nop 0");
      bulk8<64, 1024>(kA + kvoff + (w & 3) * 8192, (u32)lane * 16);
      bulk8<64, 1024>((w < 4 ? vA : kB) + kvoff + (w & 3) * 8192, (u32)lane * 16);
      (void)o;
    } else if (EXTRA == 2) {
      const char* e = kB + ((size_t)(g * 8 + s) * 8 + w) * 16384;
      bulk8<64, 1024>(e, (u32)lane * 16);
      bulk8<64, 1024>(e + 8192, (u32)lane * 16);
    }
  } else {
    kv_load(kA + kvoff, w, lane, L_K);
    if (lead) VMWAIT(4);
    asm volatile("s_barrier" ::: "memory");
    if (!lead) bulk8<R_WQA, 8192>(wqA + qoff, vq);
    kv_load(vA + kvoff, w, lane, L_V);
    if (lead) { bulk8<R_WOA, 1024>(woA + ooff, vo); bulk8<R_WQB, 8192>(wqB + qoff, vq); }
  }
  if (PRETOUCH && w == 7) {
    const size_t kvo = ((size_t)(b * 8 + g) * SK + (SK - WIN) + s * 128) * 256;
    asm volatile("s_nop 4\n\tglobal_load_dword v100, %0, %2\n\tglobal_load_dword v101, %1, %3\n\tglobal_load_dword v102, %1, %4\n\t"
                 "global_load_dword v103, %5, %6\n\tglobal_load_dword v104, %5, %7"
                 :: "v"((u32)(lane * 65536 + (512 * g + 64 * s) * 2)), "v"((u32)((512 * g + lane * 8) * 1024 + 128 * s)), "s"(wqB), "s"(woA), "s"(woB),
                    "v"((u32)(lane * 512)), "s"(kB + kvo), "s"(vB + kvo) : "memory");
  }
  { const unsigned long long t0_ = __builtin_amdgcn_s_memrealtime(); while (__builtin_amdgcn_s_memrealtime() - t0_ < 0ull) __builtin_amdgcn_s_sleep(4); }
  Addr A; u32 qa0, qa1;
  {
    const u32 grp = (u32)(b * 8 + g);
    const u32 oct = kq * 32 + (p ? 16 : 0);
    A.vB = WS_BOX + (u32)((b * 8 + c8) * 8 + w) * BOX + BOX_X + oct;
    A.vA = WS_BOX + (grp * 8 + c8) * BOX + w * 128 + oct;
    A.vL = WS_LSR + (grp * 8 + s) * 128 + c8 * 16 + (w >> 1) * 4;
    A.lw = L_RED + (8 * c8 + (lane >> 3)) * 32 + w * 4;
    A.so = lane * 2;
    A.mr = L_ML + (w >> 1) * 16;
    A.orr = L_OM + (w & 1) * 1024 + lane * 16 + (w >> 1) * 4;
    const int r16 = lane & 15, q4 = r16 >> 2, p2 = lane & 3;
    qa0 = L_Q + (lane & 3) * 272 + kq * 16;
    qa1 = qa0;
    if (lane & 4) { qa0 += L_CSI - L_Q; qa1 += L_CSI + 1088 - L_Q; }
    if (w < 4) {
      const u32 kb = L_K + 2048 * (4 * w + (r16 >> 2)) + 64 * (r16 & 3);
      A.r0 = kb + 16 * (kq ^ ((2 * (r16 >> 2)) & 3));
      A.r1 = kb + 256 + 16 * (kq ^ ((2 * (r16 >> 2) + 1) & 3));
      const u32 vb = L_V + 2048 * (4 * w + kq) + 64 * q4 + 8 * (p2 & 1);
      A.r2 = vb + 16 * (((p2 >> 1)) ^ ((2 * kq) & 3));
      A.r3 = vb + 256 + 16 * (((p2 >> 1)) ^ ((2 * kq + 1) & 3));
      A.r4 = vb + 16 * ((2 + (p2 >> 1)) ^ ((2 * kq) & 3));
      A.r5 = vb + 256 + 16 * ((2 + (p2 >> 1)) ^ ((2 * kq + 1) & 3));
      A.r7 = L_OM + w * 2048 + lane * 16;
      A.r8 = L_ML + (lane * 4 + w) * 4;
    } else {
      A.r0 = WS_BOX + (grp * 8 + (lane >> 3)) * BOX + BOX_Q + c8 * 16;
      A.r1 = L_Q + kq * 272 + (lane & 15) * 16;
      A.r2 = L_RED + lane * 32;
      A.r3 = L_XS + lane * 4;
      A.r4 = A.r5 = A.r7 = A.r8 = 0;
    }
    A.r6 = L_ST + c8 * 8;
    PIN(A.r6);
    PIN(A.vB); PIN(A.vA); PIN(A.vL); PIN(A.lw); PIN(A.so); PIN(A.mr); PIN(A.orr); PIN(A.r0); PIN(A.r1); PIN(A.r2); PIN(A.r3); PIN(A.r4); PIN(A.r5); PIN(A.r7); PIN(A.r8);
    PIN(qa0); PIN(qa1);
  }
  u32 cnt[3] = {0, 0, 0};
  for (int rep = 0; rep < 2; ++rep) {
    layer<0>(rep, ws, lds, b, g, s, w, lane, xraw, A, qa0, kB + kvoff, vB + kvoff, wqB, woA, woB, kA + kvoff, vA + kvoff, lead, tag, stampv, cnt);
    layer<1>(rep, ws, lds, b, g, s, w, lane, xraw, A, qa1, kB + kvoff, vB + kvoff, nullptr, woB, nullptr, nullptr, nullptr, lead, tag, stampv, cnt);
  }
  if (g == 0 && w == s) {
    const u32 voff = WS_BOX + (u32)((b * 8 + c8) * 8 + w) * BOX + 3u * BOXL + BOX_X + kq * 32 + (p ? 16 : 0);
    if (DLY_B) __builtin_amdgcn_s_sleep(DLY_B);
    const v4u x = poll128(ws, voff);
    float v[8];
    sum_partials(x, v);
    if (c8 == 0) *(v4u*)(out + b * 512 + 64 * w + 16 * kq + (p ? 8 : 0)) = v4u{pk(v[0], v[1]), pk(v[2], v[3]), pk(v[4], v[5]), pk(v[6], v[7])};
  }
}

extern "C" void kernel_launch(void* const* d_in, const int* in_sizes, int n_in, void* d_out, int out_size, void* d_ws, size_t ws_size,
                              hipStream_t stream) {
  (void)hipFuncSetAttribute((const void*)fused_kernel, hipFuncAttributeMaxDynamicSharedMemorySize, L_END);
  fused_kernel<<<256, 512, L_END, stream>>>((const char*)d_in[0], (const char*)d_in[1], (const char*)d_in[2], (const char*)d_in[3], (const char*)d_in[4],
                                            (const char*)d_in[6], (const char*)d_in[7], (const char*)d_in[8], (const char*)d_in[9],
                                            (unsigned short*)d_out, (char*)d_ws);
}
```

```cpp
#include <hip/hip_runtime.h>
#include <stdint.h>

typedef unsigned int u32;
typedef unsigned long long u64;
typedef u32 v4u __attribute__((ext_vector_type(4)));
typedef u32 v2u __attribute__((ext_vector_type(2)));
typedef float v4f __attribute__((ext_vector_type(4)));
typedef float v2f __attribute__((ext_vector_type(2)));
typedef __bf16 v8b __attribute__((ext_vector_type(8)));
typedef __bf16 v2b __attribute__((ext_vector_type(2)));
#define GAS __attribute__((address_space(1)))

#define DLY_C 2
#define DLY_A 8
#define DLY_B 12
#define STG 2
#define NTKV " nt"
#define NTW ""

namespace {
constexpr int SK = 4096, WIN = 1024;
constexpr u32 WS_EPOCH = 0;
constexpr u32 WS_RDY = 4096;
constexpr u32 WS_LSR = 8192;
constexpr u32 WS_BOX = WS_LSR + 131072;
constexpr u32 BOXL = 1280, BOX_Q = 1024, BOX_X = 1152, BOX_CS = 5120, BOX = 5376;
constexpr u32 WS_END = WS_BOX + 256 * BOX;
constexpr u32 L_K = 0, L_V = 65536;
constexpr u32 L_OM = 131072;
constexpr u32 L_ML = L_OM + 8192;
constexpr u32 L_Q = L_ML + 128;
constexpr u32 L_CSI = L_Q + 1088;
constexpr u32 L_RED = L_CSI + 2176;
constexpr u32 L_REDC = L_RED + 2048;
constexpr u32 L_ST = L_REDC + 2048;
constexpr u32 L_RED3 = L_ST + 64;
constexpr u32 L_XS = L_RED3 + 2048;
constexpr u32 L_END = L_XS + 256;
static_assert(L_END <= 160 * 1024, "lds");
static_assert(L_Q % 16 == 0 && L_CSI % 16 == 0 && L_RED % 16 == 0 && L_ST % 16 == 0 && L_RED3 % 16 == 0 && L_XS % 16 == 0, "align");
constexpr u32 SENT = 0xFFFFFFFFu;
constexpr u32 SPIN_CAP = 1u << 15;
constexpr float C2 = 0.08838834764831845f * 1.4426950408889634f;
constexpr u32 ONES = 0x3F803F80u;
constexpr u64 PMASK = 0xFF00FF00FF00FF00ull;
constexpr u64 C8Z = 0x0101010101010101ull;

__device__ __forceinline__ float bflo(u32 w) { return __uint_as_float(w << 16); }
__device__ __forceinline__ float bfhi(u32 w) { return __uint_as_float(w & 0xffff0000u); }
__device__ __forceinline__ u32 pk(float lo, float hi) { v2f f = {lo, hi}; return __builtin_bit_cast(u32, __builtin_convertvector(f, v2b)); }
__device__ __forceinline__ u32 safe(u32 v) { return v < 0xFFFFFFFEu ? v : 0xFFFFFFFEu; }
template <int CTRL> __device__ __forceinline__ float dpp(float x) {
  return __builtin_bit_cast(float, __builtin_amdgcn_mov_dpp(__builtin_bit_cast(int, x), CTRL, 0xf, 0xf, true));
}
template <int CTRL> __device__ __forceinline__ u32 dppu(u32 x) { return (u32)__builtin_amdgcn_mov_dpp((int)x, CTRL, 0xf, 0xf, true); }
constexpr int XOR1 = 0xB1, XOR2 = 0x4E, HMIR = 0x141, ROR8 = 0x128;
__device__ __forceinline__ float x16_add(float x) { auto s = __builtin_amdgcn_permlane16_swap(__float_as_uint(x), __float_as_uint(x), false, false); return __uint_as_float(s[0]) + __uint_as_float(s[1]); }
__device__ __forceinline__ float x32_add(float x) { auto s = __builtin_amdgcn_permlane32_swap(__float_as_uint(x), __float_as_uint(x), false, false); return __uint_as_float(s[0]) + __uint_as_float(s[1]); }
__device__ __forceinline__ v4f mfma(v4u a, v4u b, v4f c) {
  return __builtin_amdgcn_mfma_f32_16x16x32_bf16(__builtin_bit_cast(v8b, a), __builtin_bit_cast(v8b, b), c, 0, 0, 0);
}
#define BAR() asm volatile("s_waitcnt lgkmcnt(0)\n\ts_barrier" ::: "memory")
#define VMWAIT(n) asm volatile("s_waitcnt vmcnt(" #n ")" ::: "memory")

constexpr int R_WQA = 128, R_WOA = 160, R_WQB = 192, R_WOB = 224;
template <int BASE, u32 STRIDE>
__device__ __forceinline__ void bulk8(const char* base, u32 voff) {
  asm volatile("" : "+s"(base));
  const __amdgpu_buffer_rsrc_t r = __builtin_amdgcn_make_buffer_rsrc((void*)base, 0, 0x7fffffff, 0x00020000);
  asm volatile("s_nop 4\n\t"
               "buffer_load_dwordx4 v[%c10:%c11], %0, %1, 0 offen" NTW "\n\tbuffer_load_dwordx4 v[%c12:%c13], %0, %1, %3 offen" NTW "\n\t"
               "buffer_load_dwordx4 v[%c14:%c15], %0, %1, %4 offen" NTW "\n\tbuffer_load_dwordx4 v[%c16:%c17], %0, %1, %5 offen" NTW "\n\t"
               "buffer_load_dwordx4 v[%c18:%c19], %0, %1, %6 offen" NTW "\n\tbuffer_load_dwordx4 v[%c20:%c21], %0, %1, %7 offen" NTW "\n\t"
               "buffer_load_dwordx4 v[%c22:%c23], %0, %1, %8 offen" NTW "\n\tbuffer_load_dwordx4 v[%c24:%c25], %0, %1, %9 offen" NTW
               :: "v"(voff), "s"(r), "s"(0), "s"(STRIDE), "s"(2 * STRIDE), "s"(3 * STRIDE), "s"(4 * STRIDE), "s"(5 * STRIDE), "s"(6 * STRIDE), "s"(7 * STRIDE),
                  "i"(BASE), "i"(BASE + 3), "i"(BASE + 4), "i"(BASE + 7), "i"(BASE + 8), "i"(BASE + 11), "i"(BASE + 12), "i"(BASE + 15),
                  "i"(BASE + 16), "i"(BASE + 19), "i"(BASE + 20), "i"(BASE + 23), "i"(BASE + 24), "i"(BASE + 27), "i"(BASE + 28), "i"(BASE + 31)
               : "memory");
}
template <int BASE, u32 STRIDE, int I0>
__device__ __forceinline__ void bulk2(const char* base, u32 voff) {
  asm volatile("" : "+s"(base));
  const __amdgpu_buffer_rsrc_t r = __builtin_amdgcn_make_buffer_rsrc((void*)base, 0, 0x7fffffff, 0x00020000);
  asm volatile("s_nop 4\n\tbuffer_load_dwordx4 v[%c4:%c4+3], %0, %1, %2 offen" NTW "\n\tbuffer_load_dwordx4 v[%c4+4:%c4+7], %0, %1, %3 offen" NTW
               :: "v"(voff), "s"(r), "s"(I0 * STRIDE), "s"((I0 + 1) * STRIDE), "i"(BASE + 4 * I0) : "memory");
}
template <int B, int M, int R>
__device__ __forceinline__ void tp_pair(u32 slo, u32 shi) {
  if constexpr (M == R) {
    asm volatile("v_perm_b32 v100, v%c3, v%c2, %0\n\tv_perm_b32 v%c3, v%c3, v%c2, %1\n\tv_mov_b32 v%c2, v100"
                 :: "s"(slo), "s"(shi), "i"(B + 8 * M + R), "i"(B + 8 * M + 4 + R));
  } else {
    asm volatile("v_perm_b32 v100, v%c3, v%c2, %0\n\tv_perm_b32 v101, v%c3, v%c2, %1\n\t"
                 "v_perm_b32 v%c2, v%c5, v%c4, %0\n\tv_perm_b32 v%c3, v%c5, v%c4, %1\n\t"
                 "v_mov_b32 v%c4, v100\n\tv_mov_b32 v%c5, v101"
                 :: "s"(slo), "s"(shi), "i"(B + 8 * M + R), "i"(B + 8 * M + 4 + R), "i"(B + 8 * R + M), "i"(B + 8 * R + 4 + M));
  }
}
template <int B>
__device__ __forceinline__ void transpose8() {
  const u32 slo = 0x05040100u, shi = 0x07060302u;
  tp_pair<B, 0, 0>(slo, shi); tp_pair<B, 1, 1>(slo, shi); tp_pair<B, 2, 2>(slo, shi); tp_pair<B, 3, 3>(slo, shi);
  tp_pair<B, 0, 1>(slo, shi); tp_pair<B, 0, 2>(slo, shi); tp_pair<B, 0, 3>(slo, shi);
  tp_pair<B, 1, 2>(slo, shi); tp_pair<B, 1, 3>(slo, shi); tp_pair<B, 2, 3>(slo, shi);
  asm volatile("s_nop 1");
}
#define MF(d, b) "v_mfma_f32_16x16x32_bf16 v[" #d ":" #d "+3], v[48:51], v[%c[wb]+" #b ":%c[wb]+" #b "+3], 0\n\t"
#define FOLD_TAIL \
  "v_permlane32_swap_b32 v52, v68\n\t" \
  "v_permlane32_swap_b32 v56, v72\n\t" \
  "v_permlane32_swap_b32 v60, v76\n\t" \
  "v_permlane32_swap_b32 v64, v80\n\t" \
  "v_cndmask_b32_e64 v52, v52, v68, %[pm]\n\t" \
  "v_cndmask_b32_e64 v60, v60, v76, %[pm]\n\t" \
  "v_cndmask_b32_e64 v56, v56, v72, %[pm]\n\t" \
  "v_cndmask_b32_e64 v64, v64, v80, %[pm]\n\t" \
  "v_permlane16_swap_b32 v52, v60\n\t" \
  "s_nop 0\n\t" \
  "v_permlane16_swap_b32 v56, v64\n\t" \
  "v_add_f32 v52, v52, v60\n\t" \
  "v_add_f32 v56, v56, v64\n\t" \
  "s_nop 0\n\t" \
  "v_add_f32_dpp v52, v52, v52 row_ror:8 row_mask:0xf bank_mask:0xf\n\t" \
  "v_add_f32_dpp v56, v56, v56 row_ror:8 row_mask:0xf bank_mask:0xf\n\t" \
  "v_cndmask_b32_e64 v52, v52, v56, %[pm]\n\t"

__device__ __forceinline__ void poll_issue(u32 voff, const char* ws) {
  asm volatile("s_nop 4\n\tglobal_load_dwordx4 v[48:51], %0, %1 sc1" :: "v"(voff), "s"(ws) : "memory");
}
template <int WB, int POLL, int LOFF>
__device__ __forceinline__ void seg_p1(u32 voff, const char* ws, u32 ldsw, u32& spins) {
  if constexpr (POLL != 0) {
    asm volatile(
      ".if %c[pre]\n\ts_branch Lc%=\n\t.endif\n\t"
      "Lp%=:\n\t"
      "global_load_dwordx4 v[48:51], %[voff], %[ws] sc1\n\t"
      "s_add_u32 %[sp], %[sp], 1\n\t"
      "s_waitcnt vmcnt(0)\n\t"
      "Lc%=:\n\t"
      MF(52, 0) "v_max3_u32 v84, v48, v49, v50\n\t"
      MF(56, 4) "v_max_u32 v84, v84, v51\n\t"
      MF(60, 8) "v_cmp_eq_u32 vcc, -1, v84\n\t"
      MF(64, 12)
      "s_cbranch_vccz Lk%=\n\t"
      "s_cmp_lt_u32 %[sp], %[cap]\n\t"
      "s_cbranch_scc1 Lp%=\n\t"
      "Lk%=:\n\t"
      : [sp] "+s"(spins) : [voff] "v"(voff), [ws] "s"(ws), [wb] "i"(WB), [cap] "i"(SPIN_CAP), [pre] "i"(POLL == 2) : "memory", "vcc", "scc");
  } else {
    asm volatile("s_nop 1\n\t" MF(52, 0) "s_nop 0\n\t" MF(56, 4) "s_nop 0\n\t" MF(60, 8) "s_nop 0\n\t" MF(64, 12) "s_nop 0\n\t" :: [wb] "i"(WB));
  }
  asm volatile(
    MF(68, 16) "v_pk_add_f32 v[52:53], v[52:53], v[54:55]\n\t"
    MF(72, 20) "v_pk_add_f32 v[56:57], v[56:57], v[58:59]\n\t"
    MF(76, 24) "v_pk_add_f32 v[60:61], v[60:61], v[62:63]\n\t"
    MF(80, 28) "v_pk_add_f32 v[64:65], v[64:65], v[66:67]\n\t"
    "v_add_f32 v52, v52, v53\n\t"
    "v_pk_add_f32 v[68:69], v[68:69], v[70:71]\n\t"
    "v_add_f32 v56, v56, v57\n\t"
    "v_pk_add_f32 v[72:73], v[72:73], v[74:75]\n\t"
    "v_add_f32 v60, v60, v61\n\t"
    "v_pk_add_f32 v[76:77], v[76:77], v[78:79]\n\t"
    "v_add_f32 v64, v64, v65\n\t"
    "v_pk_add_f32 v[80:81], v[80:81], v[82:83]\n\t"
    "v_add_f32 v68, v68, v69\n\t"
    "v_add_f32 v72, v72, v73\n\t"
    "v_add_f32 v76, v76, v77\n\t"
    "v_add_f32 v80, v80, v81\n\t"
    "s_nop 0\n\t"
    FOLD_TAIL
    "ds_write_b32 %[lw], v52 offset:%c[lo]\n\t"
    :: [wb] "i"(WB), [pm] "s"(PMASK), [lw] "v"(ldsw), [lo] "i"(LOFF) : "memory");
}
__device__ __forceinline__ void seg_p3_issue(u32 voff, u32 voff2, const char* ws, const char* ws2) {
  asm volatile("s_nop 4\n\tglobal_load_dwordx4 v[48:51], %0, %2 sc1\n\tglobal_load_dword v84, %1, %3 sc1" :: "v"(voff), "v"(voff2), "s"(ws), "s"(ws2) : "memory");
}
template <int WB, int PRE>
__device__ __forceinline__ void seg_p3(u32 voff, u32 voff2, const char* ws, const char* ws2, u32 ldsw, u32& spins) {
  asm volatile(
    ".if %c[pre]\n\ts_branch Lc%=\n\t.endif\n\t"
    "Lp%=:\n\t"
    "global_load_dwordx4 v[48:51], %[voff], %[ws] sc1\n\t"
    "global_load_dword v84, %[voff2], %[ws2] sc1\n\t"
    "s_add_u32 %[sp], %[sp], 1\n\t"
    "s_waitcnt vmcnt(0)\n\t"
    "Lc%=:\n\t"
    MF(52, 0) "v_max3_u32 v85, v48, v49, v50\n\t"
    MF(56, 4) "v_max3_u32 v85, v85, v51, v84\n\t"
    MF(60, 8) "v_cmp_eq_u32 vcc, -1, v85\n\t"
    MF(64, 12)
    "s_cbranch_vccz Lk%=\n\t"
    "s_cmp_lt_u32 %[sp], %[cap]\n\t"
    "s_cbranch_scc1 Lp%=\n\t"
    "Lk%=:\n\t"
    "v_max_f32_dpp v85, v84, v84 quad_perm:[1,0,3,2] row_mask:0xf bank_mask:0xf\n\t"
    MF(68, 16)
    "s_nop 0\n\t"
    "v_max_f32_dpp v85, v85, v85 quad_perm:[2,3,0,1] row_mask:0xf bank_mask:0xf\n\t"
    MF(72, 20)
    "s_nop 0\n\t"
    "v_max_f32_dpp v85, v85, v85 row_half_mirror row_mask:0xf bank_mask:0xf\n\t"
    MF(76, 24)
    "v_sub_f32 v86, v84, v85\n\t"
    MF(80, 28)
    "v_exp_f32 v86, v86\n\t"
    "s_nop 1\n\t"
    "v_mov_b32_dpp v90, v86 row_newbcast:0 row_mask:0x5 bank_mask:0xf\n\t"
    "v_mov_b32_dpp v91, v86 row_newbcast:1 row_mask:0x5 bank_mask:0xf\n\t"
    "v_mov_b32_dpp v92, v86 row_newbcast:2 row_mask:0x5 bank_mask:0xf\n\t"
    "v_mov_b32_dpp v93, v86 row_newbcast:3 row_mask:0x5 bank_mask:0xf\n\t"
    "v_mov_b32_dpp v90, v86 row_newbcast:4 row_mask:0xa bank_mask:0xf\n\t"
    "v_mov_b32_dpp v91, v86 row_newbcast:5 row_mask:0xa bank_mask:0xf\n\t"
    "v_mov_b32_dpp v92, v86 row_newbcast:6 row_mask:0xa bank_mask:0xf\n\t"
    "v_mov_b32_dpp v93, v86 row_newbcast:7 row_mask:0xa bank_mask:0xf\n\t"
    "v_add_f32_dpp v87, v86, v86 quad_perm:[1,0,3,2] row_mask:0xf bank_mask:0xf\n\t"
    "v_pk_mul_f32 v[52:53], v[52:53], v[90:91]\n\t"
    "v_pk_mul_f32 v[56:57], v[56:57], v[90:91]\n\t"
    "v_add_f32_dpp v87, v87, v87 quad_perm:[2,3,0,1] row_mask:0xf bank_mask:0xf\n\t"
    "v_pk_mul_f32 v[60:61], v[60:61], v[90:91]\n\t"
    "v_pk_mul_f32 v[64:65], v[64:65], v[90:91]\n\t"
    "v_add_f32_dpp v87, v87, v87 row_half_mirror row_mask:0xf bank_mask:0xf\n\t"
    "v_pk_mul_f32 v[68:69], v[68:69], v[90:91]\n\t"
    "v_pk_mul_f32 v[72:73], v[72:73], v[90:91]\n\t"
    "v_rcp_f32 v87, v87\n\t"
    "v_pk_mul_f32 v[76:77], v[76:77], v[90:91]\n\t"
    "v_pk_mul_f32 v[80:81], v[80:81], v[90:91]\n\t"
    "v_pk_fma_f32 v[52:53], v[54:55], v[92:93], v[52:53]\n\t"
    "v_pk_fma_f32 v[68:69], v[70:71], v[92:93], v[68:69]\n\t"
    "v_pk_fma_f32 v[56:57], v[58:59], v[92:93], v[56:57]\n\t"
    "v_pk_fma_f32 v[72:73], v[74:75], v[92:93], v[72:73]\n\t"
    "v_pk_fma_f32 v[60:61], v[62:63], v[92:93], v[60:61]\n\t"
    "v_pk_fma_f32 v[76:77], v[78:79], v[92:93], v[76:77]\n\t"
    "v_pk_fma_f32 v[64:65], v[66:67], v[92:93], v[64:65]\n\t"
    "v_pk_fma_f32 v[80:81], v[82:83], v[92:93], v[80:81]\n\t"
    "v_add_f32 v52, v52, v53\n\t"
    "v_add_f32 v68, v68, v69\n\t"
    "v_add_f32 v56, v56, v57\n\t"
    "v_add_f32 v72, v72, v73\n\t"
    "v_add_f32 v60, v60, v61\n\t"
    "v_add_f32 v76, v76, v77\n\t"
    "v_add_f32 v64, v64, v65\n\t"
    "v_add_f32 v80, v80, v81\n\t"
    "s_nop 1\n\t"
    FOLD_TAIL
    "v_mul_f32 v52, v52, v87\n\t"
    "ds_write_b32 %[lw], v52 offset:%c[lo]\n\t"
    : [sp] "+s"(spins)
    : [voff] "v"(voff), [voff2] "v"(voff2), [ws] "s"(ws), [ws2] "s"(ws2), [wb] "i"(WB), [cap] "i"(SPIN_CAP), [pm] "s"(PMASK), [lw] "v"(ldsw), [lo] "i"(L_RED3 - L_RED), [pre] "i"(PRE) : "memory", "vcc", "scc");
}
#define PACK_STORE \
  "s_nop 1\n\t" \
  "v_mov_b32_dpp v53, v52 quad_perm:[1,0,3,2] row_mask:0xf bank_mask:0xf\n\t" \
  "v_cvt_pk_bf16_f32 v54, v52, v53\n\t" \
  "v_min_u32 v54, -2, v54\n\t" \
  "s_nop 1\n\t" \
  "v_mov_b32_dpp v55, v54 row_shl:2 row_mask:0xf bank_mask:0xf\n\t" \
  "v_mov_b32_dpp v56, v54 row_shl:4 row_mask:0xf bank_mask:0xf\n\t" \
  "v_mov_b32_dpp v57, v54 row_shl:6 row_mask:0xf bank_mask:0xf\n\t" \
  "s_mov_b64 exec, %[c8z]\n\t" \
  "global_store_dwordx4 %[so], v[54:57], %[ws] sc1\n\t" \
  "s_mov_b64 exec, -1\n\t" \
  "s_nop 1\n\t"
template <bool XS>
__device__ __forceinline__ void seg_final2(u32 ldsr, u32 xsr, u32 so, const char* ws) {
  constexpr u32 LO = XS ? L_RED3 - L_RED : 0;
  if constexpr (XS) {
    asm volatile(
      "ds_read_b128 v[52:55], %[lr] offset:%c[lo]\n\t"
      "ds_read_b128 v[56:59], %[lr] offset:%c[lo]+16\n\t"
      "ds_read_b32 v60, %[xr]\n\t"
      "s_waitcnt lgkmcnt(1)\n\t"
      "v_pk_add_f32 v[52:53], v[52:53], v[56:57]\n\t"
      "v_pk_add_f32 v[54:55], v[54:55], v[58:59]\n\t"
      "v_pk_add_f32 v[52:53], v[52:53], v[54:55]\n\t"
      "s_waitcnt lgkmcnt(0)\n\t"
      "v_add_f32 v52, v52, v53\n\t"
      "v_add_f32 v52, v52, v60\n\t"
      PACK_STORE
      :: [lr] "v"(ldsr), [xr] "v"(xsr), [so] "v"(so), [ws] "s"(ws), [c8z] "s"(C8Z), [lo] "i"(LO) : "memory");
  } else {
    asm volatile(
      "ds_read_b128 v[52:55], %[lr]\n\t"
      "ds_read_b128 v[56:59], %[lr] offset:16\n\t"
      "s_waitcnt lgkmcnt(0)\n\t"
      "v_pk_add_f32 v[52:53], v[52:53], v[56:57]\n\t"
      "v_pk_add_f32 v[54:55], v[54:55], v[58:59]\n\t"
      "v_pk_add_f32 v[52:53], v[52:53], v[54:55]\n\t"
      "v_add_f32 v52, v52, v53\n\t"
      PACK_STORE
      :: [lr] "v"(ldsr), [so] "v"(so), [ws] "s"(ws), [c8z] "s"(C8Z) : "memory");
  }
}
template <int RB>
__device__ __forceinline__ void qpoll_issue(u32 voff, const char* ws) {
  asm volatile("s_nop 4\n\tglobal_load_dwordx4 v[%c2:%c2+3], %0, %1 sc1" :: "v"(voff), "s"(ws), "i"(RB) : "memory");
}
template <int LOFF, int RB, int PRE>
__device__ __forceinline__ void seg_qpoll(u32 voff, const char* ws, u32 ldsw, u32& spins) {
  asm volatile(
    ".if %c[pre]\n\ts_branch Lc%=\n\t.endif\n\t"
    "Lp%=:\n\t"
    "global_load_dwordx4 v[%c[rb]:%c[rb]+3], %[voff], %[ws] sc1\n\t"
    "s_add_u32 %[sp], %[sp], 1\n\t"
    "s_waitcnt vmcnt(0)\n\t"
    "Lc%=:\n\t"
    "v_max3_u32 v70, v%c[rb], v[%c[rb]+1], v[%c[rb]+2]\n\t"
    "v_max_u32 v70, v70, v[%c[rb]+3]\n\t"
    "v_cmp_eq_u32 vcc, -1, v70\n\t"
    "ds_write_b128 %[lw], v[%c[rb]:%c[rb]+3] offset:%c[lo]\n\t"
    "s_cbranch_vccz Lk%=\n\t"
    "s_cmp_lt_u32 %[sp], %[cap]\n\t"
    "s_cbranch_scc1 Lp%=\n\t"
    "Lk%=:\n\t"
    : [sp] "+s"(spins) : [voff] "v"(voff), [ws] "s"(ws), [lw] "v"(ldsw), [cap] "i"(SPIN_CAP), [lo] "i"(LOFF), [rb] "i"(RB), [pre] "i"(PRE) : "memory", "vcc", "scc");
}
template <int TO>
__device__ __forceinline__ void seg_kpre(u32 ka0, u32 ka1) {
  asm volatile(
    "ds_read_b128 v[48:51], %0 offset:%c2\n\tds_read_b128 v[64:67], %1 offset:%c2\n\t"
    "ds_read_b128 v[52:55], %0 offset:%c2+512\n\tds_read_b128 v[68:71], %1 offset:%c2+512\n\t"
    "ds_read_b128 v[56:59], %0 offset:%c2+1024\n\tds_read_b128 v[72:75], %1 offset:%c2+1024\n\t"
    "ds_read_b128 v[60:63], %0 offset:%c2+1536\n\tds_read_b128 v[76:79], %1 offset:%c2+1536\n\t"
    "v_mov_b32 v96, 0\n\tv_mov_b32 v97, 0\n\tv_mov_b32 v98, 0\n\tv_mov_b32 v99, 0\n\t"
    "v_mov_b32 v100, 0\n\tv_mov_b32 v101, 0\n\tv_mov_b32 v102, 0\n\tv_mov_b32 v103, 0\n\t"
    "v_mov_b32 v104, 0\n\tv_mov_b32 v105, 0\n\tv_mov_b32 v106, 0\n\tv_mov_b32 v107, 0\n\t"
    "v_mov_b32 v108, 0\n\tv_mov_b32 v109, 0\n\tv_mov_b32 v110, 0\n\tv_mov_b32 v111, 0\n\t"
    :: "v"(ka0), "v"(ka1), "i"(TO) : "memory");
}
#define QK(s, k, q, c) "v_mfma_f32_16x16x32_bf16 v[" #s ":" #s "+3], v[" #k ":" #k "+3], v[" #q ":" #q "+3], " c "\n\t"
#define TR(d, a, off) "ds_read_b64_tr_b16 v[" #d ":" #d "+1], %[" #a "] offset:%c[to]+" #off "\n\t"
#define PV(r) "v_mfma_f32_16x16x32_bf16 v[" #r ":" #r "+3], v[124:127], v[" #r ":" #r "+3], 0\n\t"
#define SM(r) "v_fma_f32 v" #r ", v" #r ", v99, v121\n\tv_exp_f32 v" #r ", v" #r "\n\t"
#define PVA(acc, a, v, c) "v_mfma_f32_16x16x32_bf16 v[" #acc ":" #acc "+3], v[" #a ":" #a "+3], v[" #v ":" #v "+3], " c "\n\t"
#define PCP(d, s, ctl, bank) "v_mov_b32_dpp v" #d ", v" #s " " ctl " row_mask:0xf bank_mask:" bank "\n\t"
template <int TO>
__device__ __forceinline__ void seg_attn(u32 qa, u32 sta, u32 va0, u32 va1, u32 va2, u32 va3, u32 oa, u32 mla) {
  asm volatile(
    "ds_read_b128 v[80:83], %[qa]\n\t"
    "ds_read_b128 v[84:87], %[qa] offset:64\n\t"
    "ds_read_b128 v[88:91], %[qa] offset:128\n\t"
    "ds_read_b128 v[92:95], %[qa] offset:192\n\t"
    "ds_read_b64 v[120:121], %[sta]\n\t"
    "s_waitcnt lgkmcnt(4)\n\t"
    QK(112, 48, 80, "0") QK(116, 64, 80, "0")
    "s_waitcnt lgkmcnt(3)\n\t"
    QK(112, 52, 84, "v[112:115]") QK(116, 68, 84, "v[116:119]")
    "s_waitcnt lgkmcnt(2)\n\t"
    QK(112, 56, 88, "v[112:115]") QK(116, 72, 88, "v[116:119]")
    "s_waitcnt lgkmcnt(0)\n\t"
    QK(112, 60, 92, "v[112:115]")
    "v_add_f32_dpp v120, v120, v120 quad_perm:[1,0,3,2] row_mask:0xf bank_mask:0xf\n\t"
    "v_add_f32_dpp v121, v121, v121 quad_perm:[1,0,3,2] row_mask:0xf bank_mask:0xf\n\t"
    QK(116, 76, 92, "v[116:119]")
    TR(48, va0, 0)
    "v_add_f32_dpp v120, v120, v120 quad_perm:[2,3,0,1] row_mask:0xf bank_mask:0xf\n\t"
    "v_add_f32_dpp v121, v121, v121 quad_perm:[2,3,0,1] row_mask:0xf bank_mask:0xf\n\t"
    TR(50, va1, 0) TR(52, va2, 0)
    "v_add_f32_dpp v120, v120, v120 row_half_mirror row_mask:0xf bank_mask:0xf\n\t"
    "v_add_f32_dpp v121, v121, v121 row_half_mirror row_mask:0xf bank_mask:0xf\n\t"
    TR(54, va3, 0)
    "v_mul_f32 v122, 0xbb000000, v120\n\t"
    "v_mul_f32 v123, 0x3b000000, v121\n\t"
    "v_fma_f32 v123, -v122, v122, v123\n\t"
    "v_fmac_f32_dpp v112, v112, v122 row_shl:4 row_mask:0xf bank_mask:0xf bound_ctrl:1\n\t"
    "v_max_f32 v123, 0, v123\n\t"
    "v_fmac_f32_dpp v113, v113, v122 row_shl:4 row_mask:0xf bank_mask:0xf bound_ctrl:1\n\t"
    "v_add_f32 v123, 0x3727c5ac, v123\n\t"
    "v_fmac_f32_dpp v114, v114, v122 row_shl:4 row_mask:0xf bank_mask:0xf bound_ctrl:1\n\t"
    "v_rsq_f32 v123, v123\n\t"
    "v_fmac_f32_dpp v115, v115, v122 row_shl:4 row_mask:0xf bank_mask:0xf bound_ctrl:1\n\t"
    "v_fmac_f32_dpp v116, v116, v122 row_shl:4 row_mask:0xf bank_mask:0xf bound_ctrl:1\n\t"
    "v_max3_f32 v120, v112, v113, v114\n\t"
    "v_fmac_f32_dpp v117, v117, v122 row_shl:4 row_mask:0xf bank_mask:0xf bound_ctrl:1\n\t"
    "v_fmac_f32_dpp v118, v118, v122 row_shl:4 row_mask:0xf bank_mask:0xf bound_ctrl:1\n\t"
    "v_fmac_f32_dpp v119, v119, v122 row_shl:4 row_mask:0xf bank_mask:0xf bound_ctrl:1\n\t"
    "v_max3_f32 v120, v120, v115, v116\n\t"
    "v_mul_f32 v123, %[c2], v123\n\t"
    "v_max3_f32 v120, v120, v117, v118\n\t"
    "v_max_f32 v120, v120, v119\n\t"
    "v_mov_b32 v121, v120\n\t"
    TR(56, va0, 512) TR(58, va1, 512)
    "v_permlane16_swap_b32 v120, v121\n\t"
    "v_max_f32 v120, v120, v121\n\t"
    "v_mov_b32 v121, v120\n\t"
    TR(60, va2, 512) TR(62, va3, 512)
    "v_permlane32_swap_b32 v120, v121\n\t"
    "v_max_f32 v120, v120, v121\n\t"
    "v_mul_f32_e64 v121, -v120, v123\n\t"
    "v_mul_f32 v122, v120, v123\n\t"
    "v_fma_f32 v112, v112, v123, v121\n\t"
    "v_fma_f32 v113, v113, v123, v121\n\t"
    "v_exp_f32 v112, v112\n\t"
    TR(64, va0, 1024)
    "v_fma_f32 v114, v114, v123, v121\n\t"
    "v_exp_f32 v113, v113\n\t"
    TR(66, va1, 1024)
    "v_fma_f32 v115, v115, v123, v121\n\t"
    "v_exp_f32 v114, v114\n\t"
    TR(68, va2, 1024)
    "v_fma_f32 v116, v116, v123, v121\n\t"
    "v_exp_f32 v115, v115\n\t"
    TR(70, va3, 1024)
    "v_fma_f32 v117, v117, v123, v121\n\t"
    "v_exp_f32 v116, v116\n\t"
    "v_cvt_pk_bf16_f32 v124, v112, v113\n\t"
    "v_fma_f32 v118, v118, v123, v121\n\t"
    "v_exp_f32 v117, v117\n\t"
    TR(72, va0, 1536)
    "v_fma_f32 v119, v119, v123, v121\n\t"
    "v_exp_f32 v118, v118\n\t"
    "v_cvt_pk_bf16_f32 v125, v114, v115\n\t"
    "v_exp_f32 v119, v119\n\t"
    TR(74, va1, 1536) TR(76, va2, 1536) TR(78, va3, 1536)
    "v_cvt_pk_bf16_f32 v126, v116, v117\n\t"
    "v_cvt_pk_bf16_f32 v127, v118, v119\n\t"
    PCP(96, 124, "quad_perm:[0,1,2,3]", "0x1") PCP(100, 124, "row_shr:4", "0x2") PCP(104, 124, "quad_perm:[0,1,2,3]", "0x4") PCP(108, 124, "row_shr:4", "0x8")
    PCP(97, 125, "quad_perm:[0,1,2,3]", "0x1") PCP(101, 125, "row_shr:4", "0x2") PCP(105, 125, "quad_perm:[0,1,2,3]", "0x4") PCP(109, 125, "row_shr:4", "0x8")
    PCP(98, 126, "quad_perm:[0,1,2,3]", "0x1") PCP(102, 126, "row_shr:4", "0x2") PCP(106, 126, "quad_perm:[0,1,2,3]", "0x4") PCP(110, 126, "row_shr:4", "0x8")
    PCP(99, 127, "quad_perm:[0,1,2,3]", "0x1") PCP(103, 127, "row_shr:4", "0x2") PCP(107, 127, "quad_perm:[0,1,2,3]", "0x4") PCP(111, 127, "row_shr:4", "0x8")
    "v_mov_b32 v84, %[ones]\n\tv_mov_b32 v85, %[ones]\n\tv_mov_b32 v86, %[ones]\n\tv_mov_b32 v87, %[ones]\n\t"
    "s_waitcnt lgkmcnt(0)\n\t"
    PVA(112, 96, 48, "0") PVA(116, 96, 64, "0")
    PVA(112, 100, 52, "v[112:115]") PVA(116, 100, 68, "v[116:119]")
    PVA(112, 104, 56, "v[112:115]") PVA(116, 104, 72, "v[116:119]")
    PVA(112, 108, 60, "v[112:115]") PVA(116, 108, 76, "v[116:119]")
    "v_mfma_f32_16x16x32_bf16 v[80:83], v[84:87], v[124:127], 0\n\t"
    "s_nop 6\n\t"
    "ds_write_b128 %[oa], v[112:115]\n\t"
    "ds_write_b128 %[oa], v[116:119] offset:1024\n\t"
    "s_mov_b64 exec, 0xf\n\t"
    "ds_write2_b32 %[mla], v122, v80 offset0:0 offset1:16\n\t"
    "s_mov_b64 exec, -1\n\t"
    :: [qa] "v"(qa), [sta] "v"(sta), [va0] "v"(va0), [va1] "v"(va1), [va2] "v"(va2), [va3] "v"(va3), [oa] "v"(oa), [mla] "v"(mla),
       [c2] "s"(C2), [ones] "s"(ONES), [to] "i"(TO) : "memory");
}
__device__ __forceinline__ void seg_merge(u32 mla, u32 oa, u32 so, const char* ws) {
  asm volatile(
    "ds_read_b128 v[48:51], %[mla]\n\t"
    "ds_read_b128 v[60:63], %[mla] offset:64\n\t"
    "ds_read2st64_b32 v[56:57], %[oa] offset0:0 offset1:8\n\t"
    "ds_read2st64_b32 v[58:59], %[oa] offset0:16 offset1:24\n\t"
    "s_waitcnt lgkmcnt(3)\n\t"
    "v_max3_f32 v52, v48, v49, v50\n\t"
    "v_max_f32 v52, v52, v51\n\t"
    "v_sub_f32 v48, v48, v52\n\t"
    "v_sub_f32 v49, v49, v52\n\t"
    "v_exp_f32 v48, v48\n\t"
    "v_sub_f32 v50, v50, v52\n\t"
    "v_exp_f32 v49, v49\n\t"
    "v_sub_f32 v51, v51, v52\n\t"
    "v_exp_f32 v50, v50\n\t"
    "v_exp_f32 v51, v51\n\t"
    "s_waitcnt lgkmcnt(0)\n\t"
    "v_pk_mul_f32 v[60:61], v[60:61], v[48:49]\n\t"
    "v_pk_mul_f32 v[56:57], v[56:57], v[48:49]\n\t"
    "v_pk_fma_f32 v[60:61], v[62:63], v[50:51], v[60:61]\n\t"
    "v_pk_fma_f32 v[56:57], v[58:59], v[50:51], v[56:57]\n\t"
    "v_add_f32 v60, v60, v61\n\t"
    "v_add_f32 v56, v56, v57\n\t"
    "v_rcp_f32 v60, v60\n\t"
    "s_nop 0\n\t"
    "v_mul_f32 v52, v56, v60\n\t"
    PACK_STORE
    :: [mla] "v"(mla), [oa] "v"(oa), [so] "v"(so), [ws] "s"(ws), [c8z] "s"(C8Z) : "memory");
}

__device__ __forceinline__ void dma16(u32 voff, const char* sbase, u32 lds_dst) {
  u32 keep;
  asm volatile("s_mov_b32 %0, m0\n\ts_mov_b32 m0, %3\n\ts_nop 0\n\tglobal_load_lds_dwordx4 %1, %2" NTKV "\n\ts_mov_b32 m0, %0"
               : "=&s"(keep) : "v"(voff), "s"(sbase), "s"(lds_dst) : "memory");
}
__device__ __forceinline__ void kv_load(const char* base, int w, int lane, u32 ldsb) {
  asm volatile("" : "+s"(base));
  const int r8 = (lane & 31) >> 2;
#pragma unroll
  for (int i = 0; i < 4; ++i) {
    const u32 ch = 8 * (i & 1) + 4 * (lane >> 5) + ((lane & 3) ^ ((2 * (i >> 1) + (r8 >> 2)) & 3));
    dma16(r8 * 256 + ch * 16, base + (size_t)(16 * w + 8 * (i >> 1)) * 256, ldsb + w * 4096 + i * 1024);
  }
}
__device__ __forceinline__ bool has_sent(v4u x) {
  const u32 m = __builtin_elementwise_max(__builtin_elementwise_max(x[0], x[1]), __builtin_elementwise_max(x[2], x[3]));
  return __builtin_amdgcn_ballot_w64(m == SENT) != 0ull;
}
__device__ __forceinline__ v4u poll128(const char* base, u32 voff) {
  v4u x;
  for (u32 spins = 0;; ++spins) {
    asm volatile("s_nop 4\n\tglobal_load_dwordx4 %0, %1, %2 sc1\n\ts_waitcnt vmcnt(0)" : "=&v"(x) : "v"(voff), "s"(base) : "memory");
    if (!has_sent(x) || spins > SPIN_CAP) break;
  }
  return x;
}
__device__ __forceinline__ v4u pack8(float v) {
  const u32 x = safe(pk(v, dpp<XOR1>(v)));
  return v4u{x, dppu<0x102>(x), dppu<0x104>(x), dppu<0x106>(x)};
}
__device__ __forceinline__ void sum_partials(v4u x, float (&v)[8]) {
#pragma unroll
  for (int m = 0; m < 4; ++m) { v[2 * m] = bflo(x[m]); v[2 * m + 1] = bfhi(x[m]); }
#pragma unroll
  for (int j = 0; j < 8; ++j) v[j] += dpp<XOR1>(v[j]);
#pragma unroll
  for (int j = 0; j < 8; ++j) v[j] += dpp<XOR2>(v[j]);
#pragma unroll
  for (int j = 0; j < 8; ++j) v[j] += dpp<HMIR>(v[j]);
}
__device__ __forceinline__ void x_stats(v4u xa, char* lds, int w, int lane) {
  const bool p = (lane >> 3) & 1; const int kq = lane >> 4;
  const v4f gm = mfma(xa, xa, v4f{0.f, 0.f, 0.f, 0.f});
  const v4f sm = mfma(xa, v4u{ONES, ONES, ONES, ONES}, v4f{0.f, 0.f, 0.f, 0.f});
  float s2 = (gm[0] + gm[1]) + (gm[2] + gm[3]);
  s2 = (p == (bool)(kq >> 1)) ? s2 : 0.f;
  s2 += dpp<XOR1>(s2); s2 += dpp<XOR2>(s2); s2 += dpp<HMIR>(s2); s2 += dpp<ROR8>(s2);
  s2 = x32_add(x16_add(s2));
  float s1 = (sm[0] + sm[1]) + (sm[2] + sm[3]);
  s1 = x32_add(x16_add(s1));
  if (lane == 0) *(v2f*)(lds + L_ST + w * 8) = v2f{s1, s2};
}
}

#define OPAQUE_LANE() int lane = lane0; asm volatile("" : "+v"(lane)); const int c8 = lane & 7, kq = lane >> 4; const bool p = (lane >> 3) & 1; (void)c8; (void)p; (void)kq
#define OPAQUE_S(x) asm volatile("" : "+s"(x))

constexpr int FW = 4;

struct Addr {
  u32 vB, vA, vL, lw, so;
  u32 mr, orr;
  u32 r0, r1, r2, r3, r4, r5, r6, r7, r8;
};
#define PIN(x) asm volatile("" : "+v"(x))

template <int TYPE>
__device__ __forceinline__ void layer(const int rep, char* ws, char* lds, int b, int g, int s, int w, const int lane0, v4u xraw, Addr& A, u32 qaT,
                                      const char* vA0, const char* kB, const char* vB, const char* wqB, const char* cwo, const u64 flag) {
  constexpr int RQ = TYPE ? R_WQB : R_WQA, RO = TYPE ? R_WOB : R_WOA;
  const int L = rep * 2 + TYPE;
  OPAQUE_S(b); OPAQUE_S(g); OPAQUE_S(s); OPAQUE_S(w);
  const u32 grp = (u32)(b * 8 + g), cu = grp * 8 + s;
#define STAGGER() do { if (STG) { const int r_ = (w - 2 * b) & 7; for (int i_ = 0; i_ < r_; ++i_) __builtin_amdgcn_s_sleep(STG); } } while (0)
#define LD_WO(R, ptr) do { OPAQUE_LANE(); const char* p_ = ptr; OPAQUE_S(p_); bulk8<R, 1024>(p_ + (size_t)(512 * g + 64 * w) * 1024 + 128 * s, (u32)(lane >> 3) * 8192 + c8 * 16); } while (0)
#define LD_WQ2(R, ptr, I0) do { OPAQUE_LANE(); const char* p_ = ptr; OPAQUE_S(p_); bulk2<R, 8192, I0>(p_ + (size_t)(64 * w) * 8192 + (512 * g + 64 * s) * 2, (u32)(lane >> 3) * 65536 + c8 * 16); } while (0)
#define LD_KB() do { OPAQUE_LANE(); kv_load(kB, w, lane, L_K + 32768); } while (0)
#define LD_VB() do { OPAQUE_LANE(); kv_load(vB, w, lane, L_V + 32768); } while (0)
  const char* wsL = ws + L * BOXL;
  const char* box = ws + WS_BOX + cu * BOX + L * BOXL;
  {
    u32 sp = 0;
    if (rep == 0) {
      if (TYPE == 0) VMWAIT(4); else VMWAIT(0);
      transpose8<RQ>();
    }
    if (L == 0) {
      const v4u xa = (lane0 & 7) == 0 ? xraw : v4u{0u, 0u, 0u, 0u};
      asm volatile("v_mov_b32 v48, %0\n\tv_mov_b32 v49, %1\n\tv_mov_b32 v50, %2\n\tv_mov_b32 v51, %3" :: "v"(xa[0]), "v"(xa[1]), "v"(xa[2]), "v"(xa[3]));
      seg_p1<RQ, 0, 0>(0, ws, A.lw, sp);
    } else {
      if (DLY_B && rep) __builtin_amdgcn_s_sleep(DLY_B);
      if (rep == 0) {
        poll_issue(A.vB, wsL - BOXL);
        LD_VB();
        VMWAIT(4);
        seg_p1<RQ, 2, 0>(A.vB, wsL - BOXL, A.lw, sp);
      } else {
        seg_p1<RQ, 1, 0>(A.vB, wsL - BOXL, A.lw, sp);
      }
    }
    if (L == 0) { OPAQUE_LANE(); kv_load(vA0, w, lane, L_V); }
    v4u xa;
    asm volatile("v_mov_b32 %0, v48\n\tv_mov_b32 %1, v49\n\tv_mov_b32 %2, v50\n\tv_mov_b32 %3, v51" : "=v"(xa[0]), "=v"(xa[1]), "=v"(xa[2]), "=v"(xa[3]));
    if (rep == 0) {
      asm volatile("v_mov_b32 v48, %0\n\tv_mov_b32 v49, %0\n\tv_mov_b32 v50, %0\n\tv_mov_b32 v51, %0" :: "s"(ONES));
      seg_p1<RQ, 0, L_REDC - L_RED>(0, ws, A.lw, sp);
    }
    BAR();
    if (w == FW) {
      seg_final2<false>(A.r2, 0, A.so, box + BOX_Q);
      OPAQUE_LANE();
      if (rep == 0) {
        const __amdgpu_buffer_rsrc_t rs = __builtin_amdgcn_make_buffer_rsrc(ws, 0, WS_END, 0x00020000);
        const v4f r0 = *(const v4f*)(lds + L_REDC + lane * 32), r1 = *(const v4f*)(lds + L_REDC + lane * 32 + 16);
        const float cs = 0.125f * (((r0[0] + r0[1]) + (r0[2] + r0[3])) + ((r1[0] + r1[1]) + (r1[2] + r1[3])));
        const v4u pc = pack8(cs);
        if (c8 == 0) __builtin_amdgcn_raw_buffer_store_b128(pc, rs, WS_BOX + cu * BOX + BOX_CS + TYPE * 128 + lane * 2, 0, 16);
      }
      x_stats(xa, lds, w, lane);
      if (L == 0) {
        for (u32 spins = 0;; ++spins) {
          const u64 f = __hip_atomic_load((GAS u64*)(ws + WS_RDY + b * 512 + lane * 8), __ATOMIC_RELAXED, __HIP_MEMORY_SCOPE_AGENT);
          if (__all(f == flag) || spins > SPIN_CAP) break;
        }
        if (g == 0 && s == 0 && lane == 0) __hip_atomic_store((GAS u32*)(ws + WS_EPOCH + b * 256), (u32)flag, __ATOMIC_RELAXED, __HIP_MEMORY_SCOPE_AGENT);
      } else {
        if (DLY_C && rep) __builtin_amdgcn_s_sleep(DLY_C);
      }
      u32 sq = 0;
      if (rep == 0) {
        const char* wsC = ws + (BOX_CS + TYPE * 128 - BOX_Q);
        qpoll_issue<52>(A.r0, wsL);
        qpoll_issue<60>(A.r0, wsC);
        VMWAIT(0);
        seg_qpoll<0, 52, 1>(A.r0, wsL, A.r1, sq);
        seg_qpoll<L_CSI + TYPE * 1088 - L_Q, 60, 1>(A.r0, wsC, A.r1, sq);
      } else {
        seg_qpoll<0, 52, 0>(A.r0, wsL, A.r1, sq);
      }
    } else {
      OPAQUE_LANE();
      x_stats(xa, lds, w, lane);
    }
    if (g == 0 && w == s) {
      OPAQUE_LANE();
      float v[8];
      sum_partials(xa, v);
      if (c8 == 0) {
        float* xs = (float*)(lds + L_XS) + 16 * kq + (p ? 8 : 0);
        *(v4f*)xs = v4f{v[0], v[1], v[2], v[3]};
        *(v4f*)(xs + 4) = v4f{v[4], v[5], v[6], v[7]};
      }
    }
    if (rep == 0) {
      VMWAIT(0);
      BAR();
      if (w < 4) LD_WO(RO, cwo);
    }
  }
  if (w < 4) {
    seg_kpre<TYPE * 32768>(A.r0, A.r1);
    BAR();
    seg_attn<TYPE * 32768>(qaT, A.r6, A.r2, A.r3, A.r4, A.r5, A.r7, A.r8);
  } else {
    BAR();
    if (rep == 0) {
      STAGGER();
      LD_WO(RO, cwo);
    }
  }
  {
    BAR();
    seg_merge(A.mr, A.orr, A.so, box + w * 128);
    if (w == 7) {
      OPAQUE_LANE();
      const __amdgpu_buffer_rsrc_t rs = __builtin_amdgcn_make_buffer_rsrc(ws, 0, WS_END, 0x00020000);
      const int hh = (lane >> 2) & 3, aa = lane & 3;
      const float m1 = ((const float*)(lds + L_ML))[hh * 4 + aa], l1 = ((const float*)(lds + L_ML))[16 + hh * 4 + aa];
      float M1 = __builtin_fmaxf(m1, dpp<XOR1>(m1)); M1 = __builtin_fmaxf(M1, dpp<XOR2>(M1));
      float L1 = __builtin_amdgcn_exp2f(m1 - M1) * l1; L1 += dpp<XOR1>(L1); L1 += dpp<XOR2>(L1);
      const u32 e = safe(__float_as_uint(M1 + __builtin_amdgcn_logf(L1)));
      const v4u ev = {dppu<0x150>(e), dppu<0x154>(e), dppu<0x158>(e), dppu<0x15C>(e)};
      if (lane < 8) __builtin_amdgcn_raw_buffer_store_b128(ev, rs, WS_LSR + ((L * 32 + grp) * 8 + lane) * 128 + s * 16, 0, 16);
    }
  }
  {
    const char* wsS = ws + L * 32768;
    u32 sp = 0;
    if (rep == 0) {
      VMWAIT(0);
      transpose8<RO>();
    } else {
      if (DLY_A) __builtin_amdgcn_s_sleep(DLY_A);
    }
    if (rep == 0 && TYPE == 0) {
      seg_p3_issue(A.vA, A.vL, wsL, wsS);
      LD_WQ2(R_WQB, wqB, 0);
      LD_KB();
      VMWAIT(6);
      seg_p3<RO, 1>(A.vA, A.vL, wsL, wsS, A.lw, sp);
    } else {
      seg_p3<RO, 0>(A.vA, A.vL, wsL, wsS, A.lw, sp);
    }
    BAR();
    if (w == FW) seg_final2<true>(A.r2, A.r3, A.so, box + BOX_X);
    if (rep == 0 && TYPE == 0) { LD_WQ2(R_WQB, wqB, 2); LD_WQ2(R_WQB, wqB, 4); LD_WQ2(R_WQB, wqB, 6); }
  }
}

__global__ void __launch_bounds__(512, 2) __attribute__((amdgpu_num_vgpr(24)))
fused_kernel(const char* __restrict__ xin, const char* kA, const char* vA, const char* wqA, const char* woA,
             const char* kB, const char* vB, const char* wqB, const char* woB, unsigned short* out, char* ws) {
  extern __shared__ __attribute__((aligned(16))) char lds[];
  const int id = blockIdx.x, g = id & 7, b = id >> 6, s = (id >> 3) & 7;
  const int tid = threadIdx.x, lane = tid & 63, w = __builtin_amdgcn_readfirstlane(tid >> 6);
  const int c8 = lane & 7, kq = lane >> 4;
  const bool p = (lane >> 3) & 1;
  const __amdgpu_buffer_rsrc_t rs = __builtin_amdgcn_make_buffer_rsrc(ws, 0, WS_END, 0x00020000);
  const u32 cu = (u32)((b * 8 + g) * 8 + s);
  asm volatile("" ::: "v127", "v255");
  u32 ep; v4u xraw;
  asm volatile("s_nop 4\n\tglobal_load_dword v56, %0, %1 sc1\n\tglobal_load_dwordx4 v[52:55], %2, %3"
               :: "v"(WS_EPOCH + b * 256), "s"(ws), "v"((u32)((b * 512 + 64 * w + 16 * kq + (p ? 8 : 0)) * 2)), "s"(xin) : "memory");
  if (w == 5) {
    const v4u sv = {SENT, SENT, SENT, SENT};
#pragma unroll
    for (int i = 0; i < 5; ++i) __builtin_amdgcn_raw_buffer_store_b128(sv, rs, WS_BOX + cu * BOX + i * 1024 + lane * 16, 0, 16);
    const u32 o6 = lane < 16 ? WS_BOX + cu * BOX + 5120 + lane * 16
                             : WS_LSR + (u32)(((((lane - 16) >> 3) * 32 + b * 8 + g) * 8 + (lane & 7)) * 128 + s * 16);
    if (lane < 48) __builtin_amdgcn_raw_buffer_store_b128(sv, rs, o6, 0, 16);
  }
  if (w == 6) ((float*)(lds + L_XS))[lane] = 0.f;
  const size_t kvoff = ((size_t)(b * 8 + g) * SK + (SK - WIN) + s * 128) * 256;
  const size_t qoff = (size_t)(64 * w) * 8192 + (512 * g + 64 * s) * 2;
  if (STG) { const int r_ = (w - 2 * b) & 7; for (int i_ = 0; i_ < r_; ++i_) __builtin_amdgcn_s_sleep(STG); }
  bulk8<R_WQA, 8192>(wqA + qoff, (u32)(lane >> 3) * 65536 + c8 * 16);
  VMWAIT(8);
  asm volatile("v_mov_b32 %0, v52\n\tv_mov_b32 %1, v53\n\tv_mov_b32 %2, v54\n\tv_mov_b32 %3, v55\n\tv_mov_b32 %4, v56"
               : "=v"(xraw[0]), "=v"(xraw[1]), "=v"(xraw[2]), "=v"(xraw[3]), "=v"(ep));
  const u32 tag = (u32)__builtin_amdgcn_readfirstlane((int)ep) + 1u;
  const u64 flag = (u64)tag | ((u64)(~tag) << 32);
  asm volatile("s_barrier" ::: "memory");
  if (w == 5 && lane == 0) __hip_atomic_store((GAS u64*)(ws + WS_RDY + b * 512 + (s * 8 + g) * 8), flag, __ATOMIC_RELAXED, __HIP_MEMORY_SCOPE_AGENT);
  kv_load(kA + kvoff, w, lane, L_K);
  Addr A; u32 qa0, qa1;
  {
    const u32 grp = (u32)(b * 8 + g);
    const u32 oct = kq * 32 + (p ? 16 : 0);
    A.vB = WS_BOX + (u32)((b * 8 + c8) * 8 + w) * BOX + BOX_X + oct;
    A.vA = WS_BOX + (grp * 8 + c8) * BOX + w * 128 + oct;
    A.vL = WS_LSR + (grp * 8 + s) * 128 + c8 * 16 + (w >> 1) * 4;
    A.lw = L_RED + (8 * c8 + (lane >> 3)) * 32 + w * 4;
    A.so = lane * 2;
    A.mr = L_ML + (w >> 1) * 16;
    A.orr = L_OM + (w & 1) * 1024 + lane * 16 + (w >> 1) * 4;
    const int r16 = lane & 15, q4 = r16 >> 2, p2 = lane & 3;
    qa0 = L_Q + (lane & 3) * 272 + kq * 16;
    qa1 = qa0;
    if (lane & 4) { qa0 += L_CSI - L_Q; qa1 += L_CSI + 1088 - L_Q; }
    if (w < 4) {
      const u32 kb = L_K + 2048 * (4 * w + (r16 >> 2)) + 64 * (r16 & 3);
      A.r0 = kb + 16 * (kq ^ ((2 * (r16 >> 2)) & 3));
      A.r1 = kb + 256 + 16 * (kq ^ ((2 * (r16 >> 2) + 1) & 3));
      const u32 vb = L_V + 2048 * (4 * w + kq) + 64 * q4 + 8 * (p2 & 1);
      A.r2 = vb + 16 * (((p2 >> 1)) ^ ((2 * kq) & 3));
      A.r3 = vb + 256 + 16 * (((p2 >> 1)) ^ ((2 * kq + 1) & 3));
      A.r4 = vb + 16 * ((2 + (p2 >> 1)) ^ ((2 * kq) & 3));
      A.r5 = vb + 256 + 16 * ((2 + (p2 >> 1)) ^ ((2 * kq + 1) & 3));
      A.r7 = L_OM + w * 2048 + lane * 16;
      A.r8 = L_ML + (lane * 4 + w) * 4;
    } else {
      A.r0 = WS_BOX + (grp * 8 + (lane >> 3)) * BOX + BOX_Q + c8 * 16;
      A.r1 = L_Q + kq * 272 + (lane & 15) * 16;
      A.r2 = L_RED + lane * 32;
      A.r3 = L_XS + lane * 4;
      A.r4 = A.r5 = A.r7 = A.r8 = 0;
    }
    A.r6 = L_ST + c8 * 8;
    PIN(A.r6);
    PIN(A.vB); PIN(A.vA); PIN(A.vL); PIN(A.lw); PIN(A.so); PIN(A.mr); PIN(A.orr); PIN(A.r0); PIN(A.r1); PIN(A.r2); PIN(A.r3); PIN(A.r4); PIN(A.r5); PIN(A.r7); PIN(A.r8);
    PIN(qa0); PIN(qa1);
  }
  for (int rep = 0; rep < 2; ++rep) {
    layer<0>(rep, ws, lds, b, g, s, w, lane, xraw, A, qa0, vA + kvoff, kB + kvoff, vB + kvoff, wqB, woA, flag);
    layer<1>(rep, ws, lds, b, g, s, w, lane, xraw, A, qa1, nullptr, kB + kvoff, vB + kvoff, wqB, woB, flag);
  }
  if (g == 0 && w == s) {
    const u32 voff = WS_BOX + (u32)((b * 8 + c8) * 8 + w) * BOX + 3u * BOXL + BOX_X + kq * 32 + (p ? 16 : 0);
    if (DLY_B) __builtin_amdgcn_s_sleep(DLY_B);
    const v4u x = poll128(ws, voff);
    float v[8];
    sum_partials(x, v);
    if (c8 == 0) *(v4u*)(out + b * 512 + 64 * w + 16 * kq + (p ? 8 : 0)) = v4u{pk(v[0], v[1]), pk(v[2], v[3]), pk(v[4], v[5]), pk(v[6], v[7])};
  }
}

extern "C" void kernel_launch(void* const* d_in, const int* in_sizes, int n_in, void* d_out, int out_size, void* d_ws, size_t ws_size,
                              hipStream_t stream) {
  (void)hipFuncSetAttribute((const void*)fused_kernel, hipFuncAttributeMaxDynamicSharedMemorySize, L_END);
  fused_kernel<<<256, 512, L_END, stream>>>((const char*)d_in[0], (const char*)d_in[1], (const char*)d_in[2], (const char*)d_in[3], (const char*)d_in[4],
                                            (const char*)d_in[6], (const char*)d_in[7], (const char*)d_in[8], (const char*)d_in[9],
                                            (unsigned short*)d_out, (char*)d_ws);
}
```
